```python
import torch

def _softmax(x, axis=-1):
    return torch.softmax(x, dim=axis)
def _gelu(x):
    return torch.nn.functional.gelu(x, approximate="tanh")
def _silu(x):
    return torch.nn.functional.silu(x)
def _sigmoid(x):
    return torch.sigmoid(x)
def _layernorm(x, axis=-1, eps=1e-5):
    m = x.mean(dim=axis, keepdim=True)
    v = x.var(dim=axis, keepdim=True, unbiased=False)
    return (x - m) / torch.sqrt(v + eps)
def _scatter_add(tgt, idx, src, axis=0):
    return tgt.clone().index_add_(axis, idx, src)
def _segment_sum(src, seg, n):
    out = torch.zeros((n,) + src.shape[1:], dtype=src.dtype)
    return out.index_add_(0, seg, src)
def _pad(x, pw):
    flat = tuple(v for pair in reversed(pw) for v in pair)
    return torch.nn.functional.pad(x, flat)

kernel_name = 'recurrent_scan__gqa_block_cached__mixer_token_mlp__decode'

def setup_inputs():

    _w = (lambda a: (lambda t: t.to(torch.bfloat16) if t.is_floating_point() else t)(torch.as_tensor(a)))
    def _A_setup():
        B, Sq, D = 8, 1, 640
        Hq, Hkv, Sk, Dh = 32, 32, 4096, 128
        assert Hq % Hkv == 0, (Hq, Hkv)
        return {
            "x": _w((lambda *s: torch.randn(*s, dtype=torch.bfloat16) * 0.4)(B, Sq, D)),
            "k_cache": _w((lambda *s: torch.randn(*s, dtype=torch.bfloat16) * 1.6)(B, Hkv, Sk, Dh)),
            "v_cache": _w((lambda *s: torch.randn(*s, dtype=torch.bfloat16) * 1.6)(B, Hkv, Sk, Dh)),
            "wq": _w((lambda *s: torch.randn(*s, dtype=torch.bfloat16) * 1.6)(D, Hq * Dh) * (D ** -0.5)),
            "wo": _w((lambda *s: torch.randn(*s, dtype=torch.bfloat16) * 1.6)(Hq * Dh, D) * ((Hq * Dh) ** -0.5)),
        }
    _A_in = _A_setup()

    def _B_setup():
        B, S, D, F = 8, 1, 640, 1728
        return {
            "x": _w((lambda *s: torch.randn(*s, dtype=torch.bfloat16) * 0.4)(B, S, D)),
            "w1": _w((lambda *s: torch.randn(*s, dtype=torch.bfloat16) * 1.6)(S, F) * (S ** -0.5)),
            "b1": _w((lambda *s: torch.randn(*s, dtype=torch.bfloat16) * 1.6)(F) * 0.02),
            "w2": _w((lambda *s: torch.randn(*s, dtype=torch.bfloat16) * 1.6)(F, S) * (F ** -0.5)),
            "b2": _w((lambda *s: torch.randn(*s, dtype=torch.bfloat16) * 1.6)(S) * 0.02),
        }
    _B_in = _B_setup()

    return {
        "A_x": _A_in["x"],
        "A_k_cache": _A_in["k_cache"],
        "A_v_cache": _A_in["v_cache"],
        "A_wq": _A_in["wq"],
        "A_wo": _A_in["wo"],
        "B_x": _B_in["x"],
        "B_w1": _B_in["w1"],
        "B_b1": _B_in["b1"],
        "B_w2": _B_in["w2"],
        "B_b2": _B_in["b2"],
    }

def reference(A_x, A_k_cache, A_v_cache, A_wq, A_wo, B_x, B_w1, B_b1, B_w2, B_b2):
    def _A_ref(x, k_cache, v_cache, wq, wo):
        B, Sq, D = x.shape
        Hq, Dh = 32, 128
        Hkv = k_cache.shape[1]
        rep = Hq // Hkv
        q = (lambda x, *d: x.permute(*d))((x @ wq).reshape(B, Sq, Hq, Dh), 0, 2, 1, 3)
        k = (lambda x, n, axis: torch.repeat_interleave(x, n, dim=axis))(k_cache, rep, axis=1)
        v = (lambda x, n, axis: torch.repeat_interleave(x, n, dim=axis))(v_cache, rep, axis=1)
        scores = torch.einsum("bhqd,bhkd->bhqk", q, k) * (Dh ** -0.5)
        p = _softmax(scores, axis=-1)
        o = torch.einsum("bhqk,bhkd->bhqd", p, v)
        return (lambda x, *d: x.permute(*d))(o, 0, 2, 1, 3).reshape(B, Sq, Hq * Dh) @ wo

    def _B_ref(x, w1, b1, w2, b2):
        B, S, D = x.shape
        h = _layernorm(x, axis=-1)
        u = torch.einsum("bsd,sf->bdf", h, w1) + b1
        u = _gelu(u)
        y = torch.einsum("bdf,fs->bds", u, w2) + b2
        return (lambda x, *d: x.permute(*d))(y, 0, 2, 1)

    x = A_x
    xn = _layernorm(x, axis=-1)
    s = _A_ref(xn, A_k_cache, A_v_cache, A_wq, A_wo)
    g = _silu(_B_ref(xn, B_w1, B_b1, B_w2, B_b2))
    return x + s * g
```

```cpp
#include <hip/hip_runtime.h>
#include <hip/hip_bf16.h>
#include <stdint.h>

typedef unsigned v4u __attribute__((ext_vector_type(4)));
typedef float f32x4 __attribute__((ext_vector_type(4)));
typedef __bf16 bf16x8 __attribute__((ext_vector_type(8)));
typedef __bf16 bf16x2 __attribute__((ext_vector_type(2)));
typedef unsigned short u16;

constexpr int B_ = 8, D_ = 640, H_ = 32, SK = 4096, DH = 128, F_ = 1728;
#define RING 32
#define KV_AUX 2

__device__ __forceinline__ float rbf(float x) { return (float)(__bf16)x; }
__device__ __forceinline__ unsigned bfbits(float x) { return (unsigned)__builtin_bit_cast(u16, (__bf16)x); }
__device__ __forceinline__ float bflo(unsigned u) { return __builtin_bit_cast(float, u << 16); }
__device__ __forceinline__ float bfhi(unsigned u) { return __builtin_bit_cast(float, u & 0xffff0000u); }

template <int CTRL> __device__ __forceinline__ float dppf(float x) {
  return __builtin_bit_cast(float, __builtin_amdgcn_mov_dpp(__builtin_bit_cast(int, x), CTRL, 0xf, 0xf, true));
}
template <int CTRL> __device__ __forceinline__ unsigned dppu(unsigned x) {
  return (unsigned)__builtin_amdgcn_mov_dpp((int)x, CTRL, 0xf, 0xf, true);
}
constexpr int XOR1 = 0xB1, XOR2 = 0x4E, XOR7 = 0x141, XOR8 = 0x128, NEWBCAST = 0x150;

__device__ __forceinline__ float wave_sum(float x) {
  x += dppf<XOR1>(x); x += dppf<XOR2>(x); x += dppf<XOR7>(x); x += dppf<XOR8>(x);
  auto s = __builtin_amdgcn_permlane16_swap(__float_as_uint(x), __float_as_uint(x), false, false);
  x = __uint_as_float(s[0]) + __uint_as_float(s[1]);
  auto t = __builtin_amdgcn_permlane32_swap(__float_as_uint(x), __float_as_uint(x), false, false);
  return __uint_as_float(t[0]) + __uint_as_float(t[1]);
}
__device__ __forceinline__ float wave_max(float x) {
  x = fmaxf(x, dppf<XOR1>(x)); x = fmaxf(x, dppf<XOR2>(x)); x = fmaxf(x, dppf<XOR7>(x)); x = fmaxf(x, dppf<XOR8>(x));
  auto s = __builtin_amdgcn_permlane16_swap(__float_as_uint(x), __float_as_uint(x), false, false);
  x = fmaxf(__uint_as_float(s[0]), __uint_as_float(s[1]));
  auto t = __builtin_amdgcn_permlane32_swap(__float_as_uint(x), __float_as_uint(x), false, false);
  return fmaxf(__uint_as_float(t[0]), __uint_as_float(t[1]));
}
__device__ __forceinline__ float dot2(unsigned a, unsigned b, float acc) {
  return __builtin_amdgcn_fdot2_f32_bf16(__builtin_bit_cast(bf16x2, a), __builtin_bit_cast(bf16x2, b), acc, false);
}
__device__ __forceinline__ f32x4 mfma16(v4u a, v4u b, f32x4 c) {
  return __builtin_amdgcn_mfma_f32_16x16x32_bf16(__builtin_bit_cast(bf16x8, a), __builtin_bit_cast(bf16x8, b), c, 0, 0, 0);
}
template <int J> __device__ __forceinline__ v4u onehot(unsigned pdup, const v4u& mask) {
  const unsigned pb = dppu<NEWBCAST + J>(pdup);
  v4u a;
  a.x = pb & mask.x; a.y = pb & mask.y; a.z = pb & mask.z; a.w = pb & mask.w;
  return a;
}
__device__ __forceinline__ void lds_barrier() {
  asm volatile("s_waitcnt lgkmcnt(0)" ::: "memory");
  __builtin_amdgcn_s_barrier();
  asm volatile("" ::: "memory");
}

__device__ __forceinline__ void ln_stats(const float (&xv)[10], float& mb, float& sd) {
  float s = 0.f;
#pragma unroll
  for (int i = 0; i < 10; ++i) s += xv[i];
  s = wave_sum(s);
  const float mean = s * (1.0f / D_);
  float q = 0.f;
#pragma unroll
  for (int i = 0; i < 10; ++i) { const float d = xv[i] - mean; q = fmaf(d, d, q); }
  q = wave_sum(q);
  const float var = rbf(q * (1.0f / D_));
  mb = rbf(mean);
  sd = rbf(sqrtf(rbf(var + 1e-5f)));
}
__device__ __forceinline__ float ln_apply(float x, float mb, float sd) { return rbf(rbf(x - mb) / sd); }

template <int I, int N, typename Fn> __device__ __forceinline__ void static_for(Fn&& f) {
  if constexpr (I < N) { f(std::integral_constant<int, I>{}); static_for<I + 1, N>(f); }
}

__global__ void __launch_bounds__(512, 2)
attn_kernel(const u16* __restrict__ x, const u16* __restrict__ kc, const u16* __restrict__ vc,
            const u16* __restrict__ wq, const u16* __restrict__ wo, float* __restrict__ part) {
  __shared__ __attribute__((aligned(16))) float lds[8 * 640 + 64];
  const int bh = blockIdx.x, b = bh >> 5, h = bh & 31;
  const int tid = threadIdx.x, lane = tid & 63, w = __builtin_amdgcn_readfirstlane(tid >> 6);
  const int g = lane >> 4, c = lane & 15;

  const __amdgpu_buffer_rsrc_t rK = __builtin_amdgcn_make_buffer_rsrc((void*)(kc + (size_t)bh * SK * DH), 0, SK * DH * 2, 0x00020000);
  const __amdgpu_buffer_rsrc_t rV = __builtin_amdgcn_make_buffer_rsrc((void*)(vc + (size_t)bh * SK * DH), 0, SK * DH * 2, 0x00020000);
  const __amdgpu_buffer_rsrc_t rQ = __builtin_amdgcn_make_buffer_rsrc((void*)(wq + h * DH), 0, D_ * H_ * DH * 2 - h * DH * 2, 0x00020000);
  const __amdgpu_buffer_rsrc_t rO = __builtin_amdgcn_make_buffer_rsrc((void*)(wo + (size_t)h * DH * D_), 0, DH * D_ * 2, 0x00020000);

  const unsigned* x32 = (const unsigned*)(x + b * D_);
  unsigned xr[5];
#pragma unroll
  for (int i = 0; i < 5; ++i) xr[i] = x32[lane + 64 * i];
  const unsigned xo0 = x[b * D_ + 80 * w + 4 * c + g];
  const unsigned xo1 = x[b * D_ + 80 * w + 64 + 4 * (c & 3) + g];
  __builtin_amdgcn_sched_barrier(0);

  v4u wqr[20];
  const int qoff = (80 * w + g) * (H_ * DH * 2) + c * 16;
#pragma unroll
  for (int t = 0; t < 20; ++t) { wqr[t] = __builtin_amdgcn_raw_buffer_load_b128(rQ, qoff, t * 4 * (H_ * DH * 2), 0); __builtin_amdgcn_sched_barrier(0); }
  v4u ring[RING];
  const int koff = w * 1024 + lane * 16;
#pragma unroll
  for (int i = 0; i < RING; ++i) { ring[i] = __builtin_amdgcn_raw_buffer_load_b128(rK, koff, i * 8192, KV_AUX); __builtin_amdgcn_sched_barrier(0); }
  __builtin_amdgcn_sched_barrier(0);

  v4u mask;
  {
    const unsigned m = (c & 1) ? 0xffff0000u : 0x0000ffffu;
    mask.x = (c >> 1) == 0 ? m : 0u; mask.y = (c >> 1) == 1 ? m : 0u; mask.z = (c >> 1) == 2 ? m : 0u; mask.w = (c >> 1) == 3 ? m : 0u;
  }
  float xv[10];
#pragma unroll
  for (int i = 0; i < 5; ++i) { xv[2 * i] = bflo(xr[i]); xv[2 * i + 1] = bfhi(xr[i]); }
  float mb, sd;
  ln_stats(xv, mb, sd);
  unsigned xn0 = bfbits(ln_apply(bflo(xo0), mb, sd)), xn1 = bfbits(ln_apply(bflo(xo1), mb, sd));
  xn0 |= xn0 << 16; xn1 |= xn1 << 16;

  {
    f32x4 acc = {0.f, 0.f, 0.f, 0.f};
    static_for<0, 20>([&](auto T) {
      constexpr int t = T;
      const v4u a = onehot<(t & 15)>(t < 16 ? xn0 : xn1, mask);
      acc = mfma16(a, wqr[t], acc);
    });
    if (lane < 32) *(f32x4*)&lds[w * 128 + 8 * c + 4 * g] = acc;
  }
  lds_barrier();
  v4u qp;
  {
    f32x4 s0 = {0.f, 0.f, 0.f, 0.f}, s1 = {0.f, 0.f, 0.f, 0.f};
#pragma unroll
    for (int ww = 0; ww < 8; ++ww) {
      s0 += *(const f32x4*)&lds[ww * 128 + 8 * c];
      s1 += *(const f32x4*)&lds[ww * 128 + 8 * c + 4];
    }
    qp.x = bfbits(s0.x) | (bfbits(s0.y) << 16); qp.y = bfbits(s0.z) | (bfbits(s0.w) << 16);
    qp.z = bfbits(s1.x) | (bfbits(s1.y) << 16); qp.w = bfbits(s1.z) | (bfbits(s1.w) << 16);
  }

  constexpr int NIT = 128 / RING, GPI = RING / 16;
  float sc[8];
#pragma unroll
  for (int i = 0; i < 8; ++i) sc[i] = 0.f;
#pragma unroll 1
  for (int it = 0; it < NIT; ++it) {
    const bool lastk = (it == NIT - 1);
    const __amdgpu_buffer_rsrc_t rN = lastk ? rV : rK;
    const int sbase = lastk ? 0 : (it + 1) * RING * 8192;
    float nsc[GPI];
#pragma unroll
    for (int gi = 0; gi < GPI; ++gi) {
      float xx[16];
#pragma unroll
      for (int j = 0; j < 16; ++j) {
        const int i = gi * 16 + j;
        float d = dot2(ring[i].x, qp.x, 0.f);
        d = dot2(ring[i].y, qp.y, d); d = dot2(ring[i].z, qp.z, d); d = dot2(ring[i].w, qp.w, d);
        asm volatile("" : "+v"(d));
        xx[j] = d;
        ring[i] = __builtin_amdgcn_raw_buffer_load_b128(rN, koff, sbase + i * 8192, KV_AUX);
        __builtin_amdgcn_sched_barrier(0);
      }
#define TR_STEP(HALF, CTRL, BIT) _Pragma("unroll") for (int hh = 0; hh < (HALF); ++hh) { \
        const float lo_ = xx[hh], hi_ = xx[hh + (HALF)]; const float t1_ = lo_ + dppf<CTRL>(lo_), t2_ = hi_ + dppf<CTRL>(hi_); \
        xx[hh] = (c & (BIT)) ? t2_ : t1_; }
      TR_STEP(8, XOR8, 8) TR_STEP(4, XOR7, 4) TR_STEP(2, XOR2, 2) TR_STEP(1, XOR1, 1)
#undef TR_STEP
      nsc[gi] = rbf(rbf(xx[0]) * 0.08838834764831845f);
    }
#pragma unroll
    for (int i = 0; i < 8 - GPI; ++i) sc[i] = sc[i + GPI];
#pragma unroll
    for (int gi = 0; gi < GPI; ++gi) sc[8 - GPI + gi] = nsc[gi];
  }

  float* sm = &lds[8 * 640];
  constexpr float L2E = 1.4426950408889634f;
  {
    float m = sc[0];
#pragma unroll
    for (int i = 1; i < 8; ++i) m = fmaxf(m, sc[i]);
    m = wave_max(m);
    float l = 0.f;
#pragma unroll
    for (int i = 0; i < 8; ++i) l += __builtin_amdgcn_exp2f((sc[i] - m) * L2E);
    l = wave_sum(l);
    if (lane == 0) { sm[2 * w] = m; sm[2 * w + 1] = l; }
  }
  lds_barrier();
  unsigned pd[8];
  {
    float mw[8], lw[8];
#pragma unroll
    for (int i = 0; i < 4; ++i) { const f32x4 t = *(const f32x4*)&sm[4 * i]; mw[2 * i] = t.x; lw[2 * i] = t.y; mw[2 * i + 1] = t.z; lw[2 * i + 1] = t.w; }
    float m = mw[0];
#pragma unroll
    for (int i = 1; i < 8; ++i) m = fmaxf(m, mw[i]);
    float l = 0.f;
#pragma unroll
    for (int i = 0; i < 8; ++i) l = fmaf(lw[i], __builtin_amdgcn_exp2f((mw[i] - m) * L2E), l);
    const float inv = 1.0f / l;
#pragma unroll
    for (int i = 0; i < 8; ++i) { const unsigned p = bfbits(__builtin_amdgcn_exp2f((sc[i] - m) * L2E) * inv); pd[i] = p | (p << 16); }
  }

  f32x4 oacc = {0.f, 0.f, 0.f, 0.f};
  const int ooff = (16 * w + g) * (D_ * 2) + c * 16;
#pragma unroll 1
  for (int it = 0; it < NIT - 1; ++it) {
    const int sbase = (it + 1) * RING * 8192;
    static_for<0, RING>([&](auto I) {
      constexpr int i = I;
      const v4u a = onehot<(i & 15)>(pd[i >> 4], mask);
      oacc = mfma16(a, ring[i], oacc);
      asm volatile("" : "+v"(oacc));
      ring[i] = __builtin_amdgcn_raw_buffer_load_b128(rV, koff, sbase + i * 8192, KV_AUX);
      __builtin_amdgcn_sched_barrier(0);
    });
#pragma unroll
    for (int i = 0; i < 8 - GPI; ++i) pd[i] = pd[i + GPI];
  }
  v4u wor[20];
  static_for<0, RING>([&](auto I) {
    constexpr int i = I;
    const v4u a = onehot<(i & 15)>(pd[i >> 4], mask);
    oacc = mfma16(a, ring[i], oacc);
    asm volatile("" : "+v"(oacc));
    if constexpr (i < 20) wor[i] = __builtin_amdgcn_raw_buffer_load_b128(rO, ooff, (i / 5) * 4 * (D_ * 2) + (i % 5) * 256, 0);
    __builtin_amdgcn_sched_barrier(0);
  });
  if constexpr (RING < 20) {
    static_for<RING, 20>([&](auto I) { constexpr int i = I; wor[i] = __builtin_amdgcn_raw_buffer_load_b128(rO, ooff, (i / 5) * 4 * (D_ * 2) + (i % 5) * 256, 0); });
  }
  if (lane < 32) *(f32x4*)&lds[w * 128 + 8 * c + 4 * g] = oacc;
  lds_barrier();
  unsigned od;
  {
    float s = 0.f;
#pragma unroll
    for (int ww = 0; ww < 8; ++ww) s += lds[ww * 128 + 16 * w + 4 * (c & 3) + g];
    od = bfbits(s); od |= od << 16;
  }
  lds_barrier();
  f32x4 sacc[5];
#pragma unroll
  for (int u = 0; u < 5; ++u) sacc[u] = f32x4{0.f, 0.f, 0.f, 0.f};
  static_for<0, 4>([&](auto T) {
    constexpr int t = T;
    const v4u a = onehot<t>(od, mask);
#pragma unroll
    for (int u = 0; u < 5; ++u) sacc[u] = mfma16(a, wor[t * 5 + u], sacc[u]);
  });
  if (lane < 32) {
#pragma unroll
    for (int u = 0; u < 5; ++u) *(f32x4*)&lds[w * 640 + u * 128 + 8 * c + 4 * g] = sacc[u];
  }
  lds_barrier();
  if (tid < 160) {
    f32x4 s = {0.f, 0.f, 0.f, 0.f};
#pragma unroll
    for (int ww = 0; ww < 8; ++ww) s += *(const f32x4*)&lds[ww * 640 + 4 * tid];
    *(f32x4*)&part[(size_t)bh * D_ + 4 * tid] = s;
  }
}

__device__ __forceinline__ float gelu_bf(float u) {
  constexpr float K1 = -2.0f * 0.7978845608028654f * 1.4426950408889634f;
  constexpr float K2 = K1 * 0.044715f;
  const float e = __builtin_amdgcn_exp2f(u * fmaf(K2, u * u, K1));
  return rbf(u * __builtin_amdgcn_rcpf(1.0f + e));
}

__global__ void __launch_bounds__(256)
final_kernel(const u16* __restrict__ x, const u16* __restrict__ w1, const u16* __restrict__ b1, const u16* __restrict__ w2,
             const u16* __restrict__ b2, const float* __restrict__ part, u16* __restrict__ out) {
  __shared__ float red[4][20];
  __shared__ float hs[20];
  const int bh = blockIdx.x, b = bh >> 5, h = bh & 31;
  const int tid = threadIdx.x, lane = tid & 63, w = tid >> 6;
  const unsigned* x32 = (const unsigned*)(x + b * D_);
  float xv[10];
#pragma unroll
  for (int i = 0; i < 5; ++i) { const unsigned t = x32[lane + 64 * i]; xv[2 * i] = bflo(t); xv[2 * i + 1] = bfhi(t); }
  float mb, sd, mb2, sd2;
  ln_stats(xv, mb, sd);
#pragma unroll
  for (int i = 0; i < 10; ++i) xv[i] = ln_apply(xv[i], mb, sd);
  ln_stats(xv, mb2, sd2);
  if (tid < 20) hs[tid] = ln_apply(ln_apply(bflo(x[b * D_ + 20 * h + tid]), mb, sd), mb2, sd2);
  __syncthreads();
  float w1v[7], b1v[7], w2v[7];
#pragma unroll
  for (int i = 0; i < 7; ++i) {
    const int f = tid + 256 * i;
    const bool ok = f < F_;
    w1v[i] = ok ? bflo(w1[f]) : 0.f; b1v[i] = ok ? bflo(b1[f]) : 0.f; w2v[i] = ok ? bflo(w2[f]) : 0.f;
  }
  for (int j = 0; j < 20; ++j) {
    const float hv = hs[j];
    float y = 0.f;
#pragma unroll
    for (int i = 0; i < 7; ++i) y = fmaf(gelu_bf(rbf(rbf(hv * w1v[i]) + b1v[i])), w2v[i], y);
    y = wave_sum(y);
    if (lane == 0) red[w][j] = y;
  }
  __syncthreads();
  if (tid < 20) {
    const int col = 20 * h + tid;
    float y = (red[0][tid] + red[1][tid]) + (red[2][tid] + red[3][tid]);
    y = rbf(rbf(y) + bflo(b2[0]));
    const float gt = rbf(y / (1.0f + __expf(-y)));
    float s = 0.f;
    for (int hh = 0; hh < 32; ++hh) s += part[(size_t)(b * 32 + hh) * D_ + col];
    s = rbf(s);
    out[b * D_ + col] = (u16)bfbits(bflo(x[b * D_ + col]) + rbf(s * gt));
  }
}

extern "C" void kernel_launch(void* const* d_in, const int* in_sizes, int n_in, void* d_out, int out_size, void* d_ws,
                              size_t ws_size, hipStream_t stream) {
  const u16* x = (const u16*)d_in[0];
  const u16* kc = (const u16*)d_in[1];
  const u16* vc = (const u16*)d_in[2];
  const u16* wq = (const u16*)d_in[3];
  const u16* wo = (const u16*)d_in[4];
  const u16* w1 = (const u16*)d_in[6];
  const u16* b1 = (const u16*)d_in[7];
  const u16* w2 = (const u16*)d_in[8];
  const u16* b2 = (const u16*)d_in[9];
  float* part = (float*)d_ws;
  attn_kernel<<<256, 512, 0, stream>>>(x, kc, vc, wq, wo, part);
  final_kernel<<<256, 256, 0, stream>>>(x, w1, b1, w2, b2, part, (u16*)d_out);
}
```

```cpp
#include <hip/hip_runtime.h>
#include <hip/hip_bf16.h>
#include <stdint.h>

typedef unsigned v4u __attribute__((ext_vector_type(4)));
typedef float f32x4 __attribute__((ext_vector_type(4)));
typedef __bf16 bf16x8 __attribute__((ext_vector_type(8)));
typedef __bf16 bf16x2 __attribute__((ext_vector_type(2)));
typedef unsigned short u16;

constexpr int B_ = 8, D_ = 640, H_ = 32, SK = 4096, DH = 128, F_ = 1728;
#define RING 16
#define K_AUX 2
#define V_AUX 2
#define W_AUX 2
#define HPRIO 3

__device__ __forceinline__ float rbf(float x) { return (float)(__bf16)x; }
__device__ __forceinline__ unsigned bfbits(float x) { return (unsigned)__builtin_bit_cast(u16, (__bf16)x); }
__device__ __forceinline__ float bflo(unsigned u) { return __builtin_bit_cast(float, u << 16); }
__device__ __forceinline__ float bfhi(unsigned u) { return __builtin_bit_cast(float, u & 0xffff0000u); }

template <int CTRL> __device__ __forceinline__ float dppf(float x) {
  return __builtin_bit_cast(float, __builtin_amdgcn_mov_dpp(__builtin_bit_cast(int, x), CTRL, 0xf, 0xf, true));
}
template <int CTRL> __device__ __forceinline__ unsigned dppu(unsigned x) {
  return (unsigned)__builtin_amdgcn_mov_dpp((int)x, CTRL, 0xf, 0xf, true);
}
constexpr int XOR1 = 0xB1, XOR2 = 0x4E, XOR7 = 0x141, XOR8 = 0x128, NEWBCAST = 0x150;

__device__ __forceinline__ float wave_sum(float x) {
  x += dppf<XOR1>(x); x += dppf<XOR2>(x); x += dppf<XOR7>(x); x += dppf<XOR8>(x);
  auto s = __builtin_amdgcn_permlane16_swap(__float_as_uint(x), __float_as_uint(x), false, false);
  x = __uint_as_float(s[0]) + __uint_as_float(s[1]);
  auto t = __builtin_amdgcn_permlane32_swap(__float_as_uint(x), __float_as_uint(x), false, false);
  return __uint_as_float(t[0]) + __uint_as_float(t[1]);
}
__device__ __forceinline__ float wave_max(float x) {
  x = fmaxf(x, dppf<XOR1>(x)); x = fmaxf(x, dppf<XOR2>(x)); x = fmaxf(x, dppf<XOR7>(x)); x = fmaxf(x, dppf<XOR8>(x));
  auto s = __builtin_amdgcn_permlane16_swap(__float_as_uint(x), __float_as_uint(x), false, false);
  x = fmaxf(__uint_as_float(s[0]), __uint_as_float(s[1]));
  auto t = __builtin_amdgcn_permlane32_swap(__float_as_uint(x), __float_as_uint(x), false, false);
  return fmaxf(__uint_as_float(t[0]), __uint_as_float(t[1]));
}
__device__ __forceinline__ float dot2(unsigned a, unsigned b, float acc) {
  return __builtin_amdgcn_fdot2_f32_bf16(__builtin_bit_cast(bf16x2, a), __builtin_bit_cast(bf16x2, b), acc, false);
}
__device__ __forceinline__ f32x4 mfma16(v4u a, v4u b, f32x4 c) {
  return __builtin_amdgcn_mfma_f32_16x16x32_bf16(__builtin_bit_cast(bf16x8, a), __builtin_bit_cast(bf16x8, b), c, 0, 0, 0);
}
template <int J> __device__ __forceinline__ v4u onehot(unsigned pdup, const v4u& mask) {
  const unsigned pb = dppu<NEWBCAST + J>(pdup);
  v4u a;
  a.x = pb & mask.x; a.y = pb & mask.y; a.z = pb & mask.z; a.w = pb & mask.w;
  return a;
}
__device__ __forceinline__ void lds_barrier() {
  asm volatile("s_waitcnt lgkmcnt(0)" ::: "memory");
  __builtin_amdgcn_s_barrier();
  asm volatile("" ::: "memory");
}
__device__ __forceinline__ void ln_stats(const float (&xv)[10], float& mb, float& sd) {
  float s = 0.f;
#pragma unroll
  for (int i = 0; i < 10; ++i) s += xv[i];
  s = wave_sum(s);
  const float mean = s * (1.0f / D_);
  float q = 0.f;
#pragma unroll
  for (int i = 0; i < 10; ++i) { const float d = xv[i] - mean; q = fmaf(d, d, q); }
  q = wave_sum(q);
  const float var = rbf(q * (1.0f / D_));
  mb = rbf(mean);
  sd = rbf(sqrtf(rbf(var + 1e-5f)));
}
__device__ __forceinline__ float ln_apply(float x, float mb, float sd) { return rbf(rbf(x - mb) / sd); }

template <int I, int N, typename Fn> __device__ __forceinline__ void static_for(Fn&& f) {
  if constexpr (I < N) { f(std::integral_constant<int, I>{}); static_for<I + 1, N>(f); }
}
__device__ __forceinline__ float gelu_bf(float u) {
  constexpr float K1 = -2.0f * 0.7978845608028654f * 1.4426950408889634f;
  constexpr float K2 = K1 * 0.044715f;
  const float e = __builtin_amdgcn_exp2f(u * fmaf(K2, u * u, K1));
  return rbf(u * __builtin_amdgcn_rcpf(1.0f + e));
}

#define ST(i) do {} while (0)

constexpr int L_SM = 2560, L_GATE = 2576, L_OP = 2688, L_END = L_OP + 512;

__global__ void __launch_bounds__(512, 2)
attn_kernel(const u16* __restrict__ x, const u16* __restrict__ kc, const u16* __restrict__ vc,
            const u16* __restrict__ wq, const u16* __restrict__ wo, const u16* __restrict__ w1, const u16* __restrict__ b1,
            const u16* __restrict__ w2, const u16* __restrict__ b2, float* __restrict__ part, float* __restrict__ gate) {
  __shared__ __attribute__((aligned(16))) float lds[L_END];
  const int bh = blockIdx.x, b = bh >> 5, h = bh & 31;
  const int tid = threadIdx.x, lane = tid & 63, w = __builtin_amdgcn_readfirstlane(tid >> 6);
  const int g = lane >> 4, c = lane & 15;
  ST(0);
  v4u mask;
  {
    const unsigned m = (c & 1) ? 0xffff0000u : 0x0000ffffu;
    mask.x = (c >> 1) == 0 ? m : 0u; mask.y = (c >> 1) == 1 ? m : 0u; mask.z = (c >> 1) == 2 ? m : 0u; mask.w = (c >> 1) == 3 ? m : 0u;
  }
  constexpr float L2E = 1.4426950408889634f;

  if (w < 4) {
    const __amdgpu_buffer_rsrc_t rK = __builtin_amdgcn_make_buffer_rsrc((void*)(kc + (size_t)bh * SK * DH), 0, SK * DH * 2, 0x00020000);
    const __amdgpu_buffer_rsrc_t rV = __builtin_amdgcn_make_buffer_rsrc((void*)(vc + (size_t)bh * SK * DH), 0, SK * DH * 2, 0x00020000);
    v4u ring[RING];
    const int koff = w * 1024 + lane * 16;
#pragma unroll
    for (int i = 0; i < RING; ++i) { ring[i] = __builtin_amdgcn_raw_buffer_load_b128(rK, koff, i * 4096, K_AUX); __builtin_amdgcn_sched_barrier(0); }
    ST(1);
    lds_barrier();
    ST(2);
    v4u qp;
    {
      f32x4 s0 = *(const f32x4*)&lds[8 * c], s1 = *(const f32x4*)&lds[8 * c + 4];
#pragma unroll
      for (int ww = 1; ww < 4; ++ww) {
        s0 += *(const f32x4*)&lds[ww * 128 + 8 * c];
        s1 += *(const f32x4*)&lds[ww * 128 + 8 * c + 4];
      }
      qp.x = bfbits(s0.x) | (bfbits(s0.y) << 16); qp.y = bfbits(s0.z) | (bfbits(s0.w) << 16);
      qp.z = bfbits(s1.x) | (bfbits(s1.y) << 16); qp.w = bfbits(s1.z) | (bfbits(s1.w) << 16);
    }
    constexpr int NIT = 256 / RING, GPI = RING / 16;
    float sc[16];
#pragma unroll
    for (int i = 0; i < 16; ++i) sc[i] = 0.f;
    auto kbody = [&](auto LAST, int sbase) {
      constexpr bool last = LAST;
      float nsc[GPI];
#pragma unroll
      for (int gi = 0; gi < GPI; ++gi) {
        float xx[16];
#pragma unroll
        for (int j = 0; j < 16; ++j) {
          const int i = gi * 16 + j;
          float d = dot2(ring[i].x, qp.x, 0.f);
          d = dot2(ring[i].y, qp.y, d); d = dot2(ring[i].z, qp.z, d); d = dot2(ring[i].w, qp.w, d);
          asm volatile("" : "+v"(d));
          xx[j] = d;
          if constexpr (last) ring[i] = __builtin_amdgcn_raw_buffer_load_b128(rV, koff, i * 4096, V_AUX);
          else ring[i] = __builtin_amdgcn_raw_buffer_load_b128(rK, koff, sbase + i * 4096, K_AUX);
          __builtin_amdgcn_sched_barrier(0);
        }
#define TR_STEP(HALF, CTRL, BIT) _Pragma("unroll") for (int hh = 0; hh < (HALF); ++hh) { \
          const float lo_ = xx[hh], hi_ = xx[hh + (HALF)]; const float t1_ = lo_ + dppf<CTRL>(lo_), t2_ = hi_ + dppf<CTRL>(hi_); \
          xx[hh] = (c & (BIT)) ? t2_ : t1_; }
        TR_STEP(8, XOR8, 8) TR_STEP(4, XOR7, 4) TR_STEP(2, XOR2, 2) TR_STEP(1, XOR1, 1)
#undef TR_STEP
        nsc[gi] = rbf(rbf(xx[0]) * 0.08838834764831845f);
        asm volatile("" : "+v"(nsc[gi]));
      }
#pragma unroll
      for (int i = 0; i < 16 - GPI; ++i) sc[i] = sc[i + GPI];
#pragma unroll
      for (int gi = 0; gi < GPI; ++gi) sc[16 - GPI + gi] = nsc[gi];
    };
#pragma unroll 1
    for (int it = 0; it < NIT - 1; ++it) kbody(std::false_type{}, (it + 1) * RING * 4096);
    kbody(std::true_type{}, 0);
    ST(3);
    float* sm = &lds[L_SM];
    {
      float m = sc[0];
#pragma unroll
      for (int i = 1; i < 16; ++i) m = fmaxf(m, sc[i]);
      m = wave_max(m);
      float l = 0.f;
#pragma unroll
      for (int i = 0; i < 16; ++i) l += __builtin_amdgcn_exp2f((sc[i] - m) * L2E);
      l = wave_sum(l);
      if (lane == 0) { sm[2 * w] = m; sm[2 * w + 1] = l; }
    }
    lds_barrier();
    unsigned pd[16];
    {
      const f32x4 t0 = *(const f32x4*)&sm[0], t1 = *(const f32x4*)&sm[4];
      const float m = fmaxf(fmaxf(t0.x, t0.z), fmaxf(t1.x, t1.z));
      float l = t0.y * __builtin_amdgcn_exp2f((t0.x - m) * L2E);
      l = fmaf(t0.w, __builtin_amdgcn_exp2f((t0.z - m) * L2E), l);
      l = fmaf(t1.y, __builtin_amdgcn_exp2f((t1.x - m) * L2E), l);
      l = fmaf(t1.w, __builtin_amdgcn_exp2f((t1.z - m) * L2E), l);
      const float inv = 1.0f / l;
#pragma unroll
      for (int i = 0; i < 16; ++i) { const unsigned p = bfbits(__builtin_amdgcn_exp2f((sc[i] - m) * L2E) * inv); pd[i] = p | (p << 16); }
    }
    ST(4);
    f32x4 oacc = {0.f, 0.f, 0.f, 0.f};
#pragma unroll 1
    for (int it = 0; it < NIT - 1; ++it) {
      const int sbase = (it + 1) * RING * 4096;
      static_for<0, RING>([&](auto I) {
        constexpr int i = I;
        const v4u a = onehot<(i & 15)>(pd[i >> 4], mask);
        oacc = mfma16(a, ring[i], oacc);
        asm volatile("" : "+v"(oacc));
        ring[i] = __builtin_amdgcn_raw_buffer_load_b128(rV, koff, sbase + i * 4096, V_AUX);
        __builtin_amdgcn_sched_barrier(0);
      });
#pragma unroll
      for (int i = 0; i < 16 - GPI; ++i) pd[i] = pd[i + GPI];
    }
    static_for<0, RING>([&](auto I) {
      constexpr int i = I;
      const v4u a = onehot<(i & 15)>(pd[i >> 4], mask);
      oacc = mfma16(a, ring[i], oacc);
      asm volatile("" : "+v"(oacc));
      __builtin_amdgcn_sched_barrier(0);
    });
    ST(5);
    if (lane < 32) *(f32x4*)&lds[L_OP + w * 128 + 8 * c + 4 * g] = oacc;
    lds_barrier();
    ST(6);
    return;
  }

  __builtin_amdgcn_s_setprio(HPRIO);
  const int hw = w - 4, ht = tid - 256;
  const __amdgpu_buffer_rsrc_t rQ = __builtin_amdgcn_make_buffer_rsrc((void*)(wq + h * DH), 0, D_ * H_ * DH * 2 - h * DH * 2, 0x00020000);
  const __amdgpu_buffer_rsrc_t rO = __builtin_amdgcn_make_buffer_rsrc((void*)(wo + (size_t)h * DH * D_), 0, DH * D_ * 2, 0x00020000);
  const unsigned* x32 = (const unsigned*)(x + b * D_);
  unsigned xr[5];
#pragma unroll
  for (int i = 0; i < 5; ++i) xr[i] = __builtin_nontemporal_load(&x32[lane + 64 * i]);
  const u16* xb = x + b * D_;
  const unsigned xo0 = __builtin_nontemporal_load(&xb[160 * hw + 4 * c + g]);
  const unsigned xo1 = __builtin_nontemporal_load(&xb[160 * hw + 64 + 4 * c + g]);
  const unsigned xo2 = __builtin_nontemporal_load(&xb[160 * hw + 128 + 4 * (c & 7) + g]);
  const unsigned xg = __builtin_nontemporal_load(&xb[20 * h + (lane < 20 ? lane : 0)]);
  __builtin_amdgcn_sched_barrier(0);
  v4u wr[40];
  const int qoff = (160 * hw + g) * (H_ * DH * 2) + c * 16;
#pragma unroll
  for (int t = 0; t < 40; ++t) { wr[t] = __builtin_amdgcn_raw_buffer_load_b128(rQ, qoff, t * 4 * (H_ * DH * 2), W_AUX); __builtin_amdgcn_sched_barrier(0); }
  ST(1);
  float xv[10];
#pragma unroll
  for (int i = 0; i < 5; ++i) { xv[2 * i] = bflo(xr[i]); xv[2 * i + 1] = bfhi(xr[i]); }
  float mb, sd;
  ln_stats(xv, mb, sd);
  unsigned xn0 = bfbits(ln_apply(bflo(xo0), mb, sd)), xn1 = bfbits(ln_apply(bflo(xo1), mb, sd)), xn2 = bfbits(ln_apply(bflo(xo2), mb, sd));
  xn0 |= xn0 << 16; xn1 |= xn1 << 16; xn2 |= xn2 << 16;
  {
    f32x4 acc = {0.f, 0.f, 0.f, 0.f};
    static_for<0, 40>([&](auto T) {
      constexpr int t = T;
      const v4u a = onehot<(t & 15)>(t < 16 ? xn0 : t < 32 ? xn1 : xn2, mask);
      acc = mfma16(a, wr[t], acc);
    });
    if (lane < 32) *(f32x4*)&lds[hw * 128 + 8 * c + 4 * g] = acc;
  }
  ST(2);
  lds_barrier();
  __builtin_amdgcn_s_setprio(0);
  {
    float w1v[7], b1v[7], w2v[7];
#pragma unroll
    for (int i = 0; i < 7; ++i) {
      const int f = ht + 256 * i;
      const bool ok = f < F_;
      const int ff = ok ? f : 0;
      const unsigned a1 = __builtin_nontemporal_load(&w1[ff]), a2 = __builtin_nontemporal_load(&b1[ff]), a3 = __builtin_nontemporal_load(&w2[ff]);
      w1v[i] = ok ? bflo(a1) : 0.f; b1v[i] = ok ? bflo(a2) : 0.f; w2v[i] = ok ? bflo(a3) : 0.f;
    }
#pragma unroll
    for (int i = 0; i < 10; ++i) xv[i] = ln_apply(xv[i], mb, sd);
    float mb2, sd2;
    ln_stats(xv, mb2, sd2);
    const float hcol = ln_apply(ln_apply(bflo(xg), mb, sd), mb2, sd2);
    float ycol = 0.f;
#pragma unroll 1
    for (int j = 0; j < 20; ++j) {
      const float hv = __builtin_bit_cast(float, __builtin_amdgcn_readlane(__builtin_bit_cast(int, hcol), j));
      float y = 0.f;
#pragma unroll
      for (int i = 0; i < 7; ++i) y = fmaf(gelu_bf(rbf(rbf(hv * w1v[i]) + b1v[i])), w2v[i], y);
      y = wave_sum(y);
      ycol = (lane == j) ? y : ycol;
    }
    if (lane < 20) lds[L_GATE + hw * 20 + lane] = ycol;
  }
  ST(3);
  const int ooff = (32 * hw + g) * (D_ * 2) + c * 16;
  static_for<0, 40>([&](auto I) {
    constexpr int i = I;
    wr[i] = __builtin_amdgcn_raw_buffer_load_b128(rO, ooff, (i / 5) * 4 * (D_ * 2) + (i % 5) * 256, W_AUX);
    if constexpr (i >= 1) asm volatile("s_waitcnt vmcnt(1)" ::: "memory");
    __builtin_amdgcn_sched_barrier(0);
  });
  ST(4);
  lds_barrier();
  if (ht < 20) {
    float y = (lds[L_GATE + ht] + lds[L_GATE + 20 + ht]) + (lds[L_GATE + 40 + ht] + lds[L_GATE + 60 + ht]);
    y = rbf(rbf(y) + bflo(b2[0]));
    gate[b * D_ + 20 * h + ht] = rbf(y / (1.0f + __expf(-y)));
  }
  lds_barrier();
  ST(5);
  unsigned od;
  {
    const int k = 32 * hw + 4 * (c & 7) + g;
    const float s = (lds[L_OP + k] + lds[L_OP + 128 + k]) + (lds[L_OP + 256 + k] + lds[L_OP + 384 + k]);
    od = bfbits(s); od |= od << 16;
  }
  f32x4 sacc[5];
#pragma unroll
  for (int u = 0; u < 5; ++u) sacc[u] = f32x4{0.f, 0.f, 0.f, 0.f};
  static_for<0, 8>([&](auto T) {
    constexpr int t = T;
    const v4u a = onehot<t>(od, mask);
#pragma unroll
    for (int u = 0; u < 5; ++u) sacc[u] = mfma16(a, wr[t * 5 + u], sacc[u]);
  });
  if (lane < 32) {
#pragma unroll
    for (int u = 0; u < 5; ++u) *(f32x4*)&lds[hw * 640 + u * 128 + 8 * c + 4 * g] = sacc[u];
  }
  lds_barrier();
  if (ht < 160) {
    const f32x4 s = (*(const f32x4*)&lds[4 * ht] + *(const f32x4*)&lds[640 + 4 * ht]) + (*(const f32x4*)&lds[1280 + 4 * ht] + *(const f32x4*)&lds[1920 + 4 * ht]);
    *(f32x4*)&part[(size_t)bh * D_ + 4 * ht] = s;
  }
  ST(6);
}

__global__ void __launch_bounds__(256)
final_kernel(const u16* __restrict__ x, const float* __restrict__ part, const float* __restrict__ gate, u16* __restrict__ out) {
  const int i = blockIdx.x * 256 + threadIdx.x, b = i / D_, col = i - b * D_;
  float v[32];
#pragma unroll
  for (int hh = 0; hh < 32; ++hh) v[hh] = part[(size_t)(b * 32 + hh) * D_ + col];
  const float gt = gate[i], xx = bflo(x[i]);
#pragma unroll
  for (int s = 16; s > 0; s >>= 1)
#pragma unroll
    for (int hh = 0; hh < s; ++hh) v[hh] += v[hh + s];
  out[i] = (u16)bfbits(xx + rbf(rbf(v[0]) * gt));
}

extern "C" void kernel_launch(void* const* d_in, const int* in_sizes, int n_in, void* d_out, int out_size, void* d_ws,
                              size_t ws_size, hipStream_t stream) {
  const u16* x = (const u16*)d_in[0];
  const u16* kc = (const u16*)d_in[1];
  const u16* vc = (const u16*)d_in[2];
  const u16* wq = (const u16*)d_in[3];
  const u16* wo = (const u16*)d_in[4];
  const u16* w1 = (const u16*)d_in[6];
  const u16* b1 = (const u16*)d_in[7];
  const u16* w2 = (const u16*)d_in[8];
  const u16* b2 = (const u16*)d_in[9];
  float* part = (float*)d_ws;
  float* gate = part + 256 * D_;
  attn_kernel<<<256, 512, 0, stream>>>(x, kc, vc, wq, wo, w1, b1, w2, b2, part, gate);
  final_kernel<<<20, 256, 0, stream>>>(x, part, gate, (u16*)d_out);
}
```

```cpp
#include <hip/hip_runtime.h>
#include <hip/hip_bf16.h>
#include <stdint.h>

typedef unsigned v4u __attribute__((ext_vector_type(4)));
typedef float f32x4 __attribute__((ext_vector_type(4)));
typedef __bf16 bf16x8 __attribute__((ext_vector_type(8)));
typedef __bf16 bf16x2 __attribute__((ext_vector_type(2)));
typedef unsigned short u16;

constexpr int B_ = 8, D_ = 640, H_ = 32, SK = 4096, DH = 128, F_ = 1728;
#define RING 32
#define K_AUX 2
#define V_AUX 2
#define W_AUX 0
#define VTHR 256
#define WO_INFL 4
#define HPRIO 3

__device__ __forceinline__ float rbf(float x) { return (float)(__bf16)x; }
__device__ __forceinline__ unsigned bfbits(float x) { return (unsigned)__builtin_bit_cast(u16, (__bf16)x); }
__device__ __forceinline__ float bflo(unsigned u) { return __builtin_bit_cast(float, u << 16); }
__device__ __forceinline__ float bfhi(unsigned u) { return __builtin_bit_cast(float, u & 0xffff0000u); }

template <int CTRL> __device__ __forceinline__ float dppf(float x) {
  return __builtin_bit_cast(float, __builtin_amdgcn_mov_dpp(__builtin_bit_cast(int, x), CTRL, 0xf, 0xf, true));
}
template <int CTRL> __device__ __forceinline__ unsigned dppu(unsigned x) {
  return (unsigned)__builtin_amdgcn_mov_dpp((int)x, CTRL, 0xf, 0xf, true);
}
constexpr int XOR1 = 0xB1, XOR2 = 0x4E, XOR7 = 0x141, XOR8 = 0x128, NEWBCAST = 0x150;

__device__ __forceinline__ float wave_sum(float x) {
  x += dppf<XOR1>(x); x += dppf<XOR2>(x); x += dppf<XOR7>(x); x += dppf<XOR8>(x);
  auto s = __builtin_amdgcn_permlane16_swap(__float_as_uint(x), __float_as_uint(x), false, false);
  x = __uint_as_float(s[0]) + __uint_as_float(s[1]);
  auto t = __builtin_amdgcn_permlane32_swap(__float_as_uint(x), __float_as_uint(x), false, false);
  return __uint_as_float(t[0]) + __uint_as_float(t[1]);
}
__device__ __forceinline__ float wave_max(float x) {
  x = fmaxf(x, dppf<XOR1>(x)); x = fmaxf(x, dppf<XOR2>(x)); x = fmaxf(x, dppf<XOR7>(x)); x = fmaxf(x, dppf<XOR8>(x));
  auto s = __builtin_amdgcn_permlane16_swap(__float_as_uint(x), __float_as_uint(x), false, false);
  x = fmaxf(__uint_as_float(s[0]), __uint_as_float(s[1]));
  auto t = __builtin_amdgcn_permlane32_swap(__float_as_uint(x), __float_as_uint(x), false, false);
  return fmaxf(__uint_as_float(t[0]), __uint_as_float(t[1]));
}
__device__ __forceinline__ float dot2(unsigned a, unsigned b, float acc) {
  return __builtin_amdgcn_fdot2_f32_bf16(__builtin_bit_cast(bf16x2, a), __builtin_bit_cast(bf16x2, b), acc, false);
}
__device__ __forceinline__ f32x4 mfma16(v4u a, v4u b, f32x4 c) {
  return __builtin_amdgcn_mfma_f32_16x16x32_bf16(__builtin_bit_cast(bf16x8, a), __builtin_bit_cast(bf16x8, b), c, 0, 0, 0);
}
template <int J> __device__ __forceinline__ v4u onehot(unsigned pdup, const v4u& mask) {
  const unsigned pb = dppu<NEWBCAST + J>(pdup);
  v4u a;
  a.x = pb & mask.x; a.y = pb & mask.y; a.z = pb & mask.z; a.w = pb & mask.w;
  return a;
}
__device__ __forceinline__ void lds_barrier() {
  asm volatile("s_waitcnt lgkmcnt(0)" ::: "memory");
  __builtin_amdgcn_s_barrier();
  asm volatile("" ::: "memory");
}
__device__ __forceinline__ void ln_stats(const float (&xv)[10], float& mb, float& sd) {
  float s = 0.f;
#pragma unroll
  for (int i = 0; i < 10; ++i) s += xv[i];
  s = wave_sum(s);
  const float mean = s * (1.0f / D_);
  float q = 0.f;
#pragma unroll
  for (int i = 0; i < 10; ++i) { const float d = xv[i] - mean; q = fmaf(d, d, q); }
  q = wave_sum(q);
  const float var = rbf(q * (1.0f / D_));
  mb = rbf(mean);
  sd = rbf(sqrtf(rbf(var + 1e-5f)));
}
__device__ __forceinline__ float ln_apply(float x, float mb, float sd) { return rbf(rbf(x - mb) / sd); }

template <int I, int N, typename Fn> __device__ __forceinline__ void static_for(Fn&& f) {
  if constexpr (I < N) { f(std::integral_constant<int, I>{}); static_for<I + 1, N>(f); }
}
__device__ __forceinline__ float gelu_bf(float u) {
  constexpr float K1 = -2.0f * 0.7978845608028654f * 1.4426950408889634f;
  constexpr float K2 = K1 * 0.044715f;
  const float e = __builtin_amdgcn_exp2f(u * fmaf(K2, u * u, K1));
  return rbf(u * __builtin_amdgcn_rcpf(1.0f + e));
}

#define ST(i) do {} while (0)

constexpr int L_SM = 2560, L_GATE = 2576, L_OP = 2688, L_END = L_OP + 512;

__global__ void __launch_bounds__(512, 2)
attn_kernel(const u16* __restrict__ x, const u16* __restrict__ kc, const u16* __restrict__ vc,
            const u16* __restrict__ wq, const u16* __restrict__ wo, const u16* __restrict__ w1, const u16* __restrict__ b1,
            const u16* __restrict__ w2, const u16* __restrict__ b2, float* __restrict__ part, float* __restrict__ gate) {
  __shared__ __attribute__((aligned(16))) float lds[L_END];
  const int bh = blockIdx.x, b = bh >> 5, h = bh & 31;
  const int tid = threadIdx.x, lane = tid & 63, w = __builtin_amdgcn_readfirstlane(tid >> 6);
  const int g = lane >> 4, c = lane & 15;
  ST(0);
  v4u mask;
  {
    const unsigned m = (c & 1) ? 0xffff0000u : 0x0000ffffu;
    mask.x = (c >> 1) == 0 ? m : 0u; mask.y = (c >> 1) == 1 ? m : 0u; mask.z = (c >> 1) == 2 ? m : 0u; mask.w = (c >> 1) == 3 ? m : 0u;
  }
  constexpr float L2E = 1.4426950408889634f;

  if (w < 4) {
    auto swave = [&](auto VAUX) __attribute__((always_inline)) {
    constexpr int vaux = VAUX;
    const __amdgpu_buffer_rsrc_t rK = __builtin_amdgcn_make_buffer_rsrc((void*)(kc + (size_t)bh * SK * DH), 0, SK * DH * 2, 0x00020000);
    const __amdgpu_buffer_rsrc_t rV = __builtin_amdgcn_make_buffer_rsrc((void*)(vc + (size_t)bh * SK * DH), 0, SK * DH * 2, 0x00020000);
    v4u ring[RING];
    const int koff = w * 1024 + lane * 16;
#pragma unroll
    for (int i = 0; i < RING; ++i) { ring[i] = __builtin_amdgcn_raw_buffer_load_b128(rK, koff, i * 4096, K_AUX); __builtin_amdgcn_sched_barrier(0); }
    ST(1);
    lds_barrier();
    ST(2);
    v4u qp;
    {
      f32x4 s0 = *(const f32x4*)&lds[8 * c], s1 = *(const f32x4*)&lds[8 * c + 4];
#pragma unroll
      for (int ww = 1; ww < 4; ++ww) {
        s0 += *(const f32x4*)&lds[ww * 128 + 8 * c];
        s1 += *(const f32x4*)&lds[ww * 128 + 8 * c + 4];
      }
      qp.x = bfbits(s0.x) | (bfbits(s0.y) << 16); qp.y = bfbits(s0.z) | (bfbits(s0.w) << 16);
      qp.z = bfbits(s1.x) | (bfbits(s1.y) << 16); qp.w = bfbits(s1.z) | (bfbits(s1.w) << 16);
    }
    constexpr int NIT = 256 / RING, GPI = RING / 16;
    float sc[16];
#pragma unroll
    for (int i = 0; i < 16; ++i) sc[i] = 0.f;
    auto kbody = [&](auto LAST, int sbase) {
      constexpr bool last = LAST;
      float nsc[GPI];
#pragma unroll
      for (int gi = 0; gi < GPI; ++gi) {
        float xx[16];
#pragma unroll
        for (int j = 0; j < 16; ++j) {
          const int i = gi * 16 + j;
          float d = dot2(ring[i].x, qp.x, 0.f);
          d = dot2(ring[i].y, qp.y, d); d = dot2(ring[i].z, qp.z, d); d = dot2(ring[i].w, qp.w, d);
          asm volatile("" : "+v"(d));
          xx[j] = d;
          if constexpr (last) ring[i] = __builtin_amdgcn_raw_buffer_load_b128(rV, koff, i * 4096, vaux);
          else ring[i] = __builtin_amdgcn_raw_buffer_load_b128(rK, koff, sbase + i * 4096, K_AUX);
          __builtin_amdgcn_sched_barrier(0);
        }
#define TR_STEP(HALF, CTRL, BIT) _Pragma("unroll") for (int hh = 0; hh < (HALF); ++hh) { \
          const float lo_ = xx[hh], hi_ = xx[hh + (HALF)]; const float t1_ = lo_ + dppf<CTRL>(lo_), t2_ = hi_ + dppf<CTRL>(hi_); \
          xx[hh] = (c & (BIT)) ? t2_ : t1_; }
        TR_STEP(8, XOR8, 8) TR_STEP(4, XOR7, 4) TR_STEP(2, XOR2, 2) TR_STEP(1, XOR1, 1)
#undef TR_STEP
        nsc[gi] = rbf(rbf(xx[0]) * 0.08838834764831845f);
        asm volatile("" : "+v"(nsc[gi]));
      }
#pragma unroll
      for (int i = 0; i < 16 - GPI; ++i) sc[i] = sc[i + GPI];
#pragma unroll
      for (int gi = 0; gi < GPI; ++gi) sc[16 - GPI + gi] = nsc[gi];
    };
#pragma unroll 1
    for (int it = 0; it < NIT - 1; ++it) kbody(std::false_type{}, (it + 1) * RING * 4096);
    kbody(std::true_type{}, 0);
    ST(3);
    float* sm = &lds[L_SM];
    {
      float m = sc[0];
#pragma unroll
      for (int i = 1; i < 16; ++i) m = fmaxf(m, sc[i]);
      m = wave_max(m);
      float l = 0.f;
#pragma unroll
      for (int i = 0; i < 16; ++i) l += __builtin_amdgcn_exp2f((sc[i] - m) * L2E);
      l = wave_sum(l);
      if (lane == 0) { sm[2 * w] = m; sm[2 * w + 1] = l; }
    }
    lds_barrier();
    unsigned pd[16];
    {
      const f32x4 t0 = *(const f32x4*)&sm[0], t1 = *(const f32x4*)&sm[4];
      const float m = fmaxf(fmaxf(t0.x, t0.z), fmaxf(t1.x, t1.z));
      float l = t0.y * __builtin_amdgcn_exp2f((t0.x - m) * L2E);
      l = fmaf(t0.w, __builtin_amdgcn_exp2f((t0.z - m) * L2E), l);
      l = fmaf(t1.y, __builtin_amdgcn_exp2f((t1.x - m) * L2E), l);
      l = fmaf(t1.w, __builtin_amdgcn_exp2f((t1.z - m) * L2E), l);
      const float inv = 1.0f / l;
#pragma unroll
      for (int i = 0; i < 16; ++i) { const unsigned p = bfbits(__builtin_amdgcn_exp2f((sc[i] - m) * L2E) * inv); pd[i] = p | (p << 16); }
    }
    ST(4);
    f32x4 oacc = {0.f, 0.f, 0.f, 0.f};
#pragma unroll 1
    for (int it = 0; it < NIT - 1; ++it) {
      const int sbase = (it + 1) * RING * 4096;
      static_for<0, RING>([&](auto I) {
        constexpr int i = I;
        const v4u a = onehot<(i & 15)>(pd[i >> 4], mask);
        oacc = mfma16(a, ring[i], oacc);
        asm volatile("" : "+v"(oacc));
        ring[i] = __builtin_amdgcn_raw_buffer_load_b128(rV, koff, sbase + i * 4096, vaux);
        __builtin_amdgcn_sched_barrier(0);
      });
#pragma unroll
      for (int i = 0; i < 16 - GPI; ++i) pd[i] = pd[i + GPI];
    }
    static_for<0, RING>([&](auto I) {
      constexpr int i = I;
      const v4u a = onehot<(i & 15)>(pd[i >> 4], mask);
      oacc = mfma16(a, ring[i], oacc);
      asm volatile("" : "+v"(oacc));
      __builtin_amdgcn_sched_barrier(0);
    });
    ST(5);
    if (lane < 32) *(f32x4*)&lds[L_OP + w * 128 + 8 * c + 4 * g] = oacc;
    lds_barrier();
    ST(6);
    };
    if (bh >= VTHR) swave(std::integral_constant<int, 0>{});
    else swave(std::integral_constant<int, 2>{});
    return;
  }

  __builtin_amdgcn_s_setprio(HPRIO);
  const int hw = w - 4, ht = tid - 256;
  const __amdgpu_buffer_rsrc_t rQ = __builtin_amdgcn_make_buffer_rsrc((void*)(wq + h * DH), 0, D_ * H_ * DH * 2 - h * DH * 2, 0x00020000);
  const __amdgpu_buffer_rsrc_t rO = __builtin_amdgcn_make_buffer_rsrc((void*)(wo + (size_t)h * DH * D_), 0, DH * D_ * 2, 0x00020000);
  const unsigned* x32 = (const unsigned*)(x + b * D_);
  unsigned xr[5];
#pragma unroll
  for (int i = 0; i < 5; ++i) xr[i] = x32[lane + 64 * i];
  const u16* xb = x + b * D_;
  const unsigned xo0 = xb[160 * hw + 4 * c + g];
  const unsigned xo1 = xb[160 * hw + 64 + 4 * c + g];
  const unsigned xo2 = xb[160 * hw + 128 + 4 * (c & 7) + g];
  const unsigned xg = xb[20 * h + (lane < 20 ? lane : 0)];
  const int gf = ht < 216 ? ht : 0;
  const v4u gw1 = ((const v4u*)w1)[gf], gb1 = ((const v4u*)b1)[gf], gw2 = ((const v4u*)w2)[gf];
  const unsigned b2v = b2[0];
  __builtin_amdgcn_sched_barrier(0);
  v4u wr[40];
  const int qoff = (160 * hw + g) * (H_ * DH * 2) + c * 16;
#pragma unroll
  for (int t = 0; t < 40; ++t) { wr[t] = __builtin_amdgcn_raw_buffer_load_b128(rQ, qoff, t * 4 * (H_ * DH * 2), W_AUX); __builtin_amdgcn_sched_barrier(0); }
  ST(1);
  float xv[10];
#pragma unroll
  for (int i = 0; i < 5; ++i) { xv[2 * i] = bflo(xr[i]); xv[2 * i + 1] = bfhi(xr[i]); }
  float mb, sd;
  ln_stats(xv, mb, sd);
  unsigned xn0 = bfbits(ln_apply(bflo(xo0), mb, sd)), xn1 = bfbits(ln_apply(bflo(xo1), mb, sd)), xn2 = bfbits(ln_apply(bflo(xo2), mb, sd));
  xn0 |= xn0 << 16; xn1 |= xn1 << 16; xn2 |= xn2 << 16;
  {
    f32x4 acc = {0.f, 0.f, 0.f, 0.f};
    static_for<0, 40>([&](auto T) {
      constexpr int t = T;
      const v4u a = onehot<(t & 15)>(t < 16 ? xn0 : t < 32 ? xn1 : xn2, mask);
      acc = mfma16(a, wr[t], acc);
    });
    if (lane < 32) *(f32x4*)&lds[hw * 128 + 8 * c + 4 * g] = acc;
  }
  ST(2);
  lds_barrier();
  __builtin_amdgcn_s_setprio(0);
  {
    float w1v[8], b1v[8], w2v[8];
    {
      const bool ok = ht < 216;
#pragma unroll
      for (int i = 0; i < 4; ++i) {
        w1v[2 * i] = bflo(gw1[i]); w1v[2 * i + 1] = bfhi(gw1[i]);
        b1v[2 * i] = bflo(gb1[i]); b1v[2 * i + 1] = bfhi(gb1[i]);
        w2v[2 * i] = ok ? bflo(gw2[i]) : 0.f; w2v[2 * i + 1] = ok ? bfhi(gw2[i]) : 0.f;
      }
    }
#pragma unroll
    for (int i = 0; i < 10; ++i) xv[i] = ln_apply(xv[i], mb, sd);
    float mb2, sd2;
    ln_stats(xv, mb2, sd2);
    const float hcol = ln_apply(ln_apply(bflo(xg), mb, sd), mb2, sd2);
    float ycol = 0.f;
#pragma unroll 1
    for (int j = 0; j < 20; ++j) {
      const float hv = __builtin_bit_cast(float, __builtin_amdgcn_readlane(__builtin_bit_cast(int, hcol), j));
      float y = 0.f;
#pragma unroll
      for (int i = 0; i < 8; ++i) y = fmaf(gelu_bf(rbf(rbf(hv * w1v[i]) + b1v[i])), w2v[i], y);
      y = wave_sum(y);
      ycol = (lane == j) ? y : ycol;
    }
    if (lane < 20) lds[L_GATE + hw * 20 + lane] = ycol;
  }
  ST(3);
  __builtin_amdgcn_s_setprio(HPRIO);
  const int ooff = (32 * hw + g) * (D_ * 2) + c * 16;
  static_for<0, 40>([&](auto I) {
    constexpr int i = I;
    wr[i] = __builtin_amdgcn_raw_buffer_load_b128(rO, ooff, (i / 5) * 4 * (D_ * 2) + (i % 5) * 256, W_AUX);
    if constexpr (i >= WO_INFL - 1) asm volatile("s_waitcnt vmcnt(%0)" :: "i"(WO_INFL - 1) : "memory");
    __builtin_amdgcn_sched_barrier(0);
  });
  ST(4);
  lds_barrier();
  if (ht < 20) {
    float y = (lds[L_GATE + ht] + lds[L_GATE + 20 + ht]) + (lds[L_GATE + 40 + ht] + lds[L_GATE + 60 + ht]);
    y = rbf(rbf(y) + bflo(b2v));
    gate[b * D_ + 20 * h + ht] = rbf(y / (1.0f + __expf(-y)));
  }
  lds_barrier();
  ST(5);
  unsigned od;
  {
    const int k = 32 * hw + 4 * (c & 7) + g;
    const float s = (lds[L_OP + k] + lds[L_OP + 128 + k]) + (lds[L_OP + 256 + k] + lds[L_OP + 384 + k]);
    od = bfbits(s); od |= od << 16;
  }
  f32x4 sacc[5];
#pragma unroll
  for (int u = 0; u < 5; ++u) sacc[u] = f32x4{0.f, 0.f, 0.f, 0.f};
  static_for<0, 8>([&](auto T) {
    constexpr int t = T;
    const v4u a = onehot<t>(od, mask);
#pragma unroll
    for (int u = 0; u < 5; ++u) sacc[u] = mfma16(a, wr[t * 5 + u], sacc[u]);
  });
  if (lane < 32) {
#pragma unroll
    for (int u = 0; u < 5; ++u) *(f32x4*)&lds[hw * 640 + u * 128 + 8 * c + 4 * g] = sacc[u];
  }
  lds_barrier();
  if (ht < 160) {
    const f32x4 s = (*(const f32x4*)&lds[4 * ht] + *(const f32x4*)&lds[640 + 4 * ht]) + (*(const f32x4*)&lds[1280 + 4 * ht] + *(const f32x4*)&lds[1920 + 4 * ht]);
    *(f32x4*)&part[(size_t)bh * D_ + 4 * ht] = s;
  }
  ST(6);
}

__global__ void __launch_bounds__(256)
final_kernel(const u16* __restrict__ x, const float* __restrict__ part, const float* __restrict__ gate, u16* __restrict__ out) {
  const int i = blockIdx.x * 256 + threadIdx.x, b = i / D_, col = i - b * D_;
  float v[32];
#pragma unroll
  for (int hh = 0; hh < 32; ++hh) v[hh] = part[(size_t)(b * 32 + hh) * D_ + col];
  const float gt = gate[i], xx = bflo(x[i]);
#pragma unroll
  for (int s = 16; s > 0; s >>= 1)
#pragma unroll
    for (int hh = 0; hh < s; ++hh) v[hh] += v[hh + s];
  out[i] = (u16)bfbits(xx + rbf(rbf(v[0]) * gt));
}

extern "C" void kernel_launch(void* const* d_in, const int* in_sizes, int n_in, void* d_out, int out_size, void* d_ws,
                              size_t ws_size, hipStream_t stream) {
  const u16* x = (const u16*)d_in[0];
  const u16* kc = (const u16*)d_in[1];
  const u16* vc = (const u16*)d_in[2];
  const u16* wq = (const u16*)d_in[3];
  const u16* wo = (const u16*)d_in[4];
  const u16* w1 = (const u16*)d_in[6];
  const u16* b1 = (const u16*)d_in[7];
  const u16* w2 = (const u16*)d_in[8];
  const u16* b2 = (const u16*)d_in[9];
  float* part = (float*)d_ws;
  float* gate = part + 256 * D_;
  attn_kernel<<<256, 512, 0, stream>>>(x, kc, vc, wq, wo, w1, b1, w2, b2, part, gate);
  final_kernel<<<20, 256, 0, stream>>>(x, part, gate, (u16*)d_out);
}
```

```cpp
#include <hip/hip_runtime.h>
#include <hip/hip_bf16.h>
#include <stdint.h>

typedef unsigned v4u __attribute__((ext_vector_type(4)));
typedef unsigned v2u __attribute__((ext_vector_type(2)));
typedef float f32x4 __attribute__((ext_vector_type(4)));
typedef __bf16 bf16x8 __attribute__((ext_vector_type(8)));
typedef __bf16 bf16x2 __attribute__((ext_vector_type(2)));
typedef unsigned short u16;
typedef __attribute__((address_space(1))) unsigned gu32;
typedef __attribute__((address_space(1))) unsigned long long gu64;
constexpr size_t WS_EPOCH = 0, WS_GRAN = 65536;

constexpr int B_ = 8, D_ = 640, H_ = 32, SK = 4096, DH = 128, F_ = 1728;
#define RING 32
#define K_AUX 2
#define V_AUX 2
#define W_AUX 0
#define VTHR 256
#define WO_INFL 4
#define HPRIO 3

__device__ __forceinline__ float rbf(float x) { return (float)(__bf16)x; }
__device__ __forceinline__ unsigned bfbits(float x) { return (unsigned)__builtin_bit_cast(u16, (__bf16)x); }
__device__ __forceinline__ float bflo(unsigned u) { return __builtin_bit_cast(float, u << 16); }
__device__ __forceinline__ float bfhi(unsigned u) { return __builtin_bit_cast(float, u & 0xffff0000u); }

template <int CTRL> __device__ __forceinline__ float dppf(float x) {
  return __builtin_bit_cast(float, __builtin_amdgcn_mov_dpp(__builtin_bit_cast(int, x), CTRL, 0xf, 0xf, true));
}
template <int CTRL> __device__ __forceinline__ unsigned dppu(unsigned x) {
  return (unsigned)__builtin_amdgcn_mov_dpp((int)x, CTRL, 0xf, 0xf, true);
}
constexpr int XOR1 = 0xB1, XOR2 = 0x4E, XOR7 = 0x141, XOR8 = 0x128, NEWBCAST = 0x150;

__device__ __forceinline__ float wave_sum(float x) {
  x += dppf<XOR1>(x); x += dppf<XOR2>(x); x += dppf<XOR7>(x); x += dppf<XOR8>(x);
  auto s = __builtin_amdgcn_permlane16_swap(__float_as_uint(x), __float_as_uint(x), false, false);
  x = __uint_as_float(s[0]) + __uint_as_float(s[1]);
  auto t = __builtin_amdgcn_permlane32_swap(__float_as_uint(x), __float_as_uint(x), false, false);
  return __uint_as_float(t[0]) + __uint_as_float(t[1]);
}
__device__ __forceinline__ float rows_sum(float x) {
  auto s = __builtin_amdgcn_permlane16_swap(__float_as_uint(x), __float_as_uint(x), false, false);
  x = __uint_as_float(s[0]) + __uint_as_float(s[1]);
  auto t = __builtin_amdgcn_permlane32_swap(__float_as_uint(x), __float_as_uint(x), false, false);
  return __uint_as_float(t[0]) + __uint_as_float(t[1]);
}
__device__ __forceinline__ float wave_max(float x) {
  x = fmaxf(x, dppf<XOR1>(x)); x = fmaxf(x, dppf<XOR2>(x)); x = fmaxf(x, dppf<XOR7>(x)); x = fmaxf(x, dppf<XOR8>(x));
  auto s = __builtin_amdgcn_permlane16_swap(__float_as_uint(x), __float_as_uint(x), false, false);
  x = fmaxf(__uint_as_float(s[0]), __uint_as_float(s[1]));
  auto t = __builtin_amdgcn_permlane32_swap(__float_as_uint(x), __float_as_uint(x), false, false);
  return fmaxf(__uint_as_float(t[0]), __uint_as_float(t[1]));
}
__device__ __forceinline__ float dot2(unsigned a, unsigned b, float acc) {
  return __builtin_amdgcn_fdot2_f32_bf16(__builtin_bit_cast(bf16x2, a), __builtin_bit_cast(bf16x2, b), acc, false);
}
__device__ __forceinline__ f32x4 mfma16(v4u a, v4u b, f32x4 c) {
  return __builtin_amdgcn_mfma_f32_16x16x32_bf16(__builtin_bit_cast(bf16x8, a), __builtin_bit_cast(bf16x8, b), c, 0, 0, 0);
}
template <int J> __device__ __forceinline__ v4u onehot(unsigned pdup, const v4u& mask) {
  const unsigned pb = dppu<NEWBCAST + J>(pdup);
  v4u a;
  a.x = pb & mask.x; a.y = pb & mask.y; a.z = pb & mask.z; a.w = pb & mask.w;
  return a;
}
__device__ __forceinline__ void lds_barrier() {
  asm volatile("s_waitcnt lgkmcnt(0)" ::: "memory");
  __builtin_amdgcn_s_barrier();
  asm volatile("" ::: "memory");
}
__device__ __forceinline__ void ln_stats(const float (&xv)[10], float& mb, float& sd) {
  float s = 0.f;
#pragma unroll
  for (int i = 0; i < 10; ++i) s += xv[i];
  s = wave_sum(s);
  const float mean = s * (1.0f / D_);
  float q = 0.f;
#pragma unroll
  for (int i = 0; i < 10; ++i) { const float d = xv[i] - mean; q = fmaf(d, d, q); }
  q = wave_sum(q);
  const float var = rbf(q * (1.0f / D_));
  mb = rbf(mean);
  sd = rbf(sqrtf(rbf(var + 1e-5f)));
}
__device__ __forceinline__ float ln_apply(float x, float mb, float sd) { return rbf(rbf(x - mb) / sd); }

template <int I, int N, typename Fn> __device__ __forceinline__ void static_for(Fn&& f) {
  if constexpr (I < N) { f(std::integral_constant<int, I>{}); static_for<I + 1, N>(f); }
}
__device__ __forceinline__ float gelu_bf(float u) {
  constexpr float K1 = -2.0f * 0.7978845608028654f * 1.4426950408889634f;
  constexpr float K2 = K1 * 0.044715f;
  const float e = __builtin_amdgcn_exp2f(u * fmaf(K2, u * u, K1));
  return rbf(u * __builtin_amdgcn_rcpf(1.0f + e));
}

#define ST(i) do {} while (0)

constexpr int L_SM = 2560, L_GATE = 2576, L_OP = 2688, L_END = L_OP + 512;

__global__ void __launch_bounds__(512, 2)
attn_kernel(const u16* __restrict__ x, const u16* __restrict__ kc, const u16* __restrict__ vc,
            const u16* __restrict__ wq, const u16* __restrict__ wo, const u16* __restrict__ w1, const u16* __restrict__ b1,
            const u16* __restrict__ w2, const u16* __restrict__ b2, unsigned char* ws, u16* __restrict__ out) {
  __shared__ __attribute__((aligned(16))) float lds[L_END];
  const int bh = blockIdx.x, b = bh >> 5, h = bh & 31;
  const int tid = threadIdx.x, lane = tid & 63, w = __builtin_amdgcn_readfirstlane(tid >> 6);
  const int g = lane >> 4, c = lane & 15;
  ST(0);
  v4u mask;
  {
    const unsigned m = (c & 1) ? 0xffff0000u : 0x0000ffffu;
    mask.x = (c >> 1) == 0 ? m : 0u; mask.y = (c >> 1) == 1 ? m : 0u; mask.z = (c >> 1) == 2 ? m : 0u; mask.w = (c >> 1) == 3 ? m : 0u;
  }
  constexpr float L2E = 1.4426950408889634f;

  if (w < 4) {
    auto swave = [&](auto VAUX) __attribute__((always_inline)) {
    constexpr int vaux = VAUX;
    const __amdgpu_buffer_rsrc_t rK = __builtin_amdgcn_make_buffer_rsrc((void*)(kc + (size_t)bh * SK * DH), 0, SK * DH * 2, 0x00020000);
    const __amdgpu_buffer_rsrc_t rV = __builtin_amdgcn_make_buffer_rsrc((void*)(vc + (size_t)bh * SK * DH), 0, SK * DH * 2, 0x00020000);
    v4u ring[RING];
    const int koff = w * 1024 + lane * 16;
#pragma unroll
    for (int i = 0; i < RING; ++i) { ring[i] = __builtin_amdgcn_raw_buffer_load_b128(rK, koff, i * 4096, K_AUX); __builtin_amdgcn_sched_barrier(0); }
    ST(1);
    lds_barrier();
    ST(2);
    v4u qp;
    {
      f32x4 s0 = *(const f32x4*)&lds[8 * c], s1 = *(const f32x4*)&lds[8 * c + 4];
#pragma unroll
      for (int ww = 1; ww < 4; ++ww) {
        s0 += *(const f32x4*)&lds[ww * 128 + 8 * c];
        s1 += *(const f32x4*)&lds[ww * 128 + 8 * c + 4];
      }
      qp.x = bfbits(s0.x) | (bfbits(s0.y) << 16); qp.y = bfbits(s0.z) | (bfbits(s0.w) << 16);
      qp.z = bfbits(s1.x) | (bfbits(s1.y) << 16); qp.w = bfbits(s1.z) | (bfbits(s1.w) << 16);
    }
    constexpr int NIT = 256 / RING, GPI = RING / 16;
    float sc[16];
#pragma unroll
    for (int i = 0; i < 16; ++i) sc[i] = 0.f;
    auto kbody = [&](auto LAST, int sbase) {
      constexpr bool last = LAST;
      float nsc[GPI];
#pragma unroll
      for (int gi = 0; gi < GPI; ++gi) {
        float xx[16];
#pragma unroll
        for (int j = 0; j < 16; ++j) {
          const int i = gi * 16 + j;
          float d = dot2(ring[i].x, qp.x, 0.f);
          d = dot2(ring[i].y, qp.y, d); d = dot2(ring[i].z, qp.z, d); d = dot2(ring[i].w, qp.w, d);
          asm volatile("" : "+v"(d));
          xx[j] = d;
          if constexpr (last) ring[i] = __builtin_amdgcn_raw_buffer_load_b128(rV, koff, i * 4096, vaux);
          else ring[i] = __builtin_amdgcn_raw_buffer_load_b128(rK, koff, sbase + i * 4096, K_AUX);
          __builtin_amdgcn_sched_barrier(0);
        }
#define TR_STEP(HALF, CTRL, BIT) _Pragma("unroll") for (int hh = 0; hh < (HALF); ++hh) { \
          const float lo_ = xx[hh], hi_ = xx[hh + (HALF)]; const float t1_ = lo_ + dppf<CTRL>(lo_), t2_ = hi_ + dppf<CTRL>(hi_); \
          xx[hh] = (c & (BIT)) ? t2_ : t1_; }
        TR_STEP(8, XOR8, 8) TR_STEP(4, XOR7, 4) TR_STEP(2, XOR2, 2) TR_STEP(1, XOR1, 1)
#undef TR_STEP
        nsc[gi] = rbf(rbf(xx[0]) * 0.08838834764831845f);
        asm volatile("" : "+v"(nsc[gi]));
      }
#pragma unroll
      for (int i = 0; i < 16 - GPI; ++i) sc[i] = sc[i + GPI];
#pragma unroll
      for (int gi = 0; gi < GPI; ++gi) sc[16 - GPI + gi] = nsc[gi];
    };
#pragma unroll 1
    for (int it = 0; it < NIT - 1; ++it) kbody(std::false_type{}, (it + 1) * RING * 4096);
    kbody(std::true_type{}, 0);
    ST(3);
    float* sm = &lds[L_SM];
    {
      float m = sc[0];
#pragma unroll
      for (int i = 1; i < 16; ++i) m = fmaxf(m, sc[i]);
      m = wave_max(m);
      float l = 0.f;
#pragma unroll
      for (int i = 0; i < 16; ++i) l += __builtin_amdgcn_exp2f((sc[i] - m) * L2E);
      l = wave_sum(l);
      if (lane == 0) { sm[2 * w] = m; sm[2 * w + 1] = l; }
    }
    lds_barrier();
    unsigned pd[16];
    {
      const f32x4 t0 = *(const f32x4*)&sm[0], t1 = *(const f32x4*)&sm[4];
      const float m = fmaxf(fmaxf(t0.x, t0.z), fmaxf(t1.x, t1.z));
      float l = t0.y * __builtin_amdgcn_exp2f((t0.x - m) * L2E);
      l = fmaf(t0.w, __builtin_amdgcn_exp2f((t0.z - m) * L2E), l);
      l = fmaf(t1.y, __builtin_amdgcn_exp2f((t1.x - m) * L2E), l);
      l = fmaf(t1.w, __builtin_amdgcn_exp2f((t1.z - m) * L2E), l);
      const float inv = 1.0f / l;
#pragma unroll
      for (int i = 0; i < 16; ++i) { const unsigned p = bfbits(__builtin_amdgcn_exp2f((sc[i] - m) * L2E) * inv); pd[i] = p | (p << 16); }
    }
    ST(4);
    f32x4 oacc = {0.f, 0.f, 0.f, 0.f};
#pragma unroll 1
    for (int it = 0; it < NIT - 1; ++it) {
      const int sbase = (it + 1) * RING * 4096;
      static_for<0, RING>([&](auto I) {
        constexpr int i = I;
        const v4u a = onehot<(i & 15)>(pd[i >> 4], mask);
        oacc = mfma16(a, ring[i], oacc);
        asm volatile("" : "+v"(oacc));
        ring[i] = __builtin_amdgcn_raw_buffer_load_b128(rV, koff, sbase + i * 4096, vaux);
        __builtin_amdgcn_sched_barrier(0);
      });
#pragma unroll
      for (int i = 0; i < 16 - GPI; ++i) pd[i] = pd[i + GPI];
    }
    static_for<0, RING>([&](auto I) {
      constexpr int i = I;
      const v4u a = onehot<(i & 15)>(pd[i >> 4], mask);
      oacc = mfma16(a, ring[i], oacc);
      asm volatile("" : "+v"(oacc));
      __builtin_amdgcn_sched_barrier(0);
    });
    ST(5);
    if (lane < 32) *(f32x4*)&lds[L_OP + w * 128 + 8 * c + 4 * g] = oacc;
    lds_barrier();
    ST(6);
    };
    swave(std::integral_constant<int, V_AUX>{});
    return;
  }

  __builtin_amdgcn_s_setprio(HPRIO);
  const int hw = w - 4, ht = tid - 256;
  const __amdgpu_buffer_rsrc_t rQ = __builtin_amdgcn_make_buffer_rsrc((void*)(wq + h * DH), 0, D_ * H_ * DH * 2 - h * DH * 2, 0x00020000);
  const __amdgpu_buffer_rsrc_t rO = __builtin_amdgcn_make_buffer_rsrc((void*)(wo + (size_t)h * DH * D_), 0, DH * D_ * 2, 0x00020000);
  const unsigned* x32 = (const unsigned*)(x + b * D_);
  unsigned xr[5];
#pragma unroll
  for (int i = 0; i < 5; ++i) xr[i] = x32[lane + 64 * i];
  const u16* xb = x + b * D_;
  const unsigned xo0 = xb[160 * hw + 4 * c + g];
  const unsigned xo1 = xb[160 * hw + 64 + 4 * c + g];
  const unsigned xo2 = xb[160 * hw + 128 + 4 * (c & 7) + g];
  const unsigned xg = xb[20 * h + (lane < 20 ? lane : 0)];
  const int gf = ht < 216 ? ht : 0;
  const v4u gw1 = ((const v4u*)w1)[gf], gb1 = ((const v4u*)b1)[gf], gw2 = ((const v4u*)w2)[gf];
  const unsigned b2v = b2[0];
  gu32* epw = (gu32*)(ws + WS_EPOCH) + b * 64;
  const unsigned tag = __hip_atomic_load(epw, __ATOMIC_RELAXED, __HIP_MEMORY_SCOPE_AGENT) + 0x9E3779B9u;
  const unsigned xres = x32[10 * h + (c < 10 ? c : 0)];
  __builtin_amdgcn_sched_barrier(0);
  v4u wr[40];
  const int qoff = (160 * hw + g) * (H_ * DH * 2) + c * 16;
#pragma unroll
  for (int t = 0; t < 40; ++t) { wr[t] = __builtin_amdgcn_raw_buffer_load_b128(rQ, qoff, t * 4 * (H_ * DH * 2), W_AUX); __builtin_amdgcn_sched_barrier(0); }
  ST(1);
  float xv[10];
#pragma unroll
  for (int i = 0; i < 5; ++i) { xv[2 * i] = bflo(xr[i]); xv[2 * i + 1] = bfhi(xr[i]); }
  float mb, sd;
  ln_stats(xv, mb, sd);
  unsigned xn0 = bfbits(ln_apply(bflo(xo0), mb, sd)), xn1 = bfbits(ln_apply(bflo(xo1), mb, sd)), xn2 = bfbits(ln_apply(bflo(xo2), mb, sd));
  xn0 |= xn0 << 16; xn1 |= xn1 << 16; xn2 |= xn2 << 16;
  {
    f32x4 acc = {0.f, 0.f, 0.f, 0.f};
    static_for<0, 40>([&](auto T) {
      constexpr int t = T;
      const v4u a = onehot<(t & 15)>(t < 16 ? xn0 : t < 32 ? xn1 : xn2, mask);
      acc = mfma16(a, wr[t], acc);
    });
    if (lane < 32) *(f32x4*)&lds[hw * 128 + 8 * c + 4 * g] = acc;
  }
  ST(2);
  lds_barrier();
  __builtin_amdgcn_s_setprio(0);
  {
    float w1v[8], b1v[8], w2v[8];
    {
      const bool ok = ht < 216;
#pragma unroll
      for (int i = 0; i < 4; ++i) {
        w1v[2 * i] = bflo(gw1[i]); w1v[2 * i + 1] = bfhi(gw1[i]);
        b1v[2 * i] = bflo(gb1[i]); b1v[2 * i + 1] = bfhi(gb1[i]);
        w2v[2 * i] = ok ? bflo(gw2[i]) : 0.f; w2v[2 * i + 1] = ok ? bfhi(gw2[i]) : 0.f;
      }
    }
#pragma unroll
    for (int i = 0; i < 10; ++i) xv[i] = ln_apply(xv[i], mb, sd);
    float mb2, sd2;
    ln_stats(xv, mb2, sd2);
    const float hcol = ln_apply(ln_apply(bflo(xg), mb, sd), mb2, sd2);
    float ycol = 0.f;
#pragma unroll 1
    for (int j = 0; j < 20; ++j) {
      const float hv = __builtin_bit_cast(float, __builtin_amdgcn_readlane(__builtin_bit_cast(int, hcol), j));
      float y = 0.f;
#pragma unroll
      for (int i = 0; i < 8; ++i) y = fmaf(gelu_bf(rbf(rbf(hv * w1v[i]) + b1v[i])), w2v[i], y);
      y = wave_sum(y);
      ycol = (lane == j) ? y : ycol;
    }
    if (lane < 20) lds[L_GATE + hw * 20 + lane] = ycol;
  }
  ST(3);
  __builtin_amdgcn_s_setprio(HPRIO);
  const int ooff = (32 * hw + g) * (D_ * 2) + c * 16;
  static_for<0, 40>([&](auto I) {
    constexpr int i = I;
    wr[i] = __builtin_amdgcn_raw_buffer_load_b128(rO, ooff, (i / 5) * 4 * (D_ * 2) + (i % 5) * 256, W_AUX);
    if constexpr (i >= WO_INFL - 1) asm volatile("s_waitcnt vmcnt(%0)" :: "i"(WO_INFL - 1) : "memory");
    __builtin_amdgcn_sched_barrier(0);
  });
  ST(4);
  lds_barrier();
  float gt0, gt1;
  {
    const int cc = c < 10 ? 2 * c : 0;
    float y0 = (lds[L_GATE + cc] + lds[L_GATE + 20 + cc]) + (lds[L_GATE + 40 + cc] + lds[L_GATE + 60 + cc]);
    float y1 = (lds[L_GATE + cc + 1] + lds[L_GATE + 21 + cc]) + (lds[L_GATE + 41 + cc] + lds[L_GATE + 61 + cc]);
    y0 = rbf(rbf(y0) + bflo(b2v)); y1 = rbf(rbf(y1) + bflo(b2v));
    gt0 = rbf(y0 / (1.0f + __expf(-y0))); gt1 = rbf(y1 / (1.0f + __expf(-y1)));
  }
  int poff[3];
#pragma unroll
  for (int r = 0; r < 3; ++r) { const int col = ht + 256 * r, cons = col / 20; poff[r] = (cons * 32 + (col - cons * 20)) * 8; }
  int goff = g * 8192 + h * 256 + (c < 10 ? c : 0) * 16;
  asm volatile("" : "+v"(gt0), "+v"(gt1), "+v"(poff[0]), "+v"(poff[1]), "+v"(poff[2]), "+v"(goff));
  lds_barrier();
  ST(5);
  unsigned od;
  {
    const int k = 32 * hw + 4 * (c & 7) + g;
    const float s = (lds[L_OP + k] + lds[L_OP + 128 + k]) + (lds[L_OP + 256 + k] + lds[L_OP + 384 + k]);
    od = bfbits(s); od |= od << 16;
  }
  f32x4 sacc[5];
#pragma unroll
  for (int u = 0; u < 5; ++u) sacc[u] = f32x4{0.f, 0.f, 0.f, 0.f};
  static_for<0, 8>([&](auto T) {
    constexpr int t = T;
    const v4u a = onehot<t>(od, mask);
#pragma unroll
    for (int u = 0; u < 5; ++u) sacc[u] = mfma16(a, wr[t * 5 + u], sacc[u]);
  });
  if (lane < 32) {
#pragma unroll
    for (int u = 0; u < 5; ++u) *(f32x4*)&lds[hw * 640 + u * 128 + 8 * c + 4 * g] = sacc[u];
  }
  lds_barrier();
  {
    const __amdgpu_buffer_rsrc_t rP = __builtin_amdgcn_make_buffer_rsrc((void*)(ws + WS_GRAN + (size_t)bh * 8192), 0, 8192, 0x00020000);
    float sv[3];
#pragma unroll
    for (int r = 0; r < 3; ++r) { const int col = ht + 256 * r; sv[r] = (lds[col] + lds[640 + col]) + (lds[1280 + col] + lds[1920 + col]); }
#pragma unroll
    for (int r = 0; r < 3; ++r) { v2u gv; gv.x = __float_as_uint(sv[r]); gv.y = tag; __builtin_amdgcn_raw_buffer_store_b64(gv, rP, poff[r], 0, 16); }
  }
  ST(6);
  if (hw != 0) return;
  {
    const __amdgpu_buffer_rsrc_t rG = __builtin_amdgcn_make_buffer_rsrc((void*)(ws + WS_GRAN + (size_t)b * 32 * 8192), 0, 32 * 8192, 0x00020000);
    v4u gr[8];
    const unsigned long long tlim = __builtin_amdgcn_s_memrealtime() + 2000000ull;
    for (;;) {
      asm volatile("" : "+v"(goff) :: "memory");
#pragma unroll
      for (int i = 0; i < 8; ++i) gr[i] = __builtin_amdgcn_raw_buffer_load_b128(rG, goff, i * 4 * 8192, 16);
      unsigned bad = 0;
#pragma unroll
      for (int i = 0; i < 8; ++i) bad |= (gr[i].y ^ tag) | (gr[i].w ^ tag);
      if (__all(bad == 0u) || __builtin_amdgcn_s_memrealtime() > tlim) break;
      __builtin_amdgcn_s_sleep(1);
      asm volatile("" ::: "memory");
    }
    float s0 = __uint_as_float(gr[0].x), s1 = __uint_as_float(gr[0].z);
#pragma unroll
    for (int i = 1; i < 8; ++i) { s0 += __uint_as_float(gr[i].x); s1 += __uint_as_float(gr[i].z); }
    s0 = rows_sum(s0); s1 = rows_sum(s1);
    if (lane < 10) {
      const unsigned o0 = bfbits(bflo(xres) + rbf(rbf(s0) * gt0)), o1 = bfbits(bfhi(xres) + rbf(rbf(s1) * gt1));
      ((unsigned*)out)[b * (D_ / 2) + 10 * h + lane] = o0 | (o1 << 16);
    }
    if (h == 0 && lane == 0) __hip_atomic_store(epw, tag, __ATOMIC_RELAXED, __HIP_MEMORY_SCOPE_AGENT);
  }
  ST(7);
}

extern "C" void kernel_launch(void* const* d_in, const int* in_sizes, int n_in, void* d_out, int out_size, void* d_ws,
                              size_t ws_size, hipStream_t stream) {
  const u16* x = (const u16*)d_in[0];
  const u16* kc = (const u16*)d_in[1];
  const u16* vc = (const u16*)d_in[2];
  const u16* wq = (const u16*)d_in[3];
  const u16* wo = (const u16*)d_in[4];
  const u16* w1 = (const u16*)d_in[6];
  const u16* b1 = (const u16*)d_in[7];
  const u16* w2 = (const u16*)d_in[8];
  const u16* b2 = (const u16*)d_in[9];
  attn_kernel<<<256, 512, 0, stream>>>(x, kc, vc, wq, wo, w1, b1, w2, b2, (unsigned char*)d_ws, (u16*)d_out);
}
```

```cpp
#include <hip/hip_runtime.h>
#include <hip/hip_bf16.h>
#include <stdint.h>

typedef unsigned v4u __attribute__((ext_vector_type(4)));
typedef unsigned v2u __attribute__((ext_vector_type(2)));
typedef float f32x4 __attribute__((ext_vector_type(4)));
typedef __bf16 bf16x8 __attribute__((ext_vector_type(8)));
typedef __bf16 bf16x2 __attribute__((ext_vector_type(2)));
typedef unsigned short u16;
typedef __attribute__((address_space(1))) unsigned gu32;
typedef __attribute__((address_space(1))) unsigned long long gu64;
constexpr size_t WS_EPOCH = 0, WS_GRAN = 65536;

constexpr int B_ = 8, D_ = 640, H_ = 32, SK = 4096, DH = 128, F_ = 1728;
#define RING 32
#define K_AUX 2
#define V_AUX 2
#define W_AUX 0
#define VTHR 256
#define WO_INFL 4
#define HPRIO 3

__device__ __forceinline__ float rbf(float x) { return (float)(__bf16)x; }
__device__ __forceinline__ unsigned bfbits(float x) { return (unsigned)__builtin_bit_cast(u16, (__bf16)x); }
__device__ __forceinline__ float bflo(unsigned u) { return __builtin_bit_cast(float, u << 16); }
__device__ __forceinline__ float bfhi(unsigned u) { return __builtin_bit_cast(float, u & 0xffff0000u); }

template <int CTRL> __device__ __forceinline__ float dppf(float x) {
  return __builtin_bit_cast(float, __builtin_amdgcn_mov_dpp(__builtin_bit_cast(int, x), CTRL, 0xf, 0xf, true));
}
template <int CTRL> __device__ __forceinline__ unsigned dppu(unsigned x) {
  return (unsigned)__builtin_amdgcn_mov_dpp((int)x, CTRL, 0xf, 0xf, true);
}
constexpr int XOR1 = 0xB1, XOR2 = 0x4E, XOR7 = 0x141, XOR8 = 0x128, NEWBCAST = 0x150;

__device__ __forceinline__ float wave_sum(float x) {
  x += dppf<XOR1>(x); x += dppf<XOR2>(x); x += dppf<XOR7>(x); x += dppf<XOR8>(x);
  auto s = __builtin_amdgcn_permlane16_swap(__float_as_uint(x), __float_as_uint(x), false, false);
  x = __uint_as_float(s[0]) + __uint_as_float(s[1]);
  auto t = __builtin_amdgcn_permlane32_swap(__float_as_uint(x), __float_as_uint(x), false, false);
  return __uint_as_float(t[0]) + __uint_as_float(t[1]);
}
__device__ __forceinline__ float rows_sum(float x) {
  auto s = __builtin_amdgcn_permlane16_swap(__float_as_uint(x), __float_as_uint(x), false, false);
  x = __uint_as_float(s[0]) + __uint_as_float(s[1]);
  auto t = __builtin_amdgcn_permlane32_swap(__float_as_uint(x), __float_as_uint(x), false, false);
  return __uint_as_float(t[0]) + __uint_as_float(t[1]);
}
__device__ __forceinline__ float wave_max(float x) {
  x = fmaxf(x, dppf<XOR1>(x)); x = fmaxf(x, dppf<XOR2>(x)); x = fmaxf(x, dppf<XOR7>(x)); x = fmaxf(x, dppf<XOR8>(x));
  auto s = __builtin_amdgcn_permlane16_swap(__float_as_uint(x), __float_as_uint(x), false, false);
  x = fmaxf(__uint_as_float(s[0]), __uint_as_float(s[1]));
  auto t = __builtin_amdgcn_permlane32_swap(__float_as_uint(x), __float_as_uint(x), false, false);
  return fmaxf(__uint_as_float(t[0]), __uint_as_float(t[1]));
}
__device__ __forceinline__ float dot2(unsigned a, unsigned b, float acc) {
  return __builtin_amdgcn_fdot2_f32_bf16(__builtin_bit_cast(bf16x2, a), __builtin_bit_cast(bf16x2, b), acc, false);
}
__device__ __forceinline__ f32x4 mfma16(v4u a, v4u b, f32x4 c) {
  return __builtin_amdgcn_mfma_f32_16x16x32_bf16(__builtin_bit_cast(bf16x8, a), __builtin_bit_cast(bf16x8, b), c, 0, 0, 0);
}
template <int J> __device__ __forceinline__ v4u onehot(unsigned pdup, const v4u& mask) {
  const unsigned pb = dppu<NEWBCAST + J>(pdup);
  v4u a;
  a.x = pb & mask.x; a.y = pb & mask.y; a.z = pb & mask.z; a.w = pb & mask.w;
  return a;
}
__device__ __forceinline__ void lds_barrier() {
  asm volatile("s_waitcnt lgkmcnt(0)" ::: "memory");
  __builtin_amdgcn_s_barrier();
  asm volatile("" ::: "memory");
}
__device__ __forceinline__ void ln_stats(const float (&xv)[10], float& mb, float& sd) {
  float s = 0.f;
#pragma unroll
  for (int i = 0; i < 10; ++i) s += xv[i];
  s = wave_sum(s);
  const float mean = s * (1.0f / D_);
  float q = 0.f;
#pragma unroll
  for (int i = 0; i < 10; ++i) { const float d = xv[i] - mean; q = fmaf(d, d, q); }
  q = wave_sum(q);
  const float var = rbf(q * (1.0f / D_));
  mb = rbf(mean);
  sd = rbf(sqrtf(rbf(var + 1e-5f)));
}
__device__ __forceinline__ float ln_apply(float x, float mb, float sd) { return rbf(rbf(x - mb) / sd); }

template <int I, int N, typename Fn> __device__ __forceinline__ void static_for(Fn&& f) {
  if constexpr (I < N) { f(std::integral_constant<int, I>{}); static_for<I + 1, N>(f); }
}
__device__ __forceinline__ float gelu_bf(float u) {
  constexpr float K1 = -2.0f * 0.7978845608028654f * 1.4426950408889634f;
  constexpr float K2 = K1 * 0.044715f;
  const float e = __builtin_amdgcn_exp2f(u * fmaf(K2, u * u, K1));
  return rbf(u * __builtin_amdgcn_rcpf(1.0f + e));
}

#define ST(i) do {} while (0)

constexpr int L_SM = 2560, L_GATE = 2576, L_OP = 2688, L_END = L_OP + 512;

__global__ void __launch_bounds__(512, 2)
attn_kernel(const u16* __restrict__ x, const u16* __restrict__ kc, const u16* __restrict__ vc,
            const u16* __restrict__ wq, const u16* __restrict__ wo, const u16* __restrict__ w1, const u16* __restrict__ b1,
            const u16* __restrict__ w2, const u16* __restrict__ b2, unsigned char* ws, u16* __restrict__ out) {
  __shared__ __attribute__((aligned(16))) float lds[L_END];
  const int bh = blockIdx.x, b = bh >> 5, h = bh & 31;
  const int tid = threadIdx.x, lane = tid & 63, w = __builtin_amdgcn_readfirstlane(tid >> 6);
  const int g = lane >> 4, c = lane & 15;
  ST(0);
  const unsigned tstart = (unsigned)__builtin_amdgcn_s_memrealtime();
  v4u mask;
  {
    const unsigned m = (c & 1) ? 0xffff0000u : 0x0000ffffu;
    mask.x = (c >> 1) == 0 ? m : 0u; mask.y = (c >> 1) == 1 ? m : 0u; mask.z = (c >> 1) == 2 ? m : 0u; mask.w = (c >> 1) == 3 ? m : 0u;
  }
  constexpr float L2E = 1.4426950408889634f;

  if (w < 4) {
    auto swave = [&](auto VAUX) __attribute__((always_inline)) {
    constexpr int vaux = VAUX;
    const __amdgpu_buffer_rsrc_t rK = __builtin_amdgcn_make_buffer_rsrc((void*)(kc + (size_t)bh * SK * DH), 0, SK * DH * 2, 0x00020000);
    const __amdgpu_buffer_rsrc_t rV = __builtin_amdgcn_make_buffer_rsrc((void*)(vc + (size_t)bh * SK * DH), 0, SK * DH * 2, 0x00020000);
    v4u ring[RING];
    const int koff = w * 1024 + lane * 16;
#pragma unroll
    for (int i = 0; i < RING; ++i) { ring[i] = __builtin_amdgcn_raw_buffer_load_b128(rK, koff, i * 4096, K_AUX); __builtin_amdgcn_sched_barrier(0); }
    ST(1);
    lds_barrier();
    ST(2);
    v4u qp;
    {
      f32x4 s0 = *(const f32x4*)&lds[8 * c], s1 = *(const f32x4*)&lds[8 * c + 4];
#pragma unroll
      for (int ww = 1; ww < 4; ++ww) {
        s0 += *(const f32x4*)&lds[ww * 128 + 8 * c];
        s1 += *(const f32x4*)&lds[ww * 128 + 8 * c + 4];
      }
      qp.x = bfbits(s0.x) | (bfbits(s0.y) << 16); qp.y = bfbits(s0.z) | (bfbits(s0.w) << 16);
      qp.z = bfbits(s1.x) | (bfbits(s1.y) << 16); qp.w = bfbits(s1.z) | (bfbits(s1.w) << 16);
    }
    constexpr int NIT = 256 / RING, GPI = RING / 16;
    float sc[16];
#pragma unroll
    for (int i = 0; i < 16; ++i) sc[i] = 0.f;
    auto kbody = [&](auto LAST, int sbase) {
      constexpr bool last = LAST;
      float nsc[GPI];
#pragma unroll
      for (int gi = 0; gi < GPI; ++gi) {
        float xx[16];
#pragma unroll
        for (int j = 0; j < 16; ++j) {
          const int i = gi * 16 + j;
          float d = dot2(ring[i].x, qp.x, 0.f);
          d = dot2(ring[i].y, qp.y, d); d = dot2(ring[i].z, qp.z, d); d = dot2(ring[i].w, qp.w, d);
          asm volatile("" : "+v"(d));
          xx[j] = d;
          if constexpr (last) ring[i] = __builtin_amdgcn_raw_buffer_load_b128(rV, koff, i * 4096, vaux);
          else ring[i] = __builtin_amdgcn_raw_buffer_load_b128(rK, koff, sbase + i * 4096, K_AUX);
          __builtin_amdgcn_sched_barrier(0);
        }
#define TR_STEP(HALF, CTRL, BIT) _Pragma("unroll") for (int hh = 0; hh < (HALF); ++hh) { \
          const float lo_ = xx[hh], hi_ = xx[hh + (HALF)]; const float t1_ = lo_ + dppf<CTRL>(lo_), t2_ = hi_ + dppf<CTRL>(hi_); \
          xx[hh] = (c & (BIT)) ? t2_ : t1_; }
        TR_STEP(8, XOR8, 8) TR_STEP(4, XOR7, 4) TR_STEP(2, XOR2, 2) TR_STEP(1, XOR1, 1)
#undef TR_STEP
        nsc[gi] = rbf(rbf(xx[0]) * 0.08838834764831845f);
        asm volatile("" : "+v"(nsc[gi]));
      }
#pragma unroll
      for (int i = 0; i < 16 - GPI; ++i) sc[i] = sc[i + GPI];
#pragma unroll
      for (int gi = 0; gi < GPI; ++gi) sc[16 - GPI + gi] = nsc[gi];
    };
#pragma unroll 1
    for (int it = 0; it < NIT - 1; ++it) kbody(std::false_type{}, (it + 1) * RING * 4096);
    kbody(std::true_type{}, 0);
    ST(3);
    float* sm = &lds[L_SM];
    {
      float m = sc[0];
#pragma unroll
      for (int i = 1; i < 16; ++i) m = fmaxf(m, sc[i]);
      m = wave_max(m);
      float l = 0.f;
#pragma unroll
      for (int i = 0; i < 16; ++i) l += __builtin_amdgcn_exp2f((sc[i] - m) * L2E);
      l = wave_sum(l);
      if (lane == 0) { sm[2 * w] = m; sm[2 * w + 1] = l; }
    }
    lds_barrier();
    unsigned pd[16];
    {
      const f32x4 t0 = *(const f32x4*)&sm[0], t1 = *(const f32x4*)&sm[4];
      const float m = fmaxf(fmaxf(t0.x, t0.z), fmaxf(t1.x, t1.z));
      float l = t0.y * __builtin_amdgcn_exp2f((t0.x - m) * L2E);
      l = fmaf(t0.w, __builtin_amdgcn_exp2f((t0.z - m) * L2E), l);
      l = fmaf(t1.y, __builtin_amdgcn_exp2f((t1.x - m) * L2E), l);
      l = fmaf(t1.w, __builtin_amdgcn_exp2f((t1.z - m) * L2E), l);
      const float inv = 1.0f / l;
#pragma unroll
      for (int i = 0; i < 16; ++i) { const unsigned p = bfbits(__builtin_amdgcn_exp2f((sc[i] - m) * L2E) * inv); pd[i] = p | (p << 16); }
    }
    ST(4);
    f32x4 oacc = {0.f, 0.f, 0.f, 0.f};
#pragma unroll 1
    for (int it = 0; it < NIT - 1; ++it) {
      const int sbase = (it + 1) * RING * 4096;
      static_for<0, RING>([&](auto I) {
        constexpr int i = I;
        const v4u a = onehot<(i & 15)>(pd[i >> 4], mask);
        oacc = mfma16(a, ring[i], oacc);
        asm volatile("" : "+v"(oacc));
        ring[i] = __builtin_amdgcn_raw_buffer_load_b128(rV, koff, sbase + i * 4096, vaux);
        __builtin_amdgcn_sched_barrier(0);
      });
#pragma unroll
      for (int i = 0; i < 16 - GPI; ++i) pd[i] = pd[i + GPI];
    }
    static_for<0, RING>([&](auto I) {
      constexpr int i = I;
      const v4u a = onehot<(i & 15)>(pd[i >> 4], mask);
      oacc = mfma16(a, ring[i], oacc);
      asm volatile("" : "+v"(oacc));
      __builtin_amdgcn_sched_barrier(0);
    });
    ST(5);
    if (lane < 32) *(f32x4*)&lds[L_OP + w * 128 + 8 * c + 4 * g] = oacc;
    lds_barrier();
    ST(6);
    };
    swave(std::integral_constant<int, V_AUX>{});
    return;
  }

  __builtin_amdgcn_s_setprio(HPRIO);
  const int hw = w - 4, ht = tid - 256;
  const __amdgpu_buffer_rsrc_t rQ = __builtin_amdgcn_make_buffer_rsrc((void*)(wq + h * DH), 0, D_ * H_ * DH * 2 - h * DH * 2, 0x00020000);
  const __amdgpu_buffer_rsrc_t rO = __builtin_amdgcn_make_buffer_rsrc((void*)(wo + (size_t)h * DH * D_), 0, DH * D_ * 2, 0x00020000);
  const unsigned* x32 = (const unsigned*)(x + b * D_);
  unsigned xr[5];
#pragma unroll
  for (int i = 0; i < 5; ++i) xr[i] = x32[lane + 64 * i];
  const u16* xb = x + b * D_;
  const unsigned xo0 = xb[160 * hw + 4 * c + g];
  const unsigned xo1 = xb[160 * hw + 64 + 4 * c + g];
  const unsigned xo2 = xb[160 * hw + 128 + 4 * (c & 7) + g];
  const unsigned xg = xb[20 * h + (lane < 20 ? lane : 0)];
  const int gf = ht < 216 ? ht : 0;
  const v4u gw1 = ((const v4u*)w1)[gf], gb1 = ((const v4u*)b1)[gf], gw2 = ((const v4u*)w2)[gf];
  const unsigned b2v = b2[0];
  gu32* epw = (gu32*)(ws + WS_EPOCH) + b * 64;
  const unsigned tag = __hip_atomic_load(epw, __ATOMIC_RELAXED, __HIP_MEMORY_SCOPE_AGENT) + 0x9E3779B9u;
  const unsigned xres = x32[10 * h + (c < 10 ? c : 0)];
  __builtin_amdgcn_sched_barrier(0);
  v4u wr[40];
  const int qoff = (160 * hw + g) * (H_ * DH * 2) + c * 16;
#pragma unroll
  for (int t = 0; t < 40; ++t) { wr[t] = __builtin_amdgcn_raw_buffer_load_b128(rQ, qoff, t * 4 * (H_ * DH * 2), W_AUX); __builtin_amdgcn_sched_barrier(0); }
  ST(1);
  float xv[10];
#pragma unroll
  for (int i = 0; i < 5; ++i) { xv[2 * i] = bflo(xr[i]); xv[2 * i + 1] = bfhi(xr[i]); }
  float mb, sd;
  ln_stats(xv, mb, sd);
  unsigned xn0 = bfbits(ln_apply(bflo(xo0), mb, sd)), xn1 = bfbits(ln_apply(bflo(xo1), mb, sd)), xn2 = bfbits(ln_apply(bflo(xo2), mb, sd));
  xn0 |= xn0 << 16; xn1 |= xn1 << 16; xn2 |= xn2 << 16;
  {
    f32x4 acc = {0.f, 0.f, 0.f, 0.f};
    static_for<0, 40>([&](auto T) {
      constexpr int t = T;
      const v4u a = onehot<(t & 15)>(t < 16 ? xn0 : t < 32 ? xn1 : xn2, mask);
      acc = mfma16(a, wr[t], acc);
    });
    if (lane < 32) *(f32x4*)&lds[hw * 128 + 8 * c + 4 * g] = acc;
  }
  ST(2);
  lds_barrier();
  __builtin_amdgcn_s_setprio(0);
  {
    float w1v[8], b1v[8], w2v[8];
    {
      const bool ok = ht < 216;
#pragma unroll
      for (int i = 0; i < 4; ++i) {
        w1v[2 * i] = bflo(gw1[i]); w1v[2 * i + 1] = bfhi(gw1[i]);
        b1v[2 * i] = bflo(gb1[i]); b1v[2 * i + 1] = bfhi(gb1[i]);
        w2v[2 * i] = ok ? bflo(gw2[i]) : 0.f; w2v[2 * i + 1] = ok ? bfhi(gw2[i]) : 0.f;
      }
    }
#pragma unroll
    for (int i = 0; i < 10; ++i) xv[i] = ln_apply(xv[i], mb, sd);
    float mb2, sd2;
    ln_stats(xv, mb2, sd2);
    const float hcol = ln_apply(ln_apply(bflo(xg), mb, sd), mb2, sd2);
    float ycol = 0.f;
#pragma unroll 1
    for (int j = 0; j < 20; ++j) {
      const float hv = __builtin_bit_cast(float, __builtin_amdgcn_readlane(__builtin_bit_cast(int, hcol), j));
      float y = 0.f;
#pragma unroll
      for (int i = 0; i < 8; ++i) y = fmaf(gelu_bf(rbf(rbf(hv * w1v[i]) + b1v[i])), w2v[i], y);
      y = wave_sum(y);
      ycol = (lane == j) ? y : ycol;
    }
    if (lane < 20) lds[L_GATE + hw * 20 + lane] = ycol;
  }
  ST(3);
  __builtin_amdgcn_s_setprio(HPRIO);
  const int ooff = (32 * hw + g) * (D_ * 2) + c * 16;
  static_for<0, 40>([&](auto I) {
    constexpr int i = I;
    wr[i] = __builtin_amdgcn_raw_buffer_load_b128(rO, ooff, (i / 5) * 4 * (D_ * 2) + (i % 5) * 256, W_AUX);
    if constexpr (i >= WO_INFL - 1) asm volatile("s_waitcnt vmcnt(%0)" :: "i"(WO_INFL - 1) : "memory");
    __builtin_amdgcn_sched_barrier(0);
  });
  ST(4);
  lds_barrier();
  float gt0, gt1;
  {
    const int cc = c < 10 ? 2 * c : 0;
    float y0 = (lds[L_GATE + cc] + lds[L_GATE + 20 + cc]) + (lds[L_GATE + 40 + cc] + lds[L_GATE + 60 + cc]);
    float y1 = (lds[L_GATE + cc + 1] + lds[L_GATE + 21 + cc]) + (lds[L_GATE + 41 + cc] + lds[L_GATE + 61 + cc]);
    y0 = rbf(rbf(y0) + bflo(b2v)); y1 = rbf(rbf(y1) + bflo(b2v));
    gt0 = rbf(y0 / (1.0f + __expf(-y0))); gt1 = rbf(y1 / (1.0f + __expf(-y1)));
  }
  int poff[3];
#pragma unroll
  for (int r = 0; r < 3; ++r) { const int col = ht + 256 * r, cons = col / 20; poff[r] = (cons * 32 + (col - cons * 20)) * 8; }
  int goff = g * 8192 + h * 256 + (c < 11 ? c : 0) * 16;
  asm volatile("" : "+v"(gt0), "+v"(gt1), "+v"(poff[0]), "+v"(poff[1]), "+v"(poff[2]), "+v"(goff));
  lds_barrier();
  ST(5);
  unsigned od;
  {
    const int k = 32 * hw + 4 * (c & 7) + g;
    const float s = (lds[L_OP + k] + lds[L_OP + 128 + k]) + (lds[L_OP + 256 + k] + lds[L_OP + 384 + k]);
    od = bfbits(s); od |= od << 16;
  }
  f32x4 sacc[5];
#pragma unroll
  for (int u = 0; u < 5; ++u) sacc[u] = f32x4{0.f, 0.f, 0.f, 0.f};
  static_for<0, 8>([&](auto T) {
    constexpr int t = T;
    const v4u a = onehot<t>(od, mask);
#pragma unroll
    for (int u = 0; u < 5; ++u) sacc[u] = mfma16(a, wr[t * 5 + u], sacc[u]);
  });
  if (lane < 32) {
#pragma unroll
    for (int u = 0; u < 5; ++u) *(f32x4*)&lds[hw * 640 + u * 128 + 8 * c + 4 * g] = sacc[u];
  }
  lds_barrier();
  {
    const __amdgpu_buffer_rsrc_t rP = __builtin_amdgcn_make_buffer_rsrc((void*)(ws + WS_GRAN + (size_t)bh * 8192), 0, 8192, 0x00020000);
    float sv[3];
#pragma unroll
    for (int r = 0; r < 3; ++r) { const int col = ht + 256 * r; sv[r] = (lds[col] + lds[640 + col]) + (lds[1280 + col] + lds[1920 + col]); }
#pragma unroll
    for (int r = 0; r < 3; ++r) { v2u gv; gv.x = __float_as_uint(sv[r]); gv.y = tag; __builtin_amdgcn_raw_buffer_store_b64(gv, rP, poff[r], 0, 16); }
    if (ht < 64) { v2u gv; gv.x = tstart; gv.y = tag; __builtin_amdgcn_raw_buffer_store_b64(gv, rP, ((ht >> 1) * 32 + 20 + (ht & 1)) * 8, 0, 16); }
  }
  ST(6);
  if (hw != 0) return;
  {
    const __amdgpu_buffer_rsrc_t rG = __builtin_amdgcn_make_buffer_rsrc((void*)(ws + WS_GRAN + (size_t)b * 32 * 8192), 0, 32 * 8192, 0x00020000);
    v4u gr[8];
    const unsigned long long tlim = __builtin_amdgcn_s_memrealtime() + 2000000ull;
    for (;;) {
      asm volatile("" : "+v"(goff) :: "memory");
#pragma unroll
      for (int i = 0; i < 8; ++i) gr[i] = __builtin_amdgcn_raw_buffer_load_b128(rG, goff, i * 4 * 8192, 16);
      unsigned bad = 0;
#pragma unroll
      for (int i = 0; i < 8; ++i) bad |= (gr[i].y ^ tag) | (gr[i].w ^ tag);
      if (__all(bad == 0u) || __builtin_amdgcn_s_memrealtime() > tlim) break;
      __builtin_amdgcn_s_sleep(1);
      asm volatile("" ::: "memory");
    }
    float s0 = __uint_as_float(gr[0].x), s1 = __uint_as_float(gr[0].z);
#pragma unroll
    for (int i = 1; i < 8; ++i) { s0 += __uint_as_float(gr[i].x); s1 += __uint_as_float(gr[i].z); }
    s0 = rows_sum(s0); s1 = rows_sum(s1);
    if (lane < 10) {
      const unsigned o0 = bfbits(bflo(xres) + rbf(rbf(s0) * gt0)), o1 = bfbits(bfhi(xres) + rbf(rbf(s1) * gt1));
      ((unsigned*)out)[b * (D_ / 2) + 10 * h + lane] = o0 | (o1 << 16);
    }
    if (h == 0 && lane == 0) __hip_atomic_store(epw, tag, __ATOMIC_RELAXED, __HIP_MEMORY_SCOPE_AGENT);
    int rel = 0;
#pragma unroll
    for (int i = 0; i < 8; ++i) rel = min(rel, (int)(gr[i].x - tstart));
    int r0 = __builtin_amdgcn_readlane(rel, 10), r1 = __builtin_amdgcn_readlane(rel, 26), r2 = __builtin_amdgcn_readlane(rel, 42), r3 = __builtin_amdgcn_readlane(rel, 58);
    const int rm = min(min(r0, r1), min(r2, r3));
    while ((int)((unsigned)__builtin_amdgcn_s_memrealtime() - tstart) - rm < 15000) __builtin_amdgcn_s_sleep(1);
  }
  ST(7);
}

extern "C" void kernel_launch(void* const* d_in, const int* in_sizes, int n_in, void* d_out, int out_size, void* d_ws,
                              size_t ws_size, hipStream_t stream) {
  const u16* x = (const u16*)d_in[0];
  const u16* kc = (const u16*)d_in[1];
  const u16* vc = (const u16*)d_in[2];
  const u16* wq = (const u16*)d_in[3];
  const u16* wo = (const u16*)d_in[4];
  const u16* w1 = (const u16*)d_in[6];
  const u16* b1 = (const u16*)d_in[7];
  const u16* w2 = (const u16*)d_in[8];
  const u16* b2 = (const u16*)d_in[9];
  attn_kernel<<<256, 512, 0, stream>>>(x, kc, vc, wq, wo, w1, b1, w2, b2, (unsigned char*)d_ws, (u16*)d_out);
}
```

```cpp
#include <hip/hip_runtime.h>
#include <hip/hip_bf16.h>
#include <stdint.h>
#include <type_traits>

typedef unsigned v4u __attribute__((ext_vector_type(4)));
typedef unsigned v2u __attribute__((ext_vector_type(2)));
typedef float f32x2 __attribute__((ext_vector_type(2)));
typedef float f32x4 __attribute__((ext_vector_type(4)));
typedef __bf16 bf16x8 __attribute__((ext_vector_type(8)));
typedef __bf16 bf16x2 __attribute__((ext_vector_type(2)));
typedef unsigned short u16;
typedef __attribute__((address_space(1))) unsigned gu32;

constexpr int D_ = 640, H_ = 32, SK = 4096, DH = 128;
constexpr size_t WS_EPB = 0, WS_EPH = 4096, WS_QG = 2u << 20, WS_SG = 4u << 20;
constexpr unsigned TAG_STEP = 0x9E3779B9u;

#define TLO 5
#define THI 6
#define INFL 5
#define TAU 137
#define TOFF 270
#define QMODE 0
#define LAGK 8
#define VFREE 0
#define WQ_INFL 40
#define WO_INFL 2
#define QSLEEP 12
#define QSLEEP0 24
#define FSLEEP 2
#define FSLEEPM 8
#define BURST 8
#define PRETOUCH 1
#define HPRIO0 3
#define SPRIO 1
#define HPRIO 3
constexpr int NT = 2, SC1 = 16;

__device__ __forceinline__ float rbf(float x) { return (float)(__bf16)x; }
__device__ __forceinline__ unsigned bfbits(float x) { return (unsigned)__builtin_bit_cast(u16, (__bf16)x); }
__device__ __forceinline__ float bflo(unsigned u) { return __builtin_bit_cast(float, u << 16); }
__device__ __forceinline__ float bfhi(unsigned u) { return __builtin_bit_cast(float, u & 0xffff0000u); }

template <int CTRL> __device__ __forceinline__ float dppf(float x) {
  return __builtin_bit_cast(float, __builtin_amdgcn_mov_dpp(__builtin_bit_cast(int, x), CTRL, 0xf, 0xf, true));
}
template <int CTRL> __device__ __forceinline__ unsigned dppu(unsigned x) {
  return (unsigned)__builtin_amdgcn_mov_dpp((int)x, CTRL, 0xf, 0xf, true);
}
constexpr int XOR1 = 0xB1, XOR2 = 0x4E, XOR7 = 0x141, XOR8 = 0x128, NEWBCAST = 0x150;

__device__ __forceinline__ float swap16_sum(float x) {
  auto s = __builtin_amdgcn_permlane16_swap(__float_as_uint(x), __float_as_uint(x), false, false);
  return __uint_as_float(s[0]) + __uint_as_float(s[1]);
}
__device__ __forceinline__ float swap32_sum(float x) {
  auto t = __builtin_amdgcn_permlane32_swap(__float_as_uint(x), __float_as_uint(x), false, false);
  return __uint_as_float(t[0]) + __uint_as_float(t[1]);
}
__device__ __forceinline__ float half_sum(float x) {
  x += dppf<XOR1>(x); x += dppf<XOR2>(x); x += dppf<XOR7>(x); x += dppf<XOR8>(x);
  return swap16_sum(x);
}
__device__ __forceinline__ float wave_sum(float x) { return swap32_sum(half_sum(x)); }
__device__ __forceinline__ float wave_max(float x) {
  x = fmaxf(x, dppf<XOR1>(x)); x = fmaxf(x, dppf<XOR2>(x)); x = fmaxf(x, dppf<XOR7>(x)); x = fmaxf(x, dppf<XOR8>(x));
  auto s = __builtin_amdgcn_permlane16_swap(__float_as_uint(x), __float_as_uint(x), false, false);
  x = fmaxf(__uint_as_float(s[0]), __uint_as_float(s[1]));
  auto t = __builtin_amdgcn_permlane32_swap(__float_as_uint(x), __float_as_uint(x), false, false);
  return fmaxf(__uint_as_float(t[0]), __uint_as_float(t[1]));
}
__device__ __forceinline__ float rdlane(float x, int l) { return __builtin_bit_cast(float, __builtin_amdgcn_readlane(__builtin_bit_cast(int, x), l)); }
__device__ __forceinline__ float dot2(unsigned a, unsigned b, float acc) {
  return __builtin_amdgcn_fdot2_f32_bf16(__builtin_bit_cast(bf16x2, a), __builtin_bit_cast(bf16x2, b), acc, false);
}
__device__ __forceinline__ f32x4 mfma16(v4u a, v4u b, f32x4 c) {
  return __builtin_amdgcn_mfma_f32_16x16x32_bf16(__builtin_bit_cast(bf16x8, a), __builtin_bit_cast(bf16x8, b), c, 0, 0, 0);
}
template <int J> __device__ __forceinline__ v4u onehot(unsigned pdup, const v4u& mask) {
  const unsigned pb = dppu<NEWBCAST + J>(pdup);
  v4u a;
  a.x = pb & mask.x; a.y = pb & mask.y; a.z = pb & mask.z; a.w = pb & mask.w;
  return a;
}
__device__ __forceinline__ void lds_barrier() {
  asm volatile("s_waitcnt lgkmcnt(0)" ::: "memory");
  __builtin_amdgcn_s_barrier();
  asm volatile("" ::: "memory");
}
template <int N> __device__ __forceinline__ void wait_vm() { asm volatile("s_waitcnt vmcnt(%0)" ::"i"(N) : "memory"); }
__device__ __forceinline__ void wait_var(int ahead) {
  asm volatile("s_cmp_eq_u32 %0, 0\n\ts_cbranch_scc1 .Lskip%=\n\ts_waitcnt vmcnt(%1)\n.Lskip%=:\n\ts_waitcnt vmcnt(%2)" ::"s"(ahead), "i"(TLO - 1), "i"(THI - 1) : "memory", "scc");
}
__device__ __forceinline__ int sched_ahead(unsigned t0, int n) {
  const int el = (int)((unsigned)__builtin_amdgcn_s_memrealtime() - t0);
  return __builtin_amdgcn_readfirstlane(el < TOFF + ((n * (TAU * 256 / 10)) >> 8) ? 1 : 0);
}
__device__ __forceinline__ void ln_finish(float mean, float q, float& mb, float& sd) {
  const float var = rbf(q * (1.0f / D_));
  mb = rbf(mean);
  sd = rbf(sqrtf(rbf(var + 1e-5f)));
}
__device__ __forceinline__ void ln_stats(const float (&xv)[10], float& mb, float& sd) {
  float s = 0.f;
#pragma unroll
  for (int i = 0; i < 10; ++i) s += xv[i];
  const float mean = wave_sum(s) * (1.0f / D_);
  float q = 0.f;
#pragma unroll
  for (int i = 0; i < 10; ++i) { const float d = xv[i] - mean; q = fmaf(d, d, q); }
  ln_finish(mean, wave_sum(q), mb, sd);
}
__device__ __forceinline__ float ln_apply(float x, float mb, float sd) { return rbf(rbf(x - mb) / sd); }

template <int I, int N, typename Fn> __device__ __forceinline__ void static_for(Fn&& f) {
  if constexpr (I < N) { f(std::integral_constant<int, I>{}); static_for<I + 1, N>(f); }
}
__device__ __forceinline__ float gelu_bf(float u) {
  constexpr float K1 = -2.0f * 0.7978845608028654f * 1.4426950408889634f;
  constexpr float K2 = K1 * 0.044715f;
  const float e = __builtin_amdgcn_exp2f(u * fmaf(K2, u * u, K1));
  return rbf(u * __builtin_amdgcn_rcpf(1.0f + e));
}
template <int STRIDE, int SLEEP_FEW, int SLEEP_MANY>
__device__ __forceinline__ void gather8(__amdgpu_buffer_rsrc_t rs, int off, unsigned tag, float (&v0)[8], float (&v1)[8]) {
  const unsigned long long tlim = __builtin_amdgcn_s_memrealtime() + 2000000ull;
  int o[8];
#pragma unroll
  for (int i = 0; i < 8; ++i) { o[i] = off; v0[i] = 0.f; v1[i] = 0.f; }
  for (;;) {
    v4u gr[8];
#pragma unroll
    for (int i = 0; i < 8; ++i) { asm volatile("" : "+v"(o[i])::"memory"); }
#pragma unroll
    for (int i = 0; i < 8; ++i) gr[i] = __builtin_amdgcn_raw_buffer_load_b128(rs, o[i], i * STRIDE, SC1);
    int missing = 0;
#pragma unroll
    for (int i = 0; i < 8; ++i) {
      const bool got = o[i] >= 0 && gr[i].y == tag && gr[i].w == tag;
      v0[i] = got ? __uint_as_float(gr[i].x) : v0[i];
      v1[i] = got ? __uint_as_float(gr[i].z) : v1[i];
      o[i] = got ? (int)0x80000000 : o[i];
      missing += __builtin_popcountll(__builtin_amdgcn_ballot_w64(o[i] >= 0));
    }
    if (missing == 0 || __builtin_amdgcn_s_memrealtime() > tlim) break;
    if (missing > 128) __builtin_amdgcn_s_sleep(SLEEP_MANY);
    else __builtin_amdgcn_s_sleep(SLEEP_FEW);
  }
}
constexpr int brev4(int j) { return ((j & 1) << 3) | ((j & 2) << 1) | ((j & 4) >> 1) | ((j & 8) >> 3); }

#define ST(i) do {} while (0)

constexpr int L_S = 0;
constexpr int L_SM = 2560;
constexpr int L_GATE = 2576;
constexpr int L_Q = 2688;
constexpr int L_OP = 2752;
constexpr int L_SC = 3264;
constexpr int L_END = L_SC + 4096;

__global__ void __launch_bounds__(512, 2)
fused_kernel(const u16* __restrict__ x, const u16* __restrict__ kc, const u16* __restrict__ vc,
             const u16* __restrict__ wq, const u16* __restrict__ wo, const u16* __restrict__ w1, const u16* __restrict__ b1,
             const u16* __restrict__ w2, const u16* __restrict__ b2, unsigned char* ws, u16* out) {
  __shared__ __attribute__((aligned(16))) float lds[L_END];
  const int bh = blockIdx.x, b = bh >> 5, h = bh & 31;
  const int tid = threadIdx.x, lane = tid & 63, w = __builtin_amdgcn_readfirstlane(tid >> 6);
  const int g = lane >> 4, c = lane & 15;
  constexpr float L2E = 1.4426950408889634f;

  asm volatile("" ::"s"(x), "s"(wq), "s"(ws), "s"(kc));
  const __amdgpu_buffer_rsrc_t rK = __builtin_amdgcn_make_buffer_rsrc((void*)(kc + (size_t)bh * SK * DH), 0, SK * DH * 2, 0x00020000);
  const int koff = (w < 4 ? 0 : (int)0x80000000) + (tid & 255) * 16;
  v4u land[32];
  static_for<0, BURST>([&](auto N) {
    constexpr int n = N;
    land[n] = __builtin_amdgcn_raw_buffer_load_b128(rK, koff, n * 4096, NT);
    __builtin_amdgcn_sched_barrier(0);
  });

  if (w < 4) {
    __builtin_amdgcn_s_setprio(SPRIO);
    const __amdgpu_buffer_rsrc_t rV = __builtin_amdgcn_make_buffer_rsrc((void*)(vc + (size_t)bh * SK * DH), 0, SK * DH * 2, 0x00020000);
    const unsigned t0 = (unsigned)__builtin_amdgcn_s_memrealtime();
    ST(0);
    v4u ring[16];
    static_for<BURST, 32>([&](auto N) {
      constexpr int n = N;
      if constexpr (n >= BURST) wait_vm<INFL - 1>();
      land[n] = __builtin_amdgcn_raw_buffer_load_b128(rK, koff, n * 4096, NT);
      __builtin_amdgcn_sched_barrier(0);
      if constexpr (n == BURST) { ST(7); }
    });
    ST(1);
    lds_barrier();
    ST(2);
    const v4u qp = *(const v4u*)&lds[L_Q + 4 * c];
    float* scw = &lds[L_SC + w * 1024 + lane];
    float mrun = 0.f, lrun = 0.f;
    float r1a, r2a, r3a, r4a;
    auto kstep = [&](auto J, const v4u& kv, int grp, bool first) __attribute__((always_inline)) {
      constexpr int j = J;
      float d = dot2(kv.x, qp.x, 0.f);
      d = dot2(kv.y, qp.y, d); d = dot2(kv.z, qp.z, d); d = dot2(kv.w, qp.w, d);
      d += dppf<XOR8>(d);
      if constexpr (!(j & 1)) { r1a = d; asm volatile("" : "+v"(r1a)); return; }
      d = (c & 8) ? d : r1a;
      d += dppf<XOR7>(d);
      if constexpr (!(j & 2)) { r2a = d; asm volatile("" : "+v"(r2a)); return; }
      d = (c & 4) ? d : r2a;
      d += dppf<XOR2>(d);
      if constexpr (!(j & 4)) { r3a = d; asm volatile("" : "+v"(r3a)); return; }
      d = (c & 2) ? d : r3a;
      d += dppf<XOR1>(d);
      if constexpr (!(j & 8)) { r4a = d; asm volatile("" : "+v"(r4a)); return; }
      d = (c & 1) ? d : r4a;
      const float s = rbf(rbf(d) * 0.08838834764831845f);
      scw[grp * 64] = s;
      if (first) { mrun = s; lrun = 1.0f; }
      else {
        const float mn = fmaxf(mrun, s);
        lrun = fmaf(lrun, __builtin_amdgcn_exp2f((mrun - mn) * L2E), __builtin_amdgcn_exp2f((s - mn) * L2E));
        mrun = mn;
      }
      asm volatile("" : "+v"(mrun), "+v"(lrun));
    };
    static_for<0, 48>([&](auto M) {
      constexpr int m = M;
      if constexpr (m < 32) kstep(std::integral_constant<int, (m & 15)>{}, land[m], m >> 4, m < 16);
      else kstep(std::integral_constant<int, (m & 15)>{}, ring[m & 15], m >> 4, false);
      constexpr int n = 32 + (m >> 1);
      if constexpr ((m & 1) && n < 48 + LAGK) {
        wait_vm<INFL - 1>();
        ring[n & 15] = __builtin_amdgcn_raw_buffer_load_b128(rK, koff, n * 4096, NT);
      }
      __builtin_amdgcn_sched_barrier(0);
    });
#pragma unroll 1
    for (int k = 3; k < 15; ++k) {
      const int sbase = (16 * k + LAGK) * 4096;
      const int ahead = sched_ahead(t0, 16 * k + LAGK);
      static_for<0, 16>([&](auto I) {
        constexpr int i = I;
        wait_var(ahead);
        ring[(i + LAGK) & 15] = __builtin_amdgcn_raw_buffer_load_b128(rK, koff, sbase + i * 4096, NT);
        kstep(I, ring[i], k, false);
        __builtin_amdgcn_sched_barrier(0);
      });
    }
    const int ahead15 = sched_ahead(t0, 240 + LAGK);
    static_for<0, 16>([&](auto I) {
      constexpr int i = I, n = 240 + LAGK + i;
      wait_var(ahead15);
      if constexpr (n < 256) ring[n & 15] = __builtin_amdgcn_raw_buffer_load_b128(rK, koff, n * 4096, NT);
      else ring[n & 15] = __builtin_amdgcn_raw_buffer_load_b128(rV, koff, (n - 256) * 4096, NT);
      kstep(I, ring[i], 15, false);
      __builtin_amdgcn_sched_barrier(0);
    });
    static_for<LAGK, 15>([&](auto N) {
      constexpr int n = N;
      ring[n] = __builtin_amdgcn_raw_buffer_load_b128(rV, koff, n * 4096, NT);
      __builtin_amdgcn_sched_barrier(0);
    });
    ST(3);
    {
      const float m = wave_max(mrun);
      const float l = wave_sum(lrun * __builtin_amdgcn_exp2f((mrun - m) * L2E));
      if (lane == 0) { lds[L_SM + 2 * w] = m; lds[L_SM + 2 * w + 1] = l; }
    }
    float snext = scw[0];
    v4u mask;
    {
      const unsigned mm = (c & 1) ? 0xffff0000u : 0x0000ffffu;
      mask.x = (c >> 1) == 0 ? mm : 0u; mask.y = (c >> 1) == 1 ? mm : 0u; mask.z = (c >> 1) == 2 ? mm : 0u; mask.w = (c >> 1) == 3 ? mm : 0u;
    }
    lds_barrier();
    float mx, inv;
    {
      const f32x4 t0 = *(const f32x4*)&lds[L_SM], t1 = *(const f32x4*)&lds[L_SM + 4];
      mx = fmaxf(fmaxf(t0.x, t0.z), fmaxf(t1.x, t1.z));
      float l = t0.y * __builtin_amdgcn_exp2f((t0.x - mx) * L2E);
      l = fmaf(t0.w, __builtin_amdgcn_exp2f((t0.z - mx) * L2E), l);
      l = fmaf(t1.y, __builtin_amdgcn_exp2f((t1.x - mx) * L2E), l);
      l = fmaf(t1.w, __builtin_amdgcn_exp2f((t1.z - mx) * L2E), l);
      inv = 1.0f / l;
    }
    ST(4);
    f32x4 oacc = {0.f, 0.f, 0.f, 0.f};
    auto vbody = [&](auto MODE, int k) __attribute__((always_inline)) {
      constexpr int mode = MODE;
      const int sbase = (16 * k + 15) * 4096;
      const int ahead = mode == 0 ? sched_ahead(t0, 256 + 16 * k + 15) : 0;
      unsigned pd = bfbits(__builtin_amdgcn_exp2f((snext - mx) * L2E) * inv);
      pd |= pd << 16;
      if constexpr (mode != 2) snext = scw[(k + 1) * 64];
      static_for<0, 16>([&](auto I) {
        constexpr int i = I;
        if constexpr (mode != 2 || i == 0) {
          if constexpr (mode == 0) wait_var(ahead);
          ring[(i + 15) & 15] = __builtin_amdgcn_raw_buffer_load_b128(rV, koff, sbase + i * 4096, NT);
        }
        const v4u a = onehot<brev4(i)>(pd, mask);
        oacc = mfma16(a, ring[i], oacc);
        asm volatile("" : "+v"(oacc));
        __builtin_amdgcn_sched_barrier(0);
      });
    };
#pragma unroll 1
    for (int k = 0; k < 15 - VFREE; ++k) vbody(std::integral_constant<int, 0>{}, k);
    if constexpr (VFREE > 0) {
#pragma unroll 1
      for (int k = 15 - VFREE; k < 15; ++k) vbody(std::integral_constant<int, 1>{}, k);
    }
    vbody(std::integral_constant<int, 2>{}, 15);
    ST(5);
    if (lane < 32) *(f32x4*)&lds[L_OP + w * 128 + 8 * c + 4 * g] = oacc;
    lds_barrier();
    ST(6);
    return;
  }

  ST(0);
  __builtin_amdgcn_s_setprio(HPRIO0);
  const int hw = w - 4, ht = tid - 256;
  gu32* epb = (gu32*)(ws + WS_EPB) + b * 64;
  gu32* eph = (gu32*)(ws + WS_EPH) + h * 64;
  {
    const unsigned* xp = (const unsigned*)(x + (2 * hw + (lane >> 5)) * D_);
    unsigned xr[10];
    {
      typedef unsigned v4ua __attribute__((ext_vector_type(4), aligned(8)));
      typedef unsigned v2ua __attribute__((ext_vector_type(2), aligned(8)));
      const unsigned* xl = xp + (lane & 31) * 10;
      const v4ua a0 = *(const v4ua*)xl, a1 = *(const v4ua*)(xl + 4);
      const v2ua a2 = *(const v2ua*)(xl + 8);
      xr[0] = a0.x; xr[1] = a0.y; xr[2] = a0.z; xr[3] = a0.w; xr[4] = a1.x; xr[5] = a1.y; xr[6] = a1.z; xr[7] = a1.w; xr[8] = a2.x; xr[9] = a2.y;
    }
    const u16* xa = x + 2 * hw * D_ + 80 * b + g;
    const unsigned xa0 = xa[4 * c], xb0 = xa[D_ + 4 * c], xa1 = xa[64 + 4 * (c & 3)], xb1 = xa[D_ + 64 + 4 * (c & 3)];
    const unsigned tagq = __hip_atomic_load(eph, __ATOMIC_RELAXED, __HIP_MEMORY_SCOPE_AGENT) + TAG_STEP;
    const unsigned tq = __hip_atomic_load((gu32*)(ws + WS_QG + (size_t)h * 65536 + (size_t)b * 9216), __ATOMIC_RELAXED, __HIP_MEMORY_SCOPE_AGENT);
    __builtin_amdgcn_sched_barrier(0);
    const __amdgpu_buffer_rsrc_t rQ = __builtin_amdgcn_make_buffer_rsrc((void*)(wq + (size_t)80 * b * H_ * DH + h * DH), 0, 80 * H_ * DH * 2 - h * DH * 2, 0x00020000);
    v4u wr[20];
    const int qoff = g * (H_ * DH * 2) + c * 16;
#pragma unroll
    for (int t = 0; t < 20; ++t) { wr[t] = __builtin_amdgcn_raw_buffer_load_b128(rQ, qoff, t * 4 * (H_ * DH * 2), 0); __builtin_amdgcn_sched_barrier(0); }
#pragma unroll
    for (int n = 0; n < BURST; ++n) asm volatile("" ::"v"(land[n]));
    float s = 0.f;
#pragma unroll
    for (int i = 0; i < 10; ++i) s += bflo(xr[i]) + bfhi(xr[i]);
    const float mean = half_sum(s) * (1.0f / D_);
    float q = 0.f;
#pragma unroll
    for (int i = 0; i < 10; ++i) { const float d0 = bflo(xr[i]) - mean, d1 = bfhi(xr[i]) - mean; q = fmaf(d0, d0, q); q = fmaf(d1, d1, q); }
    float mb, sd;
    ln_finish(mean, half_sum(q), mb, sd);
    const float mbA = rdlane(mb, 0), sdA = rdlane(sd, 0), mbB = rdlane(mb, 32), sdB = rdlane(sd, 32);
    const unsigned xn0 = bfbits(ln_apply(bflo(xa0), mbA, sdA)) | (bfbits(ln_apply(bflo(xb0), mbB, sdB)) << 16);
    const unsigned xn1 = bfbits(ln_apply(bflo(xa1), mbA, sdA)) | (bfbits(ln_apply(bflo(xb1), mbB, sdB)) << 16);
    v4u sel;
    {
      const unsigned src = (c & 8) ? 0x0302u : 0x0100u;
      const unsigned sv = (c & 1) ? ((src << 16) | 0x0c0cu) : (0x0c0c0000u | src);
      const int r = (c & 7) >> 1;
      sel.x = r == 0 ? sv : 0x0c0c0c0cu; sel.y = r == 1 ? sv : 0x0c0c0c0cu; sel.z = r == 2 ? sv : 0x0c0c0c0cu; sel.w = r == 3 ? sv : 0x0c0c0c0cu;
    }
    f32x4 acc = {0.f, 0.f, 0.f, 0.f};
    static_for<0, 20>([&](auto T) {
      constexpr int t = T;
      const unsigned pb = dppu<NEWBCAST + (t & 15)>(t < 16 ? xn0 : xn1);
      v4u a;
      a.x = __builtin_amdgcn_perm(0u, pb, sel.x); a.y = __builtin_amdgcn_perm(0u, pb, sel.y);
      a.z = __builtin_amdgcn_perm(0u, pb, sel.z); a.w = __builtin_amdgcn_perm(0u, pb, sel.w);
      acc = mfma16(a, wr[t], acc);
    });
    asm volatile("" ::"v"(tq));
    const __amdgpu_buffer_rsrc_t rG = __builtin_amdgcn_make_buffer_rsrc((void*)(ws + WS_QG + (size_t)h * 65536), 0, 65536, 0x00020000);
    const int po = ((2 * hw + (g >> 1)) * 8 + b) * 1024 + (lane & 31) * 16;
#pragma unroll
    for (int r = 0; r < 2; ++r) {
      v4u gv; gv.x = __float_as_uint(acc[2 * r]); gv.y = tagq; gv.z = __float_as_uint(acc[2 * r + 1]); gv.w = tagq;
      __builtin_amdgcn_raw_buffer_store_b128(gv, rG, po + 512 * r, 0, SC1);
    }
    ST(1);
    if (hw == 0) {
      float v0[8], v1[8];
      __builtin_amdgcn_s_sleep(QSLEEP0);
      gather8<1024, QSLEEP, QSLEEP>(rG, b * 8192 + lane * 16, tagq, v0, v1);
      float q0 = v0[0], q1 = v1[0];
#pragma unroll
      for (int i = 1; i < 8; ++i) { q0 += v0[i]; q1 += v1[i]; }
      ((unsigned*)&lds[L_Q])[4 * c + 2 * (g & 1) + (g >> 1)] = bfbits(q0) | (bfbits(q1) << 16);
      if (b == 0 && lane == 0) __hip_atomic_store(eph, tagq, __ATOMIC_RELAXED, __HIP_MEMORY_SCOPE_AGENT);
    }
  }
  const unsigned* x32 = (const unsigned*)(x + b * D_);
  unsigned xr[5];
#pragma unroll
  for (int i = 0; i < 5; ++i) xr[i] = x32[lane + 64 * i];
  const unsigned xg = x[b * D_ + 20 * h + (lane < 20 ? lane : 0)];
  const int gf = ht < 216 ? ht : 0;
  const v4u gw1 = ((const v4u*)w1)[gf], gb1 = ((const v4u*)b1)[gf], gw2 = ((const v4u*)w2)[gf];
  const unsigned b2v = b2[0];
  const unsigned tag = __hip_atomic_load(epb, __ATOMIC_RELAXED, __HIP_MEMORY_SCOPE_AGENT) + TAG_STEP;
  const unsigned xres = x32[10 * h + (c < 10 ? c : 0)];
  if (hw == 1) {
    const unsigned char* tp = lane == 0 ? (const unsigned char*)(vc + (size_t)bh * SK * DH)
                            : lane == 1 ? (const unsigned char*)(vc + (size_t)(bh + 1) * SK * DH) - 128
                            : lane == 2 ? (const unsigned char*)(kc + (size_t)(bh + 1) * SK * DH) - 128
                            : lane == 3 ? (const unsigned char*)out
                            : lane == 4 ? ws + WS_SG + (size_t)b * 32 * 5120
                            : ws + WS_SG + (size_t)(b + 1) * 32 * 5120 - 16;
    const unsigned tv = __hip_atomic_load((gu32*)tp, __ATOMIC_RELAXED, __HIP_MEMORY_SCOPE_AGENT);
    asm volatile("" ::"v"(tv));
  }
  __builtin_amdgcn_sched_barrier(0);
  ST(2);
  lds_barrier();
  __builtin_amdgcn_s_setprio(0);
  {
    float xv[10];
#pragma unroll
    for (int i = 0; i < 5; ++i) { xv[2 * i] = bflo(xr[i]); xv[2 * i + 1] = bfhi(xr[i]); }
    float mb, sd, mb2, sd2;
    ln_stats(xv, mb, sd);
#pragma unroll
    for (int i = 0; i < 10; ++i) xv[i] = ln_apply(xv[i], mb, sd);
    ln_stats(xv, mb2, sd2);
    const float hcol = ln_apply(ln_apply(bflo(xg), mb, sd), mb2, sd2);
    float w1v[8], b1v[8], w2v[8];
    const bool ok = ht < 216;
#pragma unroll
    for (int i = 0; i < 4; ++i) {
      w1v[2 * i] = bflo(gw1[i]); w1v[2 * i + 1] = bfhi(gw1[i]);
      b1v[2 * i] = bflo(gb1[i]); b1v[2 * i + 1] = bfhi(gb1[i]);
      w2v[2 * i] = ok ? bflo(gw2[i]) : 0.f; w2v[2 * i + 1] = ok ? bfhi(gw2[i]) : 0.f;
    }
    float ycol = 0.f;
#pragma unroll 1
    for (int j = 0; j < 20; ++j) {
      const float hv = rdlane(hcol, j);
      float y = 0.f;
#pragma unroll
      for (int i = 0; i < 8; ++i) y = fmaf(gelu_bf(rbf(rbf(hv * w1v[i]) + b1v[i])), w2v[i], y);
      y = wave_sum(y);
      ycol = (lane == j) ? y : ycol;
    }
    if (lane < 20) lds[L_GATE + hw * 20 + lane] = ycol;
  }
  ST(3);
  __builtin_amdgcn_s_setprio(HPRIO);
  const __amdgpu_buffer_rsrc_t rO = __builtin_amdgcn_make_buffer_rsrc((void*)(wo + (size_t)h * DH * D_), 0, DH * D_ * 2, 0x00020000);
  const int ooff = (32 * hw + g) * (D_ * 2) + c * 16;
  v4u wr[40];
  static_for<0, 40>([&](auto I) {
    constexpr int i = I;
    if constexpr (i >= WO_INFL) wait_vm<WO_INFL - 1>();
    wr[i] = __builtin_amdgcn_raw_buffer_load_b128(rO, ooff, (i / 5) * 4 * (D_ * 2) + (i % 5) * 256, 0);
    __builtin_amdgcn_sched_barrier(0);
  });
  ST(4);
  lds_barrier();
  float gt0, gt1;
  {
    const int cc = c < 10 ? 2 * c : 0;
    float y0 = (lds[L_GATE + cc] + lds[L_GATE + 20 + cc]) + (lds[L_GATE + 40 + cc] + lds[L_GATE + 60 + cc]);
    float y1 = (lds[L_GATE + cc + 1] + lds[L_GATE + 21 + cc]) + (lds[L_GATE + 41 + cc] + lds[L_GATE + 61 + cc]);
    y0 = rbf(rbf(y0) + bflo(b2v)); y1 = rbf(rbf(y1) + bflo(b2v));
    gt0 = rbf(y0 / (1.0f + __expf(-y0))); gt1 = rbf(y1 / (1.0f + __expf(-y1)));
  }
  v4u mask;
  {
    const unsigned mm = (c & 1) ? 0xffff0000u : 0x0000ffffu;
    mask.x = (c >> 1) == 0 ? mm : 0u; mask.y = (c >> 1) == 1 ? mm : 0u; mask.z = (c >> 1) == 2 ? mm : 0u; mask.w = (c >> 1) == 3 ? mm : 0u;
  }
  int goff = c < 10 ? g * 5120 + h * 160 + c * 16 : (int)0x80000000;
  asm volatile("" : "+v"(gt0), "+v"(gt1), "+v"(goff), "+v"(mask));
  wait_vm<0>();
  lds_barrier();
  ST(5);
  unsigned od;
  {
    const int k = 32 * hw + 4 * (c & 7) + g;
    const float s = (lds[L_OP + k] + lds[L_OP + 128 + k]) + (lds[L_OP + 256 + k] + lds[L_OP + 384 + k]);
    od = bfbits(s); od |= od << 16;
  }
  f32x4 sacc[5];
#pragma unroll
  for (int u = 0; u < 5; ++u) sacc[u] = f32x4{0.f, 0.f, 0.f, 0.f};
  static_for<0, 8>([&](auto T) {
    constexpr int t = T;
    const v4u a = onehot<t>(od, mask);
#pragma unroll
    for (int u = 0; u < 5; ++u) sacc[u] = mfma16(a, wr[t * 5 + u], sacc[u]);
  });
  if (lane < 32) {
#pragma unroll
    for (int u = 0; u < 5; ++u) *(f32x4*)&lds[L_S + hw * 640 + u * 128 + 8 * c + 4 * g] = sacc[u];
  }
  lds_barrier();
  {
    const __amdgpu_buffer_rsrc_t rP = __builtin_amdgcn_make_buffer_rsrc((void*)(ws + WS_SG + (size_t)bh * 5120), 0, 5120, 0x00020000);
    auto pub = [&](int t) __attribute__((always_inline)) {
      const f32x2 sv = (*(const f32x2*)&lds[L_S + 2 * t] + *(const f32x2*)&lds[L_S + 640 + 2 * t]) + (*(const f32x2*)&lds[L_S + 1280 + 2 * t] + *(const f32x2*)&lds[L_S + 1920 + 2 * t]);
      v4u gv; gv.x = __float_as_uint(sv.x); gv.y = tag; gv.z = __float_as_uint(sv.y); gv.w = tag;
      __builtin_amdgcn_raw_buffer_store_b128(gv, rP, t * 16, 0, SC1);
    };
    pub(ht);
    if (hw == 1) pub(256 + lane);
  }
  ST(6);
  if (hw != 0) return;
  {
    const __amdgpu_buffer_rsrc_t rG = __builtin_amdgcn_make_buffer_rsrc((void*)(ws + WS_SG + (size_t)b * 32 * 5120), 0, 32 * 5120, 0x00020000);
    float v0[8], v1[8];
    gather8<4 * 5120, FSLEEP, FSLEEPM>(rG, goff, tag, v0, v1);
    float s0 = v0[0], s1 = v1[0];
#pragma unroll
    for (int i = 1; i < 8; ++i) { s0 += v0[i]; s1 += v1[i]; }
    s0 = swap32_sum(swap16_sum(s0)); s1 = swap32_sum(swap16_sum(s1));
    if (lane < 10) {
      const unsigned o0 = bfbits(bflo(xres) + rbf(rbf(s0) * gt0)), o1 = bfbits(bfhi(xres) + rbf(rbf(s1) * gt1));
      __hip_atomic_store((gu32*)out + b * (D_ / 2) + 10 * h + lane, o0 | (o1 << 16), __ATOMIC_RELAXED, __HIP_MEMORY_SCOPE_AGENT);
    }
    if (h == 0 && lane == 0) __hip_atomic_store(epb, tag, __ATOMIC_RELAXED, __HIP_MEMORY_SCOPE_AGENT);
  }
  ST(7);
}

extern "C" void kernel_launch(void* const* d_in, const int* in_sizes, int n_in, void* d_out, int out_size, void* d_ws,
                              size_t ws_size, hipStream_t stream) {
  fused_kernel<<<256, 512, 0, stream>>>((const u16*)d_in[0], (const u16*)d_in[1], (const u16*)d_in[2], (const u16*)d_in[3],
                                        (const u16*)d_in[4], (const u16*)d_in[6], (const u16*)d_in[7], (const u16*)d_in[8],
                                        (const u16*)d_in[9], (unsigned char*)d_ws, (u16*)d_out);
}
```

```cpp
#include <hip/hip_runtime.h>
#include <hip/hip_bf16.h>
#include <stdint.h>
#include <type_traits>

typedef unsigned v4u __attribute__((ext_vector_type(4)));
typedef unsigned v2u __attribute__((ext_vector_type(2)));
typedef float f32x2 __attribute__((ext_vector_type(2)));
typedef float f32x4 __attribute__((ext_vector_type(4)));
typedef __bf16 bf16x8 __attribute__((ext_vector_type(8)));
typedef __bf16 bf16x2 __attribute__((ext_vector_type(2)));
typedef unsigned short u16;
typedef __attribute__((address_space(1))) unsigned gu32;

constexpr int D_ = 640, H_ = 32, SK = 4096, DH = 128;
constexpr size_t WS_SG = 0, WS_EPB = 256 * 5120;
constexpr unsigned TAG_STEP = 0x9E3779B9u;

#define TLO 5
#define THI 6
#define INFL 5
#define TAU 137
#define TOFF 190
#define NL 2
#define WQ_INFL 3
#define WO_INFL 2
#define FSLEEP 1
#define FS0 24
#define PREW 0
#define WOEARLY 0
#define BURST 5
#define HPRIO 3
#define SPRIO 1
constexpr int NX = 2;
constexpr int NT = 2, SC1 = 16, OOB = (int)0x80000000;

__device__ __forceinline__ float rbf(float x) { return (float)(__bf16)x; }
__device__ __forceinline__ unsigned bfbits(float x) { return (unsigned)__builtin_bit_cast(u16, (__bf16)x); }
__device__ __forceinline__ float bflo(unsigned u) { return __builtin_bit_cast(float, u << 16); }
__device__ __forceinline__ float bfhi(unsigned u) { return __builtin_bit_cast(float, u & 0xffff0000u); }

template <int CTRL> __device__ __forceinline__ float dppf(float x) {
  return __builtin_bit_cast(float, __builtin_amdgcn_mov_dpp(__builtin_bit_cast(int, x), CTRL, 0xf, 0xf, true));
}
template <int CTRL> __device__ __forceinline__ unsigned dppu(unsigned x) {
  return (unsigned)__builtin_amdgcn_mov_dpp((int)x, CTRL, 0xf, 0xf, true);
}
constexpr int XOR1 = 0xB1, XOR2 = 0x4E, XOR7 = 0x141, XOR8 = 0x128, NEWBCAST = 0x150;

__device__ __forceinline__ float swap16_sum(float x) {
  auto s = __builtin_amdgcn_permlane16_swap(__float_as_uint(x), __float_as_uint(x), false, false);
  return __uint_as_float(s[0]) + __uint_as_float(s[1]);
}
__device__ __forceinline__ float swap32_sum(float x) {
  auto t = __builtin_amdgcn_permlane32_swap(__float_as_uint(x), __float_as_uint(x), false, false);
  return __uint_as_float(t[0]) + __uint_as_float(t[1]);
}
__device__ __forceinline__ float wave_sum(float x) {
  x += dppf<XOR1>(x); x += dppf<XOR2>(x); x += dppf<XOR7>(x); x += dppf<XOR8>(x);
  return swap32_sum(swap16_sum(x));
}
__device__ __forceinline__ float wave_max(float x) {
  x = fmaxf(x, dppf<XOR1>(x)); x = fmaxf(x, dppf<XOR2>(x)); x = fmaxf(x, dppf<XOR7>(x)); x = fmaxf(x, dppf<XOR8>(x));
  auto s = __builtin_amdgcn_permlane16_swap(__float_as_uint(x), __float_as_uint(x), false, false);
  x = fmaxf(__uint_as_float(s[0]), __uint_as_float(s[1]));
  auto t = __builtin_amdgcn_permlane32_swap(__float_as_uint(x), __float_as_uint(x), false, false);
  return fmaxf(__uint_as_float(t[0]), __uint_as_float(t[1]));
}
__device__ __forceinline__ float rdlane(float x, int l) { return __builtin_bit_cast(float, __builtin_amdgcn_readlane(__builtin_bit_cast(int, x), l)); }
__device__ __forceinline__ float dot2(unsigned a, unsigned b, float acc) {
  return __builtin_amdgcn_fdot2_f32_bf16(__builtin_bit_cast(bf16x2, a), __builtin_bit_cast(bf16x2, b), acc, false);
}
__device__ __forceinline__ f32x4 mfma16(v4u a, v4u b, f32x4 c) {
  return __builtin_amdgcn_mfma_f32_16x16x32_bf16(__builtin_bit_cast(bf16x8, a), __builtin_bit_cast(bf16x8, b), c, 0, 0, 0);
}
template <int J> __device__ __forceinline__ v4u onehot(unsigned pdup, const v4u& mask) {
  const unsigned pb = dppu<NEWBCAST + J>(pdup);
  v4u a;
  a.x = pb & mask.x; a.y = pb & mask.y; a.z = pb & mask.z; a.w = pb & mask.w;
  return a;
}
__device__ __forceinline__ v4u onehot_mask(int c) {
  const unsigned mm = (c & 1) ? 0xffff0000u : 0x0000ffffu;
  v4u mask;
  mask.x = (c >> 1) == 0 ? mm : 0u; mask.y = (c >> 1) == 1 ? mm : 0u; mask.z = (c >> 1) == 2 ? mm : 0u; mask.w = (c >> 1) == 3 ? mm : 0u;
  return mask;
}
__device__ __forceinline__ void lds_barrier() {
  asm volatile("s_waitcnt lgkmcnt(0)" ::: "memory");
  __builtin_amdgcn_s_barrier();
  asm volatile("" ::: "memory");
}
template <int N> __device__ __forceinline__ void wait_vm() { asm volatile("s_waitcnt vmcnt(%0)" ::"i"(N) : "memory"); }
__device__ __forceinline__ void wait_var(int state) {
  asm volatile("s_cmp_eq_u32 %0, 1\n\ts_cbranch_scc0 .Lwa%=\n\ts_waitcnt vmcnt(%1)\n.Lwa%=:\n\ts_cmp_eq_u32 %0, 2\n\ts_cbranch_scc1 .Lwb%=\n\ts_waitcnt vmcnt(%2)\n.Lwb%=:"
               ::"s"(state), "i"(TLO - 1), "i"(THI - 1) : "memory", "scc");
}
__device__ __forceinline__ int sched_ahead(unsigned t0, int n) {
  const int el = (int)((unsigned)__builtin_amdgcn_s_memrealtime() - t0);
  return __builtin_amdgcn_readfirstlane(el < TOFF + ((n * (TAU * 256 / 10)) >> 8) ? 1 : 0);
}
__device__ __forceinline__ void ln_stats(const float (&xv)[10], float& mb, float& sd) {
  float s = 0.f;
#pragma unroll
  for (int i = 0; i < 10; ++i) s += xv[i];
  const float mean = wave_sum(s) * (1.0f / D_);
  float q = 0.f;
#pragma unroll
  for (int i = 0; i < 10; ++i) { const float d = xv[i] - mean; q = fmaf(d, d, q); }
  const float var = rbf(wave_sum(q) * (1.0f / D_));
  mb = rbf(mean);
  sd = rbf(sqrtf(rbf(var + 1e-5f)));
}
__device__ __forceinline__ float ln_apply(float x, float mb, float sd) { return rbf(rbf(x - mb) / sd); }

template <int I, int N, typename Fn> __device__ __forceinline__ void static_for(Fn&& f) {
  if constexpr (I < N) { f(std::integral_constant<int, I>{}); static_for<I + 1, N>(f); }
}
__device__ __forceinline__ float gelu_bf(float u) {
  constexpr float K1 = -2.0f * 0.7978845608028654f * 1.4426950408889634f;
  constexpr float K2 = K1 * 0.044715f;
  const float e = __builtin_amdgcn_exp2f(u * fmaf(K2, u * u, K1));
  return rbf(u * __builtin_amdgcn_rcpf(1.0f + e));
}
constexpr int brev4(int j) { return ((j & 1) << 3) | ((j & 2) << 1) | ((j & 4) >> 1) | ((j & 8) >> 3); }

#define PF(k) do {} while (0)

constexpr int L_S = 0;
constexpr int L_SM = 2560;
constexpr int L_TAG = 2568;
constexpr int L_GATE = 2576;
constexpr int L_OP = 2704;
constexpr int L_SC = 3216;
constexpr int L_K = L_SC + 4096;
constexpr int L_END = L_K + 4 * 16 * NL * 256;

__global__ void __launch_bounds__(512, 2)
fused_kernel(const u16* __restrict__ x, const u16* __restrict__ kc, const u16* __restrict__ vc,
             const u16* __restrict__ wq, const u16* __restrict__ wo, const u16* __restrict__ w1, const u16* __restrict__ b1,
             const u16* __restrict__ w2, const u16* __restrict__ b2, unsigned char* ws, u16* out) {
  __shared__ __attribute__((aligned(16))) float lds[L_END];
  const int bh = blockIdx.x, b = bh >> 5, h = bh & 31;
  const int tid = threadIdx.x, lane = tid & 63, w = __builtin_amdgcn_readfirstlane(tid >> 6);
  const int g = lane >> 4, c = lane & 15;
  constexpr float L2E = 1.4426950408889634f;

  const __amdgpu_buffer_rsrc_t rP = __builtin_amdgcn_make_buffer_rsrc((void*)(ws + WS_SG + (size_t)bh * 5120), 0, 5120, 0x00020000);
  auto publish = [&](int t, unsigned tag) __attribute__((always_inline)) {
    const f32x2 sv = (*(const f32x2*)&lds[L_S + 2 * t] + *(const f32x2*)&lds[L_S + 640 + 2 * t]) + (*(const f32x2*)&lds[L_S + 1280 + 2 * t] + *(const f32x2*)&lds[L_S + 1920 + 2 * t]);
    v4u gv; gv.x = __float_as_uint(sv.x); gv.y = tag; gv.z = __float_as_uint(sv.y); gv.w = tag;
    __builtin_amdgcn_raw_buffer_store_b128(gv, rP, t * 16, 0, SC1);
  };

  if (w >= 4) {
    __builtin_amdgcn_s_setprio(HPRIO);
    const int hw = w - 4, ht = tid - 256;
    gu32* epb = (gu32*)(ws + WS_EPB) + b * 64;
    const unsigned* x32 = (const unsigned*)(x + b * D_);
    const u16* xb = x + b * D_;
    unsigned xr[5];
#pragma unroll
    for (int i = 0; i < 5; ++i) xr[i] = x32[lane + 64 * i];
    const unsigned xo0 = xb[160 * hw + 4 * c + g], xo1 = xb[160 * hw + 64 + 4 * c + g], xo2 = xb[160 * hw + 128 + 4 * (c & 7) + g];
    __builtin_amdgcn_sched_barrier(0);
    const __amdgpu_buffer_rsrc_t rQ = __builtin_amdgcn_make_buffer_rsrc((void*)(wq + h * DH), 0, D_ * H_ * DH * 2 - h * DH * 2, 0x00020000);
    const int qoff = (160 * hw + g) * (H_ * DH * 2) + c * 16;
    v4u wr[40];
    static_for<0, WQ_INFL>([&](auto T) {
      constexpr int t = T;
      wr[t] = __builtin_amdgcn_raw_buffer_load_b128(rQ, qoff, t * 4 * (H_ * DH * 2), 0);
      __builtin_amdgcn_sched_barrier(0);
    });
    v4u mask = onehot_mask(c);
    float xv[10];
#pragma unroll
    for (int i = 0; i < 5; ++i) { xv[2 * i] = bflo(xr[i]); xv[2 * i + 1] = bfhi(xr[i]); }
    float mb, sd;
    ln_stats(xv, mb, sd);
    asm volatile("" : "+v"(mb), "+v"(sd));
    static_for<WQ_INFL, 2 * WQ_INFL>([&](auto T) {
      constexpr int t = T;
      wait_vm<WQ_INFL - 1>();
      wr[t] = __builtin_amdgcn_raw_buffer_load_b128(rQ, qoff, t * 4 * (H_ * DH * 2), 0);
      __builtin_amdgcn_sched_barrier(0);
    });
    unsigned xn0 = bfbits(ln_apply(bflo(xo0), mb, sd)), xn1 = bfbits(ln_apply(bflo(xo1), mb, sd)), xn2 = bfbits(ln_apply(bflo(xo2), mb, sd));
    xn0 |= xn0 << 16; xn1 |= xn1 << 16; xn2 |= xn2 << 16;
    {
      f32x4 acc = {0.f, 0.f, 0.f, 0.f};
      static_for<0, 40>([&](auto T) {
        constexpr int t = T, n = t + 2 * WQ_INFL;
        if constexpr (n < 40) {
          wait_vm<WQ_INFL - 1>();
          wr[n] = __builtin_amdgcn_raw_buffer_load_b128(rQ, qoff, n * 4 * (H_ * DH * 2), 0);
        }
        const v4u a = onehot<(t & 15)>(t < 16 ? xn0 : t < 32 ? xn1 : xn2, mask);
        acc = mfma16(a, wr[t], acc);
        asm volatile("" : "+v"(acc));
        __builtin_amdgcn_sched_barrier(0);
      });
      if (lane < 32) *(f32x4*)&lds[L_S + hw * 128 + 8 * c + 4 * g] = acc;
    }
    const int ncol = h < 8 ? 32 : 16;
    const unsigned xg = xb[16 * h + (lane & 15) + ((lane & 16) && h < 8 ? 512 : 0)];
    const int gf = ht < 216 ? ht : 0;
    const v4u gw1 = ((const v4u*)w1)[gf], gb1 = ((const v4u*)b1)[gf], gw2 = ((const v4u*)w2)[gf];
    const unsigned b2v = b2[0];
    const unsigned tag = __hip_atomic_load(epb, __ATOMIC_RELAXED, __HIP_MEMORY_SCOPE_AGENT) + TAG_STEP;
    const int cgrp = (hw == 1 && h < 8) ? 32 + h : h;
    const unsigned xres = x32[8 * cgrp + (lane & 7)];
    __builtin_amdgcn_sched_barrier(0);
    PF(1);
    lds_barrier();
    __builtin_amdgcn_s_setprio(0);
    {
      float mb2, sd2;
#pragma unroll
      for (int i = 0; i < 10; ++i) xv[i] = ln_apply(xv[i], mb, sd);
      ln_stats(xv, mb2, sd2);
      const float hcol = ln_apply(ln_apply(bflo(xg), mb, sd), mb2, sd2);
      float w1v[8], b1v[8], w2v[8];
      const bool ok = ht < 216;
#pragma unroll
      for (int i = 0; i < 4; ++i) {
        w1v[2 * i] = bflo(gw1[i]); w1v[2 * i + 1] = bfhi(gw1[i]);
        b1v[2 * i] = bflo(gb1[i]); b1v[2 * i + 1] = bfhi(gb1[i]);
        w2v[2 * i] = ok ? bflo(gw2[i]) : 0.f; w2v[2 * i + 1] = ok ? bfhi(gw2[i]) : 0.f;
      }
      float ycol = 0.f;
#pragma unroll 1
      for (int j = 0; j < ncol; ++j) {
        const float hv = rdlane(hcol, j);
        float y = 0.f;
#pragma unroll
        for (int i = 0; i < 8; ++i) y = fmaf(gelu_bf(rbf(rbf(hv * w1v[i]) + b1v[i])), w2v[i], y);
        y = wave_sum(y);
        ycol = (lane == j) ? y : ycol;
      }
      if (lane < 32) lds[L_GATE + hw * 32 + lane] = ycol;
      if (ht == 0) ((unsigned*)lds)[L_TAG] = tag;
    }
    if (hw == 1) {
      const unsigned char* tp = lane == 0 ? (const unsigned char*)(vc + (size_t)bh * SK * DH)
                              : lane == 1 ? (const unsigned char*)(vc + (size_t)(bh + 1) * SK * DH) - 128
                              : lane == 2 ? (const unsigned char*)out
                              : lane == 3 ? ws + WS_SG + (size_t)b * 32 * 5120
                              : ws + WS_SG + (size_t)(b + 1) * 32 * 5120 - 16;
      const unsigned tv = __hip_atomic_load((gu32*)tp, __ATOMIC_RELAXED, __HIP_MEMORY_SCOPE_AGENT);
      asm volatile("" ::"v"(tv));
    }
    PF(2);
    lds_barrier();
    __builtin_amdgcn_s_setprio(HPRIO);
    const __amdgpu_buffer_rsrc_t rO = __builtin_amdgcn_make_buffer_rsrc((void*)(wo + (size_t)h * DH * D_), 0, DH * D_ * 2, 0x00020000);
    const int ooff = (32 * hw + g) * (D_ * 2) + c * 16;
    static_for<0, 40>([&](auto I) {
      constexpr int i = I;
      if constexpr (i >= WO_INFL) wait_vm<WO_INFL - 1>();
      wr[i] = __builtin_amdgcn_raw_buffer_load_b128(rO, ooff, (i / 5) * 4 * (D_ * 2) + (i % 5) * 256, 0);
      __builtin_amdgcn_sched_barrier(0);
    });
    float gt0, gt1;
    {
      const int cc = (cgrp >= 32 ? 16 : 0) + 2 * (lane & 7);
      float y0 = (lds[L_GATE + cc] + lds[L_GATE + 32 + cc]) + (lds[L_GATE + 64 + cc] + lds[L_GATE + 96 + cc]);
      float y1 = (lds[L_GATE + cc + 1] + lds[L_GATE + 33 + cc]) + (lds[L_GATE + 65 + cc] + lds[L_GATE + 97 + cc]);
      y0 = rbf(rbf(y0) + bflo(b2v)); y1 = rbf(rbf(y1) + bflo(b2v));
      gt0 = rbf(y0 / (1.0f + __expf(-y0))); gt1 = rbf(y1 / (1.0f + __expf(-y1)));
    }
    int goff = (lane >> 3) * 5120 + cgrp * 128 + (lane & 7) * 16;
    float xr0 = bflo(xres), xr1 = bfhi(xres);
    asm volatile("" : "+v"(gt0), "+v"(gt1), "+v"(goff), "+v"(mask), "+v"(xr0), "+v"(xr1));
    const __amdgpu_buffer_rsrc_t rG = __builtin_amdgcn_make_buffer_rsrc((void*)(ws + WS_SG + (size_t)b * 32 * 5120), 0, 32 * 5120, 0x00020000);
    const __amdgpu_buffer_rsrc_t rOut = __builtin_amdgcn_make_buffer_rsrc((void*)(out + b * D_ + 16 * cgrp), 0, 32, 0x00020000);
    asm volatile("" ::"s"(rG), "s"(rOut), "s"(rP));
    __builtin_amdgcn_s_waitcnt(0x0F70);
    PF(3);
    lds_barrier();
    PF(4);
    unsigned od;
    {
      const int k = 32 * hw + 4 * (c & 7) + g;
      const float s = (lds[L_OP + k] + lds[L_OP + 128 + k]) + (lds[L_OP + 256 + k] + lds[L_OP + 384 + k]);
      od = bfbits(s); od |= od << 16;
    }
    f32x4 sacc[5];
#pragma unroll
    for (int u = 0; u < 5; ++u) sacc[u] = f32x4{0.f, 0.f, 0.f, 0.f};
    static_for<0, 8>([&](auto T) {
      constexpr int t = T;
      const v4u a = onehot<t>(od, mask);
#pragma unroll
      for (int u = 0; u < 5; ++u) sacc[u] = mfma16(a, wr[t * 5 + u], sacc[u]);
    });
    if (lane < 32) {
#pragma unroll
      for (int u = 0; u < 5; ++u) *(f32x4*)&lds[L_S + hw * 640 + u * 128 + 8 * c + 4 * g] = sacc[u];
    }
    lds_barrier();
    publish(ht, tag);
    PF(5);
    if (hw == 0 || cgrp >= 32) {
      v4u gr[4];
      if (FS0) __builtin_amdgcn_s_sleep(FS0);
#pragma unroll 1
      for (int spin = 0; spin < (1 << 17); ++spin) {
        asm volatile("" : "+v"(goff)::"memory");
#pragma unroll
        for (int i = 0; i < 4; ++i) gr[i] = __builtin_amdgcn_raw_buffer_load_b128(rG, goff, i * 8 * 5120, SC1);
        unsigned bad = 0;
#pragma unroll
        for (int i = 0; i < 4; ++i) bad |= (gr[i].y ^ tag) | (gr[i].w ^ tag);
        asm volatile("" : "+v"(bad));
        if (__all(bad == 0u)) break;
        if (FSLEEP) __builtin_amdgcn_s_sleep(FSLEEP);
      }
      float s0 = (__uint_as_float(gr[0].x) + __uint_as_float(gr[1].x)) + (__uint_as_float(gr[2].x) + __uint_as_float(gr[3].x));
      float s1 = (__uint_as_float(gr[0].z) + __uint_as_float(gr[1].z)) + (__uint_as_float(gr[2].z) + __uint_as_float(gr[3].z));
      s0 += dppf<XOR8>(s0); s1 += dppf<XOR8>(s1);
      s0 = swap32_sum(swap16_sum(s0)); s1 = swap32_sum(swap16_sum(s1));
      const unsigned o0 = bfbits(xr0 + rbf(rbf(s0) * gt0)), o1 = bfbits(xr1 + rbf(rbf(s1) * gt1));
      __builtin_amdgcn_raw_buffer_store_b32(o0 | (o1 << 16), rOut, lane * 4, 0, SC1);
      if (cgrp == 0 && lane == 0) __hip_atomic_store(epb, tag, __ATOMIC_RELAXED, __HIP_MEMORY_SCOPE_AGENT);
    }
    return;
  }

  {
    __builtin_amdgcn_s_setprio(SPRIO);
    const __amdgpu_buffer_rsrc_t rK = __builtin_amdgcn_make_buffer_rsrc((void*)(kc + (size_t)bh * SK * DH), 0, SK * DH * 2, 0x00020000);
    const __amdgpu_buffer_rsrc_t rV = __builtin_amdgcn_make_buffer_rsrc((void*)(vc + (size_t)bh * SK * DH), 0, SK * DH * 2, 0x00020000);
    const int koff = tid * 16;
    v4u* lk = (v4u*)&lds[L_K] + w * (16 * NL * 64) + lane;
    unsigned t0;
    v4u stg[8], land[16 * NX], ring[16];
    static_for<0, 16 * (NL + NX + 1)>([&](auto N) {
      constexpr int n = N;
      if constexpr (n >= BURST) wait_vm<INFL - 1>();
      const v4u kv = __builtin_amdgcn_raw_buffer_load_b128(rK, koff, n * 4096, NT);
      if constexpr (n < 16 * NL) stg[n & 7] = kv;
      else if constexpr (n < 16 * (NL + NX)) land[n - 16 * NL] = kv;
      else ring[n & 15] = kv;
      if constexpr (n >= 6 && n - 6 < 16 * NL) lk[(n - 6) * 64] = stg[(n - 6) & 7];
      if constexpr (n == BURST - 1) {
        t0 = (unsigned)__builtin_amdgcn_s_memrealtime();
      }
      if constexpr ((n & 15) == 15) { PF(1 + (n >> 4)); }
      __builtin_amdgcn_sched_barrier(0);
    });
    lds_barrier();
    v4u qp;
    {
      f32x4 s0 = *(const f32x4*)&lds[L_S + 8 * c], s1 = *(const f32x4*)&lds[L_S + 8 * c + 4];
#pragma unroll
      for (int ww = 1; ww < 4; ++ww) { s0 += *(const f32x4*)&lds[L_S + ww * 128 + 8 * c]; s1 += *(const f32x4*)&lds[L_S + ww * 128 + 8 * c + 4]; }
      qp.x = bfbits(s0.x) | (bfbits(s0.y) << 16); qp.y = bfbits(s0.z) | (bfbits(s0.w) << 16);
      qp.z = bfbits(s1.x) | (bfbits(s1.y) << 16); qp.w = bfbits(s1.z) | (bfbits(s1.w) << 16);
    }
    float* scw = &lds[L_SC + w * 1024 + lane];
    float mrun = 0.f, lrun = 0.f;
    float ra[4], rb[4];
    auto kstep = [&](auto J, const v4u& kv, float (&r)[4], int grp, bool first) __attribute__((always_inline)) {
      constexpr int j = J;
      float d = dot2(kv.x, qp.x, 0.f);
      d = dot2(kv.y, qp.y, d); d = dot2(kv.z, qp.z, d); d = dot2(kv.w, qp.w, d);
      d += dppf<XOR8>(d);
      if constexpr (!(j & 1)) { r[0] = d; asm volatile("" : "+v"(r[0])); return; }
      d = (c & 8) ? d : r[0];
      d += dppf<XOR7>(d);
      if constexpr (!(j & 2)) { r[1] = d; asm volatile("" : "+v"(r[1])); return; }
      d = (c & 4) ? d : r[1];
      d += dppf<XOR2>(d);
      if constexpr (!(j & 4)) { r[2] = d; asm volatile("" : "+v"(r[2])); return; }
      d = (c & 2) ? d : r[2];
      d += dppf<XOR1>(d);
      if constexpr (!(j & 8)) { r[3] = d; asm volatile("" : "+v"(r[3])); return; }
      d = (c & 1) ? d : r[3];
      const float s = rbf(rbf(d) * 0.08838834764831845f);
      scw[grp * 64] = s;
      if (first) { mrun = s; lrun = 1.0f; }
      else {
        const float mn = fmaxf(mrun, s);
        lrun = fmaf(lrun, __builtin_amdgcn_exp2f((mrun - mn) * L2E), __builtin_amdgcn_exp2f((s - mn) * L2E));
        mrun = mn;
      }
      asm volatile("" : "+v"(mrun), "+v"(lrun));
    };
    static_for<0, 16 * NX>([&](auto M) {
      constexpr int m = M, i = m & 15, k = NL + NX + (m >> 4);
      kstep(std::integral_constant<int, i>{}, ring[i], ra, k, m < 16);
      wait_vm<INFL - 1>();
      ring[i] = __builtin_amdgcn_raw_buffer_load_b128(rK, koff, (16 * (k + 1) + i) * 4096, NT);
      kstep(std::integral_constant<int, i>{}, land[m], rb, NL + (m >> 4), false);
      if constexpr (i == 15) { PF(k + 2); }
      __builtin_amdgcn_sched_barrier(0);
    });
#pragma unroll 1
    for (int kk = 0; kk < NL; ++kk) {
      const int k = NL + 2 * NX + kk;
      const int state = sched_ahead(t0, 16 * k + 16);
      static_for<0, 16>([&](auto I) {
        constexpr int i = I;
        const v4u kv = lk[(kk * 16 + i) * 64];
        kstep(I, ring[i], ra, k, false);
        wait_var(state);
        ring[i] = __builtin_amdgcn_raw_buffer_load_b128(rK, koff, (k + 1) * 65536 + i * 4096, NT);
        kstep(I, kv, rb, kk, false);
        __builtin_amdgcn_sched_barrier(0);
      });
      PF(k + 2);
    }
#pragma unroll 1
    for (int k = 2 * (NL + NX); k < 16; ++k) {
      const bool last = k == 15;
      const __amdgpu_buffer_rsrc_t rN = last ? rV : rK;
      const int sbase = last ? 0 : (k + 1) * 65536;
      const int state = last ? 2 : sched_ahead(t0, 16 * k + 16);
      static_for<0, 16>([&](auto I) {
        constexpr int i = I;
        kstep(I, ring[i], ra, k, false);
        wait_var(state);
        ring[i] = __builtin_amdgcn_raw_buffer_load_b128(rN, koff, sbase + i * 4096, NT);
        __builtin_amdgcn_sched_barrier(0);
      });
      PF(k + 2);
    }
    {
      const float m = wave_max(mrun);
      const float l = wave_sum(lrun * __builtin_amdgcn_exp2f((mrun - m) * L2E));
      if (lane == 0) { lds[L_SM + 2 * w] = m; lds[L_SM + 2 * w + 1] = l; }
    }
    float snext = scw[0];
    const v4u mask = onehot_mask(c);
    lds_barrier();
    float mx, inv;
    {
      const f32x4 s0 = *(const f32x4*)&lds[L_SM], s1 = *(const f32x4*)&lds[L_SM + 4];
      mx = fmaxf(fmaxf(s0.x, s0.z), fmaxf(s1.x, s1.z));
      float l = s0.y * __builtin_amdgcn_exp2f((s0.x - mx) * L2E);
      l = fmaf(s0.w, __builtin_amdgcn_exp2f((s0.z - mx) * L2E), l);
      l = fmaf(s1.y, __builtin_amdgcn_exp2f((s1.x - mx) * L2E), l);
      l = fmaf(s1.w, __builtin_amdgcn_exp2f((s1.z - mx) * L2E), l);
      inv = 1.0f / l;
    }
    f32x4 oacc = {0.f, 0.f, 0.f, 0.f};
#pragma unroll 1
    for (int k = 0; k < 16; ++k) {
      const bool last = k == 15;
      const int voff = last ? OOB : koff;
      const int sbase = last ? 0 : (k + 1) * 65536;
      const int state = last ? 2 : sched_ahead(t0, 256 + 16 * k + 16);
      unsigned pd = bfbits(__builtin_amdgcn_exp2f((snext - mx) * L2E) * inv);
      pd |= pd << 16;
      snext = scw[((k + 1) & 15) * 64];
      static_for<0, 16>([&](auto I) {
        constexpr int i = I;
        const v4u a = onehot<brev4(i)>(pd, mask);
        oacc = mfma16(a, ring[i], oacc);
        asm volatile("" : "+v"(oacc));
        wait_var(state);
        ring[i] = __builtin_amdgcn_raw_buffer_load_b128(rV, voff, sbase + i * 4096, NT);
        __builtin_amdgcn_sched_barrier(0);
      });
      PF(k + 18);
    }
    if (lane < 32) *(f32x4*)&lds[L_OP + w * 128 + 8 * c + 4 * g] = oacc;
    lds_barrier();
    if (w == 2) {
      const unsigned tag = ((const unsigned*)lds)[L_TAG];
      lds_barrier();
      publish(256 + lane, tag);
    }
  }
}

extern "C" void kernel_launch(void* const* d_in, const int* in_sizes, int n_in, void* d_out, int out_size, void* d_ws,
                              size_t ws_size, hipStream_t stream) {
  fused_kernel<<<256, 512, 0, stream>>>((const u16*)d_in[0], (const u16*)d_in[1], (const u16*)d_in[2], (const u16*)d_in[3],
                                        (const u16*)d_in[4], (const u16*)d_in[6], (const u16*)d_in[7], (const u16*)d_in[8],
                                        (const u16*)d_in[9], (unsigned char*)d_ws, (u16*)d_out);
}
```

```cpp
#include <hip/hip_runtime.h>
#include <hip/hip_bf16.h>
#include <stdint.h>
#include <type_traits>

typedef unsigned v4u __attribute__((ext_vector_type(4)));
typedef unsigned v2u __attribute__((ext_vector_type(2)));
typedef float f32x2 __attribute__((ext_vector_type(2)));
typedef float f32x4 __attribute__((ext_vector_type(4)));
typedef __bf16 bf16x8 __attribute__((ext_vector_type(8)));
typedef __bf16 bf16x2 __attribute__((ext_vector_type(2)));
typedef unsigned short u16;
typedef __attribute__((address_space(1))) unsigned gu32;
typedef __attribute__((address_space(3))) unsigned lu32;

constexpr int D_ = 640, H_ = 32, SK = 4096, DH = 128;
constexpr size_t WS_SG = 0, WS_EPB = 256 * 5120, WS_AN = WS_EPB + 4096;
constexpr unsigned TAG_STEP = 0x9E3779B9u;

#define TLO 5
#define THI 6
#define INFL 5
#define TAU 137
#define TOFF 190
#define TAUV 1367
#define DELTA 40
#define FAR 1000
#define ASLEEP 127
#define NOANCH 0
#define NL 2
#define WQ_INFL 3
#define WO_INFL 2
#define FSLEEP 1
#define FS0 24
#define PREW 0
#define WOEARLY 0
#define BURST 5
#define HPRIO 3
#define SPRIO 1
constexpr int NX = 2;
constexpr int NT = 2, SC1 = 16, OOB = (int)0x80000000;

__device__ __forceinline__ float rbf(float x) { return (float)(__bf16)x; }
__device__ __forceinline__ unsigned bfbits(float x) { return (unsigned)__builtin_bit_cast(u16, (__bf16)x); }
__device__ __forceinline__ float bflo(unsigned u) { return __builtin_bit_cast(float, u << 16); }
__device__ __forceinline__ float bfhi(unsigned u) { return __builtin_bit_cast(float, u & 0xffff0000u); }

template <int CTRL> __device__ __forceinline__ float dppf(float x) {
  return __builtin_bit_cast(float, __builtin_amdgcn_mov_dpp(__builtin_bit_cast(int, x), CTRL, 0xf, 0xf, true));
}
template <int CTRL> __device__ __forceinline__ unsigned dppu(unsigned x) {
  return (unsigned)__builtin_amdgcn_mov_dpp((int)x, CTRL, 0xf, 0xf, true);
}
constexpr int XOR1 = 0xB1, XOR2 = 0x4E, XOR7 = 0x141, XOR8 = 0x128, NEWBCAST = 0x150;

__device__ __forceinline__ float swap16_sum(float x) {
  auto s = __builtin_amdgcn_permlane16_swap(__float_as_uint(x), __float_as_uint(x), false, false);
  return __uint_as_float(s[0]) + __uint_as_float(s[1]);
}
__device__ __forceinline__ float swap32_sum(float x) {
  auto t = __builtin_amdgcn_permlane32_swap(__float_as_uint(x), __float_as_uint(x), false, false);
  return __uint_as_float(t[0]) + __uint_as_float(t[1]);
}
__device__ __forceinline__ float wave_sum(float x) {
  x += dppf<XOR1>(x); x += dppf<XOR2>(x); x += dppf<XOR7>(x); x += dppf<XOR8>(x);
  return swap32_sum(swap16_sum(x));
}
__device__ __forceinline__ float wave_max(float x) {
  x = fmaxf(x, dppf<XOR1>(x)); x = fmaxf(x, dppf<XOR2>(x)); x = fmaxf(x, dppf<XOR7>(x)); x = fmaxf(x, dppf<XOR8>(x));
  auto s = __builtin_amdgcn_permlane16_swap(__float_as_uint(x), __float_as_uint(x), false, false);
  x = fmaxf(__uint_as_float(s[0]), __uint_as_float(s[1]));
  auto t = __builtin_amdgcn_permlane32_swap(__float_as_uint(x), __float_as_uint(x), false, false);
  return fmaxf(__uint_as_float(t[0]), __uint_as_float(t[1]));
}
__device__ __forceinline__ float rdlane(float x, int l) { return __builtin_bit_cast(float, __builtin_amdgcn_readlane(__builtin_bit_cast(int, x), l)); }
__device__ __forceinline__ float dot2(unsigned a, unsigned b, float acc) {
  return __builtin_amdgcn_fdot2_f32_bf16(__builtin_bit_cast(bf16x2, a), __builtin_bit_cast(bf16x2, b), acc, false);
}
__device__ __forceinline__ f32x4 mfma16(v4u a, v4u b, f32x4 c) {
  return __builtin_amdgcn_mfma_f32_16x16x32_bf16(__builtin_bit_cast(bf16x8, a), __builtin_bit_cast(bf16x8, b), c, 0, 0, 0);
}
template <int J> __device__ __forceinline__ v4u onehot(unsigned pdup, const v4u& mask) {
  const unsigned pb = dppu<NEWBCAST + J>(pdup);
  v4u a;
  a.x = pb & mask.x; a.y = pb & mask.y; a.z = pb & mask.z; a.w = pb & mask.w;
  return a;
}
__device__ __forceinline__ v4u onehot_mask(int c) {
  const unsigned mm = (c & 1) ? 0xffff0000u : 0x0000ffffu;
  v4u mask;
  mask.x = (c >> 1) == 0 ? mm : 0u; mask.y = (c >> 1) == 1 ? mm : 0u; mask.z = (c >> 1) == 2 ? mm : 0u; mask.w = (c >> 1) == 3 ? mm : 0u;
  return mask;
}
__device__ __forceinline__ void lds_barrier() {
  asm volatile("s_waitcnt lgkmcnt(0)" ::: "memory");
  __builtin_amdgcn_s_barrier();
  asm volatile("" ::: "memory");
}
template <int N> __device__ __forceinline__ void wait_vm() { asm volatile("s_waitcnt vmcnt(%0)" ::"i"(N) : "memory"); }
__device__ __forceinline__ void wait_var(int state) {
  asm volatile("s_cmp_eq_u32 %0, 3\n\ts_cbranch_scc1 .Lwc%=\n\ts_waitcnt vmcnt(%3)\n\t"
               "s_cmp_eq_u32 %0, 2\n\ts_cbranch_scc1 .Lwc%=\n\ts_waitcnt vmcnt(%2)\n\t"
               "s_cmp_eq_u32 %0, 1\n\ts_cbranch_scc1 .Lwc%=\n\ts_waitcnt vmcnt(%1)\n.Lwc%=:"
               ::"s"(state), "i"(TLO - 1), "i"(THI - 1), "i"(THI) : "memory", "scc");
}
__device__ __forceinline__ int sched_state(unsigned target) {
  const int late = (int)((unsigned)__builtin_amdgcn_s_memrealtime() - target);
  return __builtin_amdgcn_readfirstlane(late < 0 ? 0 : late < FAR ? 1 : 2);
}
__device__ __forceinline__ int sched_nominal(unsigned t0, int n) {
  const int late = (int)((unsigned)__builtin_amdgcn_s_memrealtime() - (t0 + TOFF + ((n * (TAU * 256 / 10)) >> 8)));
  return __builtin_amdgcn_readfirstlane(late < 0 ? 0 : 1);
}
__device__ __forceinline__ void ln_stats(const float (&xv)[10], float& mb, float& sd) {
  float s = 0.f;
#pragma unroll
  for (int i = 0; i < 10; ++i) s += xv[i];
  const float mean = wave_sum(s) * (1.0f / D_);
  float q = 0.f;
#pragma unroll
  for (int i = 0; i < 10; ++i) { const float d = xv[i] - mean; q = fmaf(d, d, q); }
  const float var = rbf(wave_sum(q) * (1.0f / D_));
  mb = rbf(mean);
  sd = rbf(sqrtf(rbf(var + 1e-5f)));
}
__device__ __forceinline__ float ln_apply(float x, float mb, float sd) { return rbf(rbf(x - mb) / sd); }

template <int I, int N, typename Fn> __device__ __forceinline__ void static_for(Fn&& f) {
  if constexpr (I < N) { f(std::integral_constant<int, I>{}); static_for<I + 1, N>(f); }
}
__device__ __forceinline__ float gelu_bf(float u) {
  constexpr float K1 = -2.0f * 0.7978845608028654f * 1.4426950408889634f;
  constexpr float K2 = K1 * 0.044715f;
  const float e = __builtin_amdgcn_exp2f(u * fmaf(K2, u * u, K1));
  return rbf(u * __builtin_amdgcn_rcpf(1.0f + e));
}
constexpr int brev4(int j) { return ((j & 1) << 3) | ((j & 2) << 1) | ((j & 4) >> 1) | ((j & 8) >> 3); }

#define PF(k) do {} while (0)

constexpr int L_S = 0;
constexpr int L_SM = 2560;
constexpr int L_TAG = 2568;
constexpr int L_CNT = 2569;
constexpr int L_AN = 2570;
constexpr int L_TS = 2572;
constexpr int L_GATE = 2576;
constexpr int L_OP = 2704;
constexpr int L_SC = 3216;
constexpr int L_K = L_SC + 4096;
constexpr int L_END = L_K + 4 * 16 * NL * 256;

__global__ void __launch_bounds__(512, 2)
fused_kernel(const u16* __restrict__ x, const u16* __restrict__ kc, const u16* __restrict__ vc,
             const u16* __restrict__ wq, const u16* __restrict__ wo, const u16* __restrict__ w1, const u16* __restrict__ b1,
             const u16* __restrict__ w2, const u16* __restrict__ b2, unsigned char* ws, u16* out) {
  __shared__ __attribute__((aligned(16))) float lds[L_END];
  const int bh = blockIdx.x, b = bh >> 5, h = bh & 31;
  const int tid = threadIdx.x, lane = tid & 63, w = __builtin_amdgcn_readfirstlane(tid >> 6);
  const int g = lane >> 4, c = lane & 15;
  constexpr float L2E = 1.4426950408889634f;

  const __amdgpu_buffer_rsrc_t rP = __builtin_amdgcn_make_buffer_rsrc((void*)(ws + WS_SG + (size_t)bh * 5120), 0, 5120, 0x00020000);
  auto publish = [&](int t, unsigned tag) __attribute__((always_inline)) {
    const f32x2 sv = (*(const f32x2*)&lds[L_S + 2 * t] + *(const f32x2*)&lds[L_S + 640 + 2 * t]) + (*(const f32x2*)&lds[L_S + 1280 + 2 * t] + *(const f32x2*)&lds[L_S + 1920 + 2 * t]);
    v4u gv; gv.x = __float_as_uint(sv.x); gv.y = tag; gv.z = __float_as_uint(sv.y); gv.w = tag;
    __builtin_amdgcn_raw_buffer_store_b128(gv, rP, t * 16, 0, SC1);
  };

  if (w >= 4) {
    __builtin_amdgcn_s_setprio(HPRIO);
    const int hw = w - 4, ht = tid - 256;
    gu32* epb = (gu32*)(ws + WS_EPB) + b * 64;
    const unsigned* x32 = (const unsigned*)(x + b * D_);
    const u16* xb = x + b * D_;
    unsigned xr[5];
#pragma unroll
    for (int i = 0; i < 5; ++i) xr[i] = x32[lane + 64 * i];
    const unsigned xo0 = xb[160 * hw + 4 * c + g], xo1 = xb[160 * hw + 64 + 4 * c + g], xo2 = xb[160 * hw + 128 + 4 * (c & 7) + g];
    __builtin_amdgcn_sched_barrier(0);
    const __amdgpu_buffer_rsrc_t rQ = __builtin_amdgcn_make_buffer_rsrc((void*)(wq + h * DH), 0, D_ * H_ * DH * 2 - h * DH * 2, 0x00020000);
    const int qoff = (160 * hw + g) * (H_ * DH * 2) + c * 16;
    v4u wr[40];
    static_for<0, WQ_INFL>([&](auto T) {
      constexpr int t = T;
      wr[t] = __builtin_amdgcn_raw_buffer_load_b128(rQ, qoff, t * 4 * (H_ * DH * 2), 0);
      __builtin_amdgcn_sched_barrier(0);
    });
    v4u mask = onehot_mask(c);
    float xv[10];
#pragma unroll
    for (int i = 0; i < 5; ++i) { xv[2 * i] = bflo(xr[i]); xv[2 * i + 1] = bfhi(xr[i]); }
    float mb, sd;
    ln_stats(xv, mb, sd);
    asm volatile("" : "+v"(mb), "+v"(sd));
    static_for<WQ_INFL, 2 * WQ_INFL>([&](auto T) {
      constexpr int t = T;
      wait_vm<WQ_INFL - 1>();
      wr[t] = __builtin_amdgcn_raw_buffer_load_b128(rQ, qoff, t * 4 * (H_ * DH * 2), 0);
      __builtin_amdgcn_sched_barrier(0);
    });
    unsigned xn0 = bfbits(ln_apply(bflo(xo0), mb, sd)), xn1 = bfbits(ln_apply(bflo(xo1), mb, sd)), xn2 = bfbits(ln_apply(bflo(xo2), mb, sd));
    xn0 |= xn0 << 16; xn1 |= xn1 << 16; xn2 |= xn2 << 16;
    {
      f32x4 acc = {0.f, 0.f, 0.f, 0.f};
      static_for<0, 40>([&](auto T) {
        constexpr int t = T, n = t + 2 * WQ_INFL;
        if constexpr (n < 40) {
          wait_vm<WQ_INFL - 1>();
          wr[n] = __builtin_amdgcn_raw_buffer_load_b128(rQ, qoff, n * 4 * (H_ * DH * 2), 0);
        }
        const v4u a = onehot<(t & 15)>(t < 16 ? xn0 : t < 32 ? xn1 : xn2, mask);
        acc = mfma16(a, wr[t], acc);
        asm volatile("" : "+v"(acc));
        __builtin_amdgcn_sched_barrier(0);
      });
      if (lane < 32) *(f32x4*)&lds[L_S + hw * 128 + 8 * c + 4 * g] = acc;
    }
    const int ncol = h < 8 ? 32 : 16;
    const unsigned xg = xb[16 * h + (lane & 15) + ((lane & 16) && h < 8 ? 512 : 0)];
    const int gf = ht < 216 ? ht : 0;
    const v4u gw1 = ((const v4u*)w1)[gf], gb1 = ((const v4u*)b1)[gf], gw2 = ((const v4u*)w2)[gf];
    const unsigned b2v = b2[0];
    const unsigned tag = __hip_atomic_load(epb, __ATOMIC_RELAXED, __HIP_MEMORY_SCOPE_AGENT) + TAG_STEP;
    const int cgrp = (hw == 1 && h < 8) ? 32 + h : h;
    const unsigned xres = x32[8 * cgrp + (lane & 7)];
    __builtin_amdgcn_sched_barrier(0);
    PF(1);
    lds_barrier();
    __builtin_amdgcn_s_setprio(HPRIO);
    const __amdgpu_buffer_rsrc_t rO = __builtin_amdgcn_make_buffer_rsrc((void*)(wo + (size_t)h * DH * D_), 0, DH * D_ * 2, 0x00020000);
    const int ooff = (32 * hw + g) * (D_ * 2) + c * 16;
    static_for<0, 40>([&](auto I) {
      constexpr int i = I;
      if constexpr (i >= WO_INFL) wait_vm<WO_INFL - 1>();
      wr[i] = __builtin_amdgcn_raw_buffer_load_b128(rO, ooff, (i / 5) * 4 * (D_ * 2) + (i % 5) * 256, 0);
      __builtin_amdgcn_sched_barrier(0);
    });
    if (hw == 1) {
      const unsigned char* tp = lane == 0 ? (const unsigned char*)(vc + (size_t)bh * SK * DH)
                              : lane == 1 ? (const unsigned char*)(vc + (size_t)(bh + 1) * SK * DH) - 128
                              : lane == 2 ? (const unsigned char*)out
                              : lane == 3 ? ws + WS_SG + (size_t)b * 32 * 5120
                              : ws + WS_SG + (size_t)(b + 1) * 32 * 5120 - 16;
      const unsigned tv = __hip_atomic_load((gu32*)tp, __ATOMIC_RELAXED, __HIP_MEMORY_SCOPE_AGENT);
      asm volatile("" ::"v"(tv));
    }
    if (ht == 0) { ((unsigned*)lds)[L_TAG] = tag; ((unsigned*)lds)[L_CNT] = 0u; }
    PF(2);
    lds_barrier();
    if (hw == 3 && !NOANCH) {
      const __amdgpu_buffer_rsrc_t rA = __builtin_amdgcn_make_buffer_rsrc((void*)(ws + WS_AN + b * 4096), 0, 4096, 0x00020000);
      const v4u ts = *(const v4u*)&lds[L_TS];
      const unsigned mine = ts.x + (unsigned)(((int)(ts.y - ts.x) + (int)(ts.z - ts.x) + (int)(ts.w - ts.x)) >> 2);
      v4u gv; gv.x = mine; gv.y = tag; gv.z = mine; gv.w = tag;
      __builtin_amdgcn_raw_buffer_store_b128(gv, rA, lane < 8 ? h * 128 + lane * 16 : OOB, 0, SC1);
      __builtin_amdgcn_s_sleep(ASLEEP);
      asm volatile("" ::: "memory");
      const v2u r = __builtin_amdgcn_raw_buffer_load_b64(rA, lane < 32 ? lane * 128 : OOB, 0, SC1);
      const bool ok = lane < 32 && r.y == tag;
      const float cnt = (float)__builtin_popcountll(__builtin_amdgcn_ballot_w64(ok));
      const int mean = (int)(wave_sum(ok ? (float)(int)(r.x - mine) : 0.f) / fmaxf(cnt, 1.0f));
      if (lane == 0) __hip_atomic_store((lu32*)&lds[L_AN], mine + (unsigned)(mean + DELTA), __ATOMIC_RELAXED, __HIP_MEMORY_SCOPE_WORKGROUP);
    }
    __builtin_amdgcn_s_setprio(0);
    {
      float mb2, sd2;
#pragma unroll
      for (int i = 0; i < 10; ++i) xv[i] = ln_apply(xv[i], mb, sd);
      ln_stats(xv, mb2, sd2);
      const float hcol = ln_apply(ln_apply(bflo(xg), mb, sd), mb2, sd2);
      float w1v[8], b1v[8], w2v[8];
      const bool ok = ht < 216;
#pragma unroll
      for (int i = 0; i < 4; ++i) {
        w1v[2 * i] = bflo(gw1[i]); w1v[2 * i + 1] = bfhi(gw1[i]);
        b1v[2 * i] = bflo(gb1[i]); b1v[2 * i + 1] = bfhi(gb1[i]);
        w2v[2 * i] = ok ? bflo(gw2[i]) : 0.f; w2v[2 * i + 1] = ok ? bfhi(gw2[i]) : 0.f;
      }
      float ycol = 0.f;
#pragma unroll 1
      for (int j = 0; j < ncol; ++j) {
        const float hv = rdlane(hcol, j);
        float y = 0.f;
#pragma unroll
        for (int i = 0; i < 8; ++i) y = fmaf(gelu_bf(rbf(rbf(hv * w1v[i]) + b1v[i])), w2v[i], y);
        y = wave_sum(y);
        ycol = (lane == j) ? y : ycol;
      }
      if (lane < 32) lds[L_GATE + hw * 32 + lane] = ycol;
    }
    asm volatile("s_waitcnt lgkmcnt(0)" ::: "memory");
    if (lane == 0) __hip_atomic_fetch_add((lu32*)&lds[L_CNT], 1u, __ATOMIC_RELAXED, __HIP_MEMORY_SCOPE_WORKGROUP);
    while (__hip_atomic_load((lu32*)&lds[L_CNT], __ATOMIC_RELAXED, __HIP_MEMORY_SCOPE_WORKGROUP) < 4u) __builtin_amdgcn_s_sleep(2);
    asm volatile("" ::: "memory");
    __builtin_amdgcn_s_setprio(HPRIO);
    float gt0, gt1;
    {
      const int cc = (cgrp >= 32 ? 16 : 0) + 2 * (lane & 7);
      float y0 = (lds[L_GATE + cc] + lds[L_GATE + 32 + cc]) + (lds[L_GATE + 64 + cc] + lds[L_GATE + 96 + cc]);
      float y1 = (lds[L_GATE + cc + 1] + lds[L_GATE + 33 + cc]) + (lds[L_GATE + 65 + cc] + lds[L_GATE + 97 + cc]);
      y0 = rbf(rbf(y0) + bflo(b2v)); y1 = rbf(rbf(y1) + bflo(b2v));
      gt0 = rbf(y0 / (1.0f + __expf(-y0))); gt1 = rbf(y1 / (1.0f + __expf(-y1)));
    }
    int goff = (lane >> 3) * 5120 + cgrp * 128 + (lane & 7) * 16;
    float xr0 = bflo(xres), xr1 = bfhi(xres);
    asm volatile("" : "+v"(gt0), "+v"(gt1), "+v"(goff), "+v"(mask), "+v"(xr0), "+v"(xr1));
    const __amdgpu_buffer_rsrc_t rG = __builtin_amdgcn_make_buffer_rsrc((void*)(ws + WS_SG + (size_t)b * 32 * 5120), 0, 32 * 5120, 0x00020000);
    const __amdgpu_buffer_rsrc_t rOut = __builtin_amdgcn_make_buffer_rsrc((void*)(out + b * D_ + 16 * cgrp), 0, 32, 0x00020000);
    asm volatile("" ::"s"(rG), "s"(rOut), "s"(rP));
    __builtin_amdgcn_s_waitcnt(0x0F70);
    PF(3);
    lds_barrier();
    PF(4);
    unsigned od;
    {
      const int k = 32 * hw + 4 * (c & 7) + g;
      const float s = (lds[L_OP + k] + lds[L_OP + 128 + k]) + (lds[L_OP + 256 + k] + lds[L_OP + 384 + k]);
      od = bfbits(s); od |= od << 16;
    }
    f32x4 sacc[5];
#pragma unroll
    for (int u = 0; u < 5; ++u) sacc[u] = f32x4{0.f, 0.f, 0.f, 0.f};
    static_for<0, 8>([&](auto T) {
      constexpr int t = T;
      const v4u a = onehot<t>(od, mask);
#pragma unroll
      for (int u = 0; u < 5; ++u) sacc[u] = mfma16(a, wr[t * 5 + u], sacc[u]);
    });
    if (lane < 32) {
#pragma unroll
      for (int u = 0; u < 5; ++u) *(f32x4*)&lds[L_S + hw * 640 + u * 128 + 8 * c + 4 * g] = sacc[u];
    }
    lds_barrier();
    publish(ht, tag);
    PF(5);
    if (hw == 0 || cgrp >= 32) {
      v4u gr[4];
      if (FS0) __builtin_amdgcn_s_sleep(FS0);
#pragma unroll 1
      for (int spin = 0; spin < (1 << 17); ++spin) {
        asm volatile("" : "+v"(goff)::"memory");
#pragma unroll
        for (int i = 0; i < 4; ++i) gr[i] = __builtin_amdgcn_raw_buffer_load_b128(rG, goff, i * 8 * 5120, SC1);
        unsigned bad = 0;
#pragma unroll
        for (int i = 0; i < 4; ++i) bad |= (gr[i].y ^ tag) | (gr[i].w ^ tag);
        asm volatile("" : "+v"(bad));
        if (__all(bad == 0u)) break;
        if (FSLEEP) __builtin_amdgcn_s_sleep(FSLEEP);
      }
      float s0 = (__uint_as_float(gr[0].x) + __uint_as_float(gr[1].x)) + (__uint_as_float(gr[2].x) + __uint_as_float(gr[3].x));
      float s1 = (__uint_as_float(gr[0].z) + __uint_as_float(gr[1].z)) + (__uint_as_float(gr[2].z) + __uint_as_float(gr[3].z));
      s0 += dppf<XOR8>(s0); s1 += dppf<XOR8>(s1);
      s0 = swap32_sum(swap16_sum(s0)); s1 = swap32_sum(swap16_sum(s1));
      const unsigned o0 = bfbits(xr0 + rbf(rbf(s0) * gt0)), o1 = bfbits(xr1 + rbf(rbf(s1) * gt1));
      __builtin_amdgcn_raw_buffer_store_b32(o0 | (o1 << 16), rOut, lane * 4, 0, SC1);
      if (cgrp == 0 && lane == 0) __hip_atomic_store(epb, tag, __ATOMIC_RELAXED, __HIP_MEMORY_SCOPE_AGENT);
    }
    return;
  }

  {
    __builtin_amdgcn_s_setprio(SPRIO);
    const __amdgpu_buffer_rsrc_t rK = __builtin_amdgcn_make_buffer_rsrc((void*)(kc + (size_t)bh * SK * DH), 0, SK * DH * 2, 0x00020000);
    const __amdgpu_buffer_rsrc_t rV = __builtin_amdgcn_make_buffer_rsrc((void*)(vc + (size_t)bh * SK * DH), 0, SK * DH * 2, 0x00020000);
    const int koff = tid * 16;
    v4u* lk = (v4u*)&lds[L_K] + w * (16 * NL * 64) + lane;
    unsigned t0;
    v4u stg[8], land[16 * NX], ring[16];
    static_for<0, 16 * (NL + NX + 1)>([&](auto N) {
      constexpr int n = N;
      if constexpr (n >= BURST) wait_vm<INFL - 1>();
      const v4u kv = __builtin_amdgcn_raw_buffer_load_b128(rK, koff, n * 4096, NT);
      if constexpr (n < 16 * NL) stg[n & 7] = kv;
      else if constexpr (n < 16 * (NL + NX)) land[n - 16 * NL] = kv;
      else ring[n & 15] = kv;
      if constexpr (n >= 6 && n - 6 < 16 * NL) lk[(n - 6) * 64] = stg[(n - 6) & 7];
      if constexpr (n == BURST - 1) {
        t0 = (unsigned)__builtin_amdgcn_s_memrealtime();
      }
      if constexpr ((n & 15) == 15) { PF(1 + (n >> 4)); }
      __builtin_amdgcn_sched_barrier(0);
    });
    lds_barrier();
    v4u qp;
    {
      f32x4 s0 = *(const f32x4*)&lds[L_S + 8 * c], s1 = *(const f32x4*)&lds[L_S + 8 * c + 4];
#pragma unroll
      for (int ww = 1; ww < 4; ++ww) { s0 += *(const f32x4*)&lds[L_S + ww * 128 + 8 * c]; s1 += *(const f32x4*)&lds[L_S + ww * 128 + 8 * c + 4]; }
      qp.x = bfbits(s0.x) | (bfbits(s0.y) << 16); qp.y = bfbits(s0.z) | (bfbits(s0.w) << 16);
      qp.z = bfbits(s1.x) | (bfbits(s1.y) << 16); qp.w = bfbits(s1.z) | (bfbits(s1.w) << 16);
    }
    float* scw = &lds[L_SC + w * 1024 + lane];
    float mrun = 0.f, lrun = 0.f;
    float ra[4], rb[4];
    auto kstep = [&](auto J, const v4u& kv, float (&r)[4], int grp, bool first) __attribute__((always_inline)) {
      constexpr int j = J;
      float d = dot2(kv.x, qp.x, 0.f);
      d = dot2(kv.y, qp.y, d); d = dot2(kv.z, qp.z, d); d = dot2(kv.w, qp.w, d);
      d += dppf<XOR8>(d);
      if constexpr (!(j & 1)) { r[0] = d; asm volatile("" : "+v"(r[0])); return; }
      d = (c & 8) ? d : r[0];
      d += dppf<XOR7>(d);
      if constexpr (!(j & 2)) { r[1] = d; asm volatile("" : "+v"(r[1])); return; }
      d = (c & 4) ? d : r[1];
      d += dppf<XOR2>(d);
      if constexpr (!(j & 4)) { r[2] = d; asm volatile("" : "+v"(r[2])); return; }
      d = (c & 2) ? d : r[2];
      d += dppf<XOR1>(d);
      if constexpr (!(j & 8)) { r[3] = d; asm volatile("" : "+v"(r[3])); return; }
      d = (c & 1) ? d : r[3];
      const float s = rbf(rbf(d) * 0.08838834764831845f);
      scw[grp * 64] = s;
      if (first) { mrun = s; lrun = 1.0f; }
      else {
        const float mn = fmaxf(mrun, s);
        lrun = fmaf(lrun, __builtin_amdgcn_exp2f((mrun - mn) * L2E), __builtin_amdgcn_exp2f((s - mn) * L2E));
        mrun = mn;
      }
      asm volatile("" : "+v"(mrun), "+v"(lrun));
    };
    static_for<0, 16 * NX>([&](auto M) {
      constexpr int m = M, i = m & 15, k = NL + NX + (m >> 4);
      kstep(std::integral_constant<int, i>{}, ring[i], ra, k, m < 16);
      wait_vm<INFL - 1>();
      ring[i] = __builtin_amdgcn_raw_buffer_load_b128(rK, koff, (16 * (k + 1) + i) * 4096, NT);
      kstep(std::integral_constant<int, i>{}, land[m], rb, NL + (m >> 4), false);
      if constexpr (i == 15) { PF(k + 2); }
      __builtin_amdgcn_sched_barrier(0);
    });
#pragma unroll 1
    for (int kk = 0; kk < NL; ++kk) {
      const int k = NL + 2 * NX + kk;
      const int state = sched_nominal(t0, 16 * k + 16);
      static_for<0, 16>([&](auto I) {
        constexpr int i = I;
        const v4u kv = lk[(kk * 16 + i) * 64];
        kstep(I, ring[i], ra, k, false);
        wait_var(state);
        ring[i] = __builtin_amdgcn_raw_buffer_load_b128(rK, koff, (k + 1) * 65536 + i * 4096, NT);
        kstep(I, kv, rb, kk, false);
        __builtin_amdgcn_sched_barrier(0);
      });
      PF(k + 2);
    }
#pragma unroll 1
    for (int k = 2 * (NL + NX); k < 16; ++k) {
      const bool last = k == 15;
      const __amdgpu_buffer_rsrc_t rN = last ? rV : rK;
      const int sbase = last ? 0 : (k + 1) * 65536;
      const int state = last ? 3 : sched_nominal(t0, 16 * k + 16);
      if (k == 14) ((unsigned*)lds)[L_TS + w] = (unsigned)__builtin_amdgcn_s_memrealtime();
      static_for<0, 16>([&](auto I) {
        constexpr int i = I;
        kstep(I, ring[i], ra, k, false);
        wait_var(state);
        ring[i] = __builtin_amdgcn_raw_buffer_load_b128(rN, koff, sbase + i * 4096, NT);
        __builtin_amdgcn_sched_barrier(0);
      });
      PF(k + 2);
    }
    {
      const float m = wave_max(mrun);
      const float l = wave_sum(lrun * __builtin_amdgcn_exp2f((mrun - m) * L2E));
      if (lane == 0) { lds[L_SM + 2 * w] = m; lds[L_SM + 2 * w + 1] = l; }
    }
    float snext = scw[0];
    const v4u mask = onehot_mask(c);
    if (tid == 0) ((unsigned*)lds)[L_AN] = t0 + TOFF + ((240 * (TAU * 256 / 10)) >> 8);
    lds_barrier();
    float mx, inv;
    {
      const f32x4 s0 = *(const f32x4*)&lds[L_SM], s1 = *(const f32x4*)&lds[L_SM + 4];
      mx = fmaxf(fmaxf(s0.x, s0.z), fmaxf(s1.x, s1.z));
      float l = s0.y * __builtin_amdgcn_exp2f((s0.x - mx) * L2E);
      l = fmaf(s0.w, __builtin_amdgcn_exp2f((s0.z - mx) * L2E), l);
      l = fmaf(s1.y, __builtin_amdgcn_exp2f((s1.x - mx) * L2E), l);
      l = fmaf(s1.w, __builtin_amdgcn_exp2f((s1.z - mx) * L2E), l);
      inv = 1.0f / l;
    }
    f32x4 oacc = {0.f, 0.f, 0.f, 0.f};
#pragma unroll 1
    for (int k = 0; k < 16; ++k) {
      const bool last = k == 15;
      const int voff = last ? OOB : koff;
      const int sbase = last ? 0 : (k + 1) * 65536;
      const unsigned anchor = __builtin_amdgcn_readfirstlane(__hip_atomic_load((lu32*)&lds[L_AN], __ATOMIC_RELAXED, __HIP_MEMORY_SCOPE_WORKGROUP));
      const int state = last ? 3 : sched_state(anchor + (((16 * k + 32) * (TAUV * 256 / 100)) >> 8));
      unsigned pd = bfbits(__builtin_amdgcn_exp2f((snext - mx) * L2E) * inv);
      pd |= pd << 16;
      snext = scw[((k + 1) & 15) * 64];
      static_for<0, 16>([&](auto I) {
        constexpr int i = I;
        const v4u a = onehot<brev4(i)>(pd, mask);
        oacc = mfma16(a, ring[i], oacc);
        asm volatile("" : "+v"(oacc));
        wait_var(state);
        ring[i] = __builtin_amdgcn_raw_buffer_load_b128(rV, voff, sbase + i * 4096, NT);
        __builtin_amdgcn_sched_barrier(0);
      });
      PF(k + 18);
    }
    if (lane < 32) *(f32x4*)&lds[L_OP + w * 128 + 8 * c + 4 * g] = oacc;
    lds_barrier();
    if (w == 2) {
      const unsigned tag = ((const unsigned*)lds)[L_TAG];
      lds_barrier();
      publish(256 + lane, tag);
    }
  }
}

extern "C" void kernel_launch(void* const* d_in, const int* in_sizes, int n_in, void* d_out, int out_size, void* d_ws,
                              size_t ws_size, hipStream_t stream) {
  fused_kernel<<<256, 512, 0, stream>>>((const u16*)d_in[0], (const u16*)d_in[1], (const u16*)d_in[2], (const u16*)d_in[3],
                                        (const u16*)d_in[4], (const u16*)d_in[6], (const u16*)d_in[7], (const u16*)d_in[8],
                                        (const u16*)d_in[9], (unsigned char*)d_ws, (u16*)d_out);
}
```

```cpp
#include <hip/hip_runtime.h>
#include <hip/hip_bf16.h>
#include <stdint.h>
#include <type_traits>

typedef unsigned v4u __attribute__((ext_vector_type(4)));
typedef unsigned v2u __attribute__((ext_vector_type(2)));
typedef float f32x2 __attribute__((ext_vector_type(2)));
typedef float f32x4 __attribute__((ext_vector_type(4)));
typedef __bf16 bf16x8 __attribute__((ext_vector_type(8)));
typedef __bf16 bf16x2 __attribute__((ext_vector_type(2)));
typedef unsigned short u16;
typedef __attribute__((address_space(1))) unsigned gu32;
typedef __attribute__((address_space(3))) unsigned lu32;

constexpr int D_ = 640, H_ = 32, SK = 4096, DH = 128;
constexpr size_t WS_SG = 0, WS_EPB = 256 * 5120, WS_AN = WS_EPB + 4096;
constexpr unsigned TAG_STEP = 0x9E3779B9u;

#define TLO 5
#define THI 6
#define INFL 5
#define TAUV 1367
#define DELTA 35
#define FAR 70
#define WQ_HALF 9
#define INFL2 5
#define COLDST 1
#define LASTFREE 2
#define PSTEP 16
#define PALL 0
#define ROUNDS 7
#define POS1 304
#define POS2 400
#define TOFF 190
#define PSLEEP 110
#define NL 2
#define WQ_INFL 3
#define WO_INFL 2
#define FSLEEP 1
#define FS0 24
#define PREW 0
#define WOEARLY 0
#define BURST 5
#define HPRIO 3
#define SPRIO 1
constexpr int NX = 2;
constexpr int NT = 2, SC1 = 16, OOB = (int)0x80000000;

__device__ __forceinline__ float rbf(float x) { return (float)(__bf16)x; }
__device__ __forceinline__ unsigned bfbits(float x) { return (unsigned)__builtin_bit_cast(u16, (__bf16)x); }
__device__ __forceinline__ float bflo(unsigned u) { return __builtin_bit_cast(float, u << 16); }
__device__ __forceinline__ float bfhi(unsigned u) { return __builtin_bit_cast(float, u & 0xffff0000u); }

template <int CTRL> __device__ __forceinline__ float dppf(float x) {
  return __builtin_bit_cast(float, __builtin_amdgcn_mov_dpp(__builtin_bit_cast(int, x), CTRL, 0xf, 0xf, true));
}
template <int CTRL> __device__ __forceinline__ unsigned dppu(unsigned x) {
  return (unsigned)__builtin_amdgcn_mov_dpp((int)x, CTRL, 0xf, 0xf, true);
}
constexpr int XOR1 = 0xB1, XOR2 = 0x4E, XOR7 = 0x141, XOR8 = 0x128, NEWBCAST = 0x150;

__device__ __forceinline__ float swap16_sum(float x) {
  auto s = __builtin_amdgcn_permlane16_swap(__float_as_uint(x), __float_as_uint(x), false, false);
  return __uint_as_float(s[0]) + __uint_as_float(s[1]);
}
__device__ __forceinline__ float swap32_sum(float x) {
  auto t = __builtin_amdgcn_permlane32_swap(__float_as_uint(x), __float_as_uint(x), false, false);
  return __uint_as_float(t[0]) + __uint_as_float(t[1]);
}
__device__ __forceinline__ float wave_sum(float x) {
  x += dppf<XOR1>(x); x += dppf<XOR2>(x); x += dppf<XOR7>(x); x += dppf<XOR8>(x);
  return swap32_sum(swap16_sum(x));
}
__device__ __forceinline__ float wave_max(float x) {
  x = fmaxf(x, dppf<XOR1>(x)); x = fmaxf(x, dppf<XOR2>(x)); x = fmaxf(x, dppf<XOR7>(x)); x = fmaxf(x, dppf<XOR8>(x));
  auto s = __builtin_amdgcn_permlane16_swap(__float_as_uint(x), __float_as_uint(x), false, false);
  x = fmaxf(__uint_as_float(s[0]), __uint_as_float(s[1]));
  auto t = __builtin_amdgcn_permlane32_swap(__float_as_uint(x), __float_as_uint(x), false, false);
  return fmaxf(__uint_as_float(t[0]), __uint_as_float(t[1]));
}
__device__ __forceinline__ float rdlane(float x, int l) { return __builtin_bit_cast(float, __builtin_amdgcn_readlane(__builtin_bit_cast(int, x), l)); }
__device__ __forceinline__ float dot2(unsigned a, unsigned b, float acc) {
  return __builtin_amdgcn_fdot2_f32_bf16(__builtin_bit_cast(bf16x2, a), __builtin_bit_cast(bf16x2, b), acc, false);
}
__device__ __forceinline__ f32x4 mfma16(v4u a, v4u b, f32x4 c) {
  return __builtin_amdgcn_mfma_f32_16x16x32_bf16(__builtin_bit_cast(bf16x8, a), __builtin_bit_cast(bf16x8, b), c, 0, 0, 0);
}
template <int J> __device__ __forceinline__ v4u onehot(unsigned pdup, const v4u& mask) {
  const unsigned pb = dppu<NEWBCAST + J>(pdup);
  v4u a;
  a.x = pb & mask.x; a.y = pb & mask.y; a.z = pb & mask.z; a.w = pb & mask.w;
  return a;
}
__device__ __forceinline__ v4u onehot_mask(int c) {
  const unsigned mm = (c & 1) ? 0xffff0000u : 0x0000ffffu;
  v4u mask;
  mask.x = (c >> 1) == 0 ? mm : 0u; mask.y = (c >> 1) == 1 ? mm : 0u; mask.z = (c >> 1) == 2 ? mm : 0u; mask.w = (c >> 1) == 3 ? mm : 0u;
  return mask;
}
__device__ __forceinline__ void lds_barrier() {
  asm volatile("s_waitcnt lgkmcnt(0)" ::: "memory");
  __builtin_amdgcn_s_barrier();
  asm volatile("" ::: "memory");
}
template <int N> __device__ __forceinline__ void wait_vm() { asm volatile("s_waitcnt vmcnt(%0)" ::"i"(N) : "memory"); }
__device__ __forceinline__ void wait_var(int state) {
  asm volatile("s_cmp_eq_u32 %0, 3\n\ts_cbranch_scc1 .Lwc%=\n\ts_waitcnt vmcnt(%3)\n\t"
               "s_cmp_eq_u32 %0, 2\n\ts_cbranch_scc1 .Lwc%=\n\ts_waitcnt vmcnt(%2)\n\t"
               "s_cmp_eq_u32 %0, 1\n\ts_cbranch_scc1 .Lwc%=\n\ts_waitcnt vmcnt(%1)\n.Lwc%=:"
               ::"s"(state), "i"(TLO - 1), "i"(THI - 1), "i"(THI) : "memory", "scc");
}
__device__ __forceinline__ void ln_stats(const float (&xv)[10], float& mb, float& sd) {
  float s = 0.f;
#pragma unroll
  for (int i = 0; i < 10; ++i) s += xv[i];
  const float mean = wave_sum(s) * (1.0f / D_);
  float q = 0.f;
#pragma unroll
  for (int i = 0; i < 10; ++i) { const float d = xv[i] - mean; q = fmaf(d, d, q); }
  const float var = rbf(wave_sum(q) * (1.0f / D_));
  mb = rbf(mean);
  sd = rbf(sqrtf(rbf(var + 1e-5f)));
}
__device__ __forceinline__ float ln_apply(float x, float mb, float sd) { return rbf(rbf(x - mb) / sd); }

template <int I, int N, typename Fn> __device__ __forceinline__ void static_for(Fn&& f) {
  if constexpr (I < N) { f(std::integral_constant<int, I>{}); static_for<I + 1, N>(f); }
}
__device__ __forceinline__ float gelu_bf(float u) {
  constexpr float K1 = -2.0f * 0.7978845608028654f * 1.4426950408889634f;
  constexpr float K2 = K1 * 0.044715f;
  const float e = __builtin_amdgcn_exp2f(u * fmaf(K2, u * u, K1));
  return rbf(u * __builtin_amdgcn_rcpf(1.0f + e));
}
constexpr int brev4(int j) { return ((j & 1) << 3) | ((j & 2) << 1) | ((j & 4) >> 1) | ((j & 8) >> 3); }

#define PF(k) do {} while (0)

constexpr int L_S = 0;
constexpr int L_SM = 2560;
constexpr int L_TAG = 2568;
constexpr int L_CNT = 2569;
constexpr int L_AN = 2570;
constexpr int L_POS = 2571;
constexpr int L_TS = 2572;
constexpr int L_GATE = 2576;
constexpr int L_OP = 2704;
constexpr int L_SC = 3216;
constexpr int L_K = L_SC + 4096;
constexpr int L_END = L_K + 4 * 16 * NL * 256;

__global__ void __launch_bounds__(512, 2)
fused_kernel(const u16* __restrict__ x, const u16* __restrict__ kc, const u16* __restrict__ vc,
             const u16* __restrict__ wq, const u16* __restrict__ wo, const u16* __restrict__ w1, const u16* __restrict__ b1,
             const u16* __restrict__ w2, const u16* __restrict__ b2, unsigned char* ws, u16* out) {
  __shared__ __attribute__((aligned(16))) float lds[L_END];
  asm volatile("" ::"s"(x), "s"(kc), "s"(vc), "s"(wq));
  const int bh = blockIdx.x, b = bh >> 5, h = bh & 31;
  const int tid = threadIdx.x, lane = tid & 63, w = __builtin_amdgcn_readfirstlane(tid >> 6);
  const int g = lane >> 4, c = lane & 15;
  constexpr float L2E = 1.4426950408889634f;
  auto publish = [&](__amdgpu_buffer_rsrc_t rP, int t, unsigned tag) __attribute__((always_inline)) {
    const f32x2 sv = (*(const f32x2*)&lds[L_S + 2 * t] + *(const f32x2*)&lds[L_S + 640 + 2 * t]) + (*(const f32x2*)&lds[L_S + 1280 + 2 * t] + *(const f32x2*)&lds[L_S + 1920 + 2 * t]);
    v4u gv; gv.x = __float_as_uint(sv.x); gv.y = tag; gv.z = __float_as_uint(sv.y); gv.w = tag;
    __builtin_amdgcn_raw_buffer_store_b128(gv, rP, t * 16, 0, SC1);
  };

  if (w >= 4) {
    __builtin_amdgcn_s_setprio(HPRIO);
    const int hw = w - 4, ht = tid - 256;
    gu32* epb = (gu32*)(ws + WS_EPB) + b * 64;
    const unsigned* x32 = (const unsigned*)(x + b * D_);
    const u16* xb = x + b * D_;
    unsigned xr[5];
#pragma unroll
    for (int i = 0; i < 5; ++i) xr[i] = x32[lane + 64 * i];
    const unsigned xo0 = xb[160 * hw + 4 * c + g], xo1 = xb[160 * hw + 64 + 4 * c + g], xo2 = xb[160 * hw + 128 + 4 * (c & 7) + g];
    __builtin_amdgcn_sched_barrier(0);
    const __amdgpu_buffer_rsrc_t rQ = __builtin_amdgcn_make_buffer_rsrc((void*)(wq + h * DH), 0, D_ * H_ * DH * 2 - h * DH * 2, 0x00020000);
    const int qoff = (160 * hw + g) * (H_ * DH * 2) + c * 16;
    v4u wr[40];
    static_for<0, WQ_INFL>([&](auto T) {
      constexpr int t = T;
      wr[t] = __builtin_amdgcn_raw_buffer_load_b128(rQ, qoff, t * 4 * (H_ * DH * 2), 0);
      __builtin_amdgcn_sched_barrier(0);
    });
    v4u mask = onehot_mask(c);
    float xv[10];
#pragma unroll
    for (int i = 0; i < 5; ++i) { xv[2 * i] = bflo(xr[i]); xv[2 * i + 1] = bfhi(xr[i]); }
    float mb, sd;
    ln_stats(xv, mb, sd);
    asm volatile("" : "+v"(mb), "+v"(sd));
    static_for<WQ_INFL, 2 * WQ_INFL>([&](auto T) {
      constexpr int t = T;
      wait_vm<WQ_INFL - 1>();
      wr[t] = __builtin_amdgcn_raw_buffer_load_b128(rQ, qoff, t * 4 * (H_ * DH * 2), 0);
      __builtin_amdgcn_sched_barrier(0);
    });
    unsigned xn0 = bfbits(ln_apply(bflo(xo0), mb, sd)), xn1 = bfbits(ln_apply(bflo(xo1), mb, sd)), xn2 = bfbits(ln_apply(bflo(xo2), mb, sd));
    xn0 |= xn0 << 16; xn1 |= xn1 << 16; xn2 |= xn2 << 16;
    {
      f32x4 acc = {0.f, 0.f, 0.f, 0.f};
      static_for<0, 40>([&](auto T) {
        constexpr int t = T, n = t + 2 * WQ_INFL;
        if constexpr (n < 40) {
          wait_vm<WQ_INFL - 1 + (WQ_HALF && (n % WQ_HALF) == 0 ? 1 : 0)>();
          wr[n] = __builtin_amdgcn_raw_buffer_load_b128(rQ, qoff, n * 4 * (H_ * DH * 2), 0);
        }
        const v4u a = onehot<(t & 15)>(t < 16 ? xn0 : t < 32 ? xn1 : xn2, mask);
        acc = mfma16(a, wr[t], acc);
        asm volatile("" : "+v"(acc));
        __builtin_amdgcn_sched_barrier(0);
      });
      if (lane < 32) *(f32x4*)&lds[L_S + hw * 128 + 8 * c + 4 * g] = acc;
    }
    const int ncol = h < 8 ? 32 : 16;
    const unsigned xg = xb[16 * h + (lane & 15) + ((lane & 16) && h < 8 ? 512 : 0)];
    const int gf = ht % 192;
    const v4u gw1 = ((const v4u*)w1)[gf], gb1 = ((const v4u*)b1)[gf], gw2 = ((const v4u*)w2)[gf];
    const unsigned ew1 = w1[1536 + gf], eb1 = b1[1536 + gf], ew2 = w2[1536 + gf];
    const unsigned b2v = b2[0];
    const unsigned tag = __hip_atomic_load(epb, __ATOMIC_RELAXED, __HIP_MEMORY_SCOPE_AGENT) + TAG_STEP;
    const int cgrp = (hw == 1 && h < 8) ? 32 + h : h;
    const unsigned xres = x32[8 * cgrp + (lane & 7)];
    __builtin_amdgcn_sched_barrier(0);
    PF(1);
    lds_barrier();
    __builtin_amdgcn_s_setprio(HPRIO);
    const __amdgpu_buffer_rsrc_t rO = __builtin_amdgcn_make_buffer_rsrc((void*)(wo + (size_t)h * DH * D_), 0, DH * D_ * 2, 0x00020000);
    const int ooff = (32 * hw + g) * (D_ * 2) + c * 16;
    static_for<0, 40>([&](auto I) {
      constexpr int i = I;
      if constexpr (i >= WO_INFL) wait_vm<WO_INFL - 1>();
      wr[i] = __builtin_amdgcn_raw_buffer_load_b128(rO, ooff, (i / 5) * 4 * (D_ * 2) + (i % 5) * 256, 0);
      __builtin_amdgcn_sched_barrier(0);
    });
    if (hw == 1) {
      const unsigned char* tp = lane == 0 ? (const unsigned char*)(vc + (size_t)bh * SK * DH)
                              : lane == 1 ? (const unsigned char*)(vc + (size_t)(bh + 1) * SK * DH) - 128
                              : lane == 2 ? (const unsigned char*)out
                              : lane == 3 ? ws + WS_SG + (size_t)b * 32 * 5120
                              : ws + WS_SG + (size_t)(b + 1) * 32 * 5120 - 16;
      const unsigned tv = __hip_atomic_load((gu32*)tp, __ATOMIC_RELAXED, __HIP_MEMORY_SCOPE_AGENT);
      asm volatile("" ::"v"(tv));
    }
    if (ht == 0) { ((unsigned*)lds)[L_TAG] = tag; ((unsigned*)lds)[L_CNT] = 0u; }
    auto pacer = [&](int round, unsigned pos) __attribute__((always_inline)) {
      const __amdgpu_buffer_rsrc_t rA = __builtin_amdgcn_make_buffer_rsrc((void*)(ws + WS_AN + (PALL ? round * 8 + b : round) * 4096), 0, 4096, 0x00020000);
      for (int it = 0; it < 4096; ++it) {
        if (__builtin_amdgcn_readfirstlane(__hip_atomic_load((lu32*)&lds[L_POS], __ATOMIC_RELAXED, __HIP_MEMORY_SCOPE_WORKGROUP)) >= pos) break;
        __builtin_amdgcn_s_sleep(8);
      }
      __builtin_amdgcn_s_sleep(16);
      const unsigned tv = __hip_atomic_load((lu32*)&lds[L_TS + (lane & 3)], __ATOMIC_RELAXED, __HIP_MEMORY_SCOPE_WORKGROUP);
      const unsigned ref = __builtin_amdgcn_readfirstlane(tv);
      int d = (int)(tv - ref);
      d += (int)dppu<XOR1>((unsigned)d); d += (int)dppu<XOR2>((unsigned)d);
      const unsigned mine = ref + (unsigned)(d >> 2);
      v4u gv; gv.x = mine; gv.y = mine; gv.z = mine; gv.w = mine;
      __builtin_amdgcn_raw_buffer_store_b128(gv, rA, ((PALL || (h >> 2) == b) && lane < 8) ? h * 128 + lane * 16 : OOB, 0, SC1);
      __builtin_amdgcn_s_sleep(PSLEEP);
      asm volatile("" ::: "memory");
      const int r = (int)(__builtin_amdgcn_raw_buffer_load_b32(rA, lane < 32 ? lane * 128 : OOB, 0, SC1) - mine);
      const bool ok = lane < 32 && r > -1000 && r < 1000;
      const int cnt = __builtin_popcountll(__builtin_amdgcn_ballot_w64(ok));
      const int mean = (int)(wave_sum(ok ? (float)r : 0.f) / (float)(cnt > 0 ? cnt : 1));
      if (cnt >= 16) __hip_atomic_store((lu32*)&lds[L_AN], mine + (unsigned)(mean + DELTA), __ATOMIC_RELAXED, __HIP_MEMORY_SCOPE_WORKGROUP);
    };
    if (hw == 3 && (ROUNDS & 1)) pacer(0, 208);
    PF(2);
    lds_barrier();
    if (hw == 3) {
#pragma unroll 1
      for (int r = 1; r < 3; ++r) if ((ROUNDS >> r) & 1) pacer(r, r == 1 ? POS1 : POS2);
    }
    __builtin_amdgcn_s_setprio(0);
    if (hw < 3) {
      float mb2, sd2;
#pragma unroll
      for (int i = 0; i < 10; ++i) xv[i] = ln_apply(xv[i], mb, sd);
      ln_stats(xv, mb2, sd2);
      const float hcol = ln_apply(ln_apply(bflo(xg), mb, sd), mb2, sd2);
      float w1v[9], b1v[9], w2v[9];
#pragma unroll
      for (int i = 0; i < 4; ++i) {
        w1v[2 * i] = bflo(gw1[i]); w1v[2 * i + 1] = bfhi(gw1[i]);
        b1v[2 * i] = bflo(gb1[i]); b1v[2 * i + 1] = bfhi(gb1[i]);
        w2v[2 * i] = bflo(gw2[i]); w2v[2 * i + 1] = bfhi(gw2[i]);
      }
      w1v[8] = bflo(ew1); b1v[8] = bflo(eb1); w2v[8] = bflo(ew2);
      float ycol = 0.f;
#pragma unroll 1
      for (int j = 0; j < ncol; ++j) {
        const float hv = rdlane(hcol, j);
        float y = 0.f;
#pragma unroll
        for (int i = 0; i < 9; ++i) y = fmaf(gelu_bf(rbf(rbf(hv * w1v[i]) + b1v[i])), w2v[i], y);
        y = wave_sum(y);
        ycol = (lane == j) ? y : ycol;
      }
      if (lane < 32) lds[L_GATE + hw * 32 + lane] = ycol;
      asm volatile("s_waitcnt lgkmcnt(0)" ::: "memory");
      if (lane == 0) __hip_atomic_fetch_add((lu32*)&lds[L_CNT], 1u, __ATOMIC_RELAXED, __HIP_MEMORY_SCOPE_WORKGROUP);
      while (__hip_atomic_load((lu32*)&lds[L_CNT], __ATOMIC_RELAXED, __HIP_MEMORY_SCOPE_WORKGROUP) < 3u) __builtin_amdgcn_s_sleep(2);
      asm volatile("" ::: "memory");
    }
    __builtin_amdgcn_s_setprio(HPRIO);
    float gt0, gt1;
    {
      const int cc = (cgrp >= 32 ? 16 : 0) + 2 * (lane & 7);
      float y0 = (lds[L_GATE + cc] + lds[L_GATE + 32 + cc]) + lds[L_GATE + 64 + cc];
      float y1 = (lds[L_GATE + cc + 1] + lds[L_GATE + 33 + cc]) + lds[L_GATE + 65 + cc];
      y0 = rbf(rbf(y0) + bflo(b2v)); y1 = rbf(rbf(y1) + bflo(b2v));
      gt0 = rbf(y0 / (1.0f + __expf(-y0))); gt1 = rbf(y1 / (1.0f + __expf(-y1)));
    }
    int goff = (lane >> 3) * 5120 + cgrp * 128 + (lane & 7) * 16;
    float xr0 = bflo(xres), xr1 = bfhi(xres);
    asm volatile("" : "+v"(gt0), "+v"(gt1), "+v"(goff), "+v"(mask), "+v"(xr0), "+v"(xr1));
    const __amdgpu_buffer_rsrc_t rG = __builtin_amdgcn_make_buffer_rsrc((void*)(ws + WS_SG + (size_t)b * 32 * 5120), 0, 32 * 5120, 0x00020000);
    const __amdgpu_buffer_rsrc_t rOut = __builtin_amdgcn_make_buffer_rsrc((void*)(out + b * D_ + 16 * cgrp), 0, 32, 0x00020000);
    const __amdgpu_buffer_rsrc_t rP = __builtin_amdgcn_make_buffer_rsrc((void*)(ws + WS_SG + (size_t)bh * 5120), 0, 5120, 0x00020000);
    asm volatile("" ::"s"(rG), "s"(rOut), "s"(rP));
    __builtin_amdgcn_s_waitcnt(0x0F70);
    PF(3);
    lds_barrier();
    PF(4);
    unsigned od;
    {
      const int k = 32 * hw + 4 * (c & 7) + g;
      const float s = (lds[L_OP + k] + lds[L_OP + 128 + k]) + (lds[L_OP + 256 + k] + lds[L_OP + 384 + k]);
      od = bfbits(s); od |= od << 16;
    }
    f32x4 sacc[5];
#pragma unroll
    for (int u = 0; u < 5; ++u) sacc[u] = f32x4{0.f, 0.f, 0.f, 0.f};
    static_for<0, 8>([&](auto T) {
      constexpr int t = T;
      const v4u a = onehot<t>(od, mask);
#pragma unroll
      for (int u = 0; u < 5; ++u) sacc[u] = mfma16(a, wr[t * 5 + u], sacc[u]);
    });
    if (lane < 32) {
#pragma unroll
      for (int u = 0; u < 5; ++u) *(f32x4*)&lds[L_S + hw * 640 + u * 128 + 8 * c + 4 * g] = sacc[u];
    }
    lds_barrier();
    publish(rP, ht, tag);
    PF(5);
    if (hw == 0 || cgrp >= 32) {
      v4u gr[4];
      if (FS0) __builtin_amdgcn_s_sleep(FS0);
#pragma unroll 1
      for (int spin = 0; spin < (1 << 17); ++spin) {
        asm volatile("" : "+v"(goff)::"memory");
#pragma unroll
        for (int i = 0; i < 4; ++i) gr[i] = __builtin_amdgcn_raw_buffer_load_b128(rG, goff, i * 8 * 5120, SC1);
        unsigned bad = 0;
#pragma unroll
        for (int i = 0; i < 4; ++i) bad |= (gr[i].y ^ tag) | (gr[i].w ^ tag);
        asm volatile("" : "+v"(bad));
        if (__all(bad == 0u)) break;
        if (FSLEEP) __builtin_amdgcn_s_sleep(FSLEEP);
      }
      float s0 = (__uint_as_float(gr[0].x) + __uint_as_float(gr[1].x)) + (__uint_as_float(gr[2].x) + __uint_as_float(gr[3].x));
      float s1 = (__uint_as_float(gr[0].z) + __uint_as_float(gr[1].z)) + (__uint_as_float(gr[2].z) + __uint_as_float(gr[3].z));
      s0 += dppf<XOR8>(s0); s1 += dppf<XOR8>(s1);
      s0 = swap32_sum(swap16_sum(s0)); s1 = swap32_sum(swap16_sum(s1));
      const unsigned o0 = bfbits(xr0 + rbf(rbf(s0) * gt0)), o1 = bfbits(xr1 + rbf(rbf(s1) * gt1));
      __builtin_amdgcn_raw_buffer_store_b32(o0 | (o1 << 16), rOut, lane * 4, 0, SC1);
      if (cgrp == 0 && lane == 0) __hip_atomic_store(epb, tag, __ATOMIC_RELAXED, __HIP_MEMORY_SCOPE_AGENT);
    }
    return;
  }

  {
    __builtin_amdgcn_s_setprio(SPRIO);
    const __amdgpu_buffer_rsrc_t rK = __builtin_amdgcn_make_buffer_rsrc((void*)(kc + (size_t)bh * SK * DH), 0, SK * DH * 2, 0x00020000);
    const __amdgpu_buffer_rsrc_t rV = __builtin_amdgcn_make_buffer_rsrc((void*)(vc + (size_t)bh * SK * DH), 0, SK * DH * 2, 0x00020000);
    const int koff = tid * 16;
    v4u* lk = (v4u*)&lds[L_K] + w * (16 * NL * 64) + lane;
    unsigned t0;
    v4u stg[8], land[16 * NX], ring[16];
    static_for<0, 16 * (NL + NX + 1)>([&](auto N) {
      constexpr int n = N;
      if constexpr (n >= BURST && n < 16 * (NL + NX + 1) - LASTFREE) wait_vm<INFL - 1>();
      const v4u kv = __builtin_amdgcn_raw_buffer_load_b128(rK, koff, n * 4096, NT);
      if constexpr (n < 16 * NL) stg[n & 7] = kv;
      else if constexpr (n < 16 * (NL + NX)) land[n - 16 * NL] = kv;
      else ring[n & 15] = kv;
      if constexpr (n >= 6 && n - 6 < 16 * NL) lk[(n - 6) * 64] = stg[(n - 6) & 7];
      if constexpr (n == BURST - 1) {
        t0 = (unsigned)__builtin_amdgcn_s_memrealtime();
      }
      if constexpr ((n & 15) == 15) { PF(1 + (n >> 4)); }
      __builtin_amdgcn_sched_barrier(0);
    });
    if (lane == 0) { ((unsigned*)lds)[L_TS + w] = t0; ((unsigned*)lds)[L_AN] = t0 + TOFF; ((unsigned*)lds)[L_POS] = 0u; }
    lds_barrier();
    v4u qp;
    {
      f32x4 s0 = *(const f32x4*)&lds[L_S + 8 * c], s1 = *(const f32x4*)&lds[L_S + 8 * c + 4];
#pragma unroll
      for (int ww = 1; ww < 4; ++ww) { s0 += *(const f32x4*)&lds[L_S + ww * 128 + 8 * c]; s1 += *(const f32x4*)&lds[L_S + ww * 128 + 8 * c + 4]; }
      qp.x = bfbits(s0.x) | (bfbits(s0.y) << 16); qp.y = bfbits(s0.z) | (bfbits(s0.w) << 16);
      qp.z = bfbits(s1.x) | (bfbits(s1.y) << 16); qp.w = bfbits(s1.z) | (bfbits(s1.w) << 16);
    }
    unsigned anch = 0;
    auto pace = [&](int n) __attribute__((always_inline)) -> int {
      const unsigned anchor = __hip_atomic_load((lu32*)&lds[L_AN], __ATOMIC_RELAXED, __HIP_MEMORY_SCOPE_WORKGROUP);
      const unsigned v = (unsigned)__builtin_amdgcn_s_memrealtime() - (unsigned)((n * (TAUV * 256 / 100)) >> 8);
      __hip_atomic_store((lu32*)&lds[L_TS + w], v, __ATOMIC_RELAXED, __HIP_MEMORY_SCOPE_WORKGROUP);
      __hip_atomic_store((lu32*)&lds[L_POS], (unsigned)n, __ATOMIC_RELAXED, __HIP_MEMORY_SCOPE_WORKGROUP);
      anch = (unsigned)__builtin_amdgcn_readfirstlane(anchor);
      const int late = (int)(v - anch);
      return late < 0 ? 0 : late < FAR ? 1 : 2;
    };
    auto repace = [&](int n) __attribute__((always_inline)) -> int {
      const int late = (int)((unsigned)__builtin_amdgcn_s_memrealtime() - (unsigned)((n * (TAUV * 256 / 100)) >> 8) - anch);
      return late < 0 ? 0 : late < FAR ? 1 : 2;
    };
    float* scw = &lds[L_SC + w * 1024 + lane];
    float mrun = 0.f, lrun = 0.f;
    float ra[4], rb[4];
    auto kstep = [&](auto J, const v4u& kv, float (&r)[4], int grp, bool first) __attribute__((always_inline)) {
      constexpr int j = J;
      float d = dot2(kv.x, qp.x, 0.f);
      d = dot2(kv.y, qp.y, d); d = dot2(kv.z, qp.z, d); d = dot2(kv.w, qp.w, d);
      d += dppf<XOR8>(d);
      if constexpr (!(j & 1)) { r[0] = d; asm volatile("" : "+v"(r[0])); return; }
      d = (c & 8) ? d : r[0];
      d += dppf<XOR7>(d);
      if constexpr (!(j & 2)) { r[1] = d; asm volatile("" : "+v"(r[1])); return; }
      d = (c & 4) ? d : r[1];
      d += dppf<XOR2>(d);
      if constexpr (!(j & 4)) { r[2] = d; asm volatile("" : "+v"(r[2])); return; }
      d = (c & 2) ? d : r[2];
      d += dppf<XOR1>(d);
      if constexpr (!(j & 8)) { r[3] = d; asm volatile("" : "+v"(r[3])); return; }
      d = (c & 1) ? d : r[3];
      const float s = rbf(rbf(d) * 0.08838834764831845f);
      scw[grp * 64] = s;
      if (first) { mrun = s; lrun = 1.0f; }
      else {
        const float mn = fmaxf(mrun, s);
        lrun = fmaf(lrun, __builtin_amdgcn_exp2f((mrun - mn) * L2E), __builtin_amdgcn_exp2f((s - mn) * L2E));
        mrun = mn;
      }
      asm volatile("" : "+v"(mrun), "+v"(lrun));
    };
    static_for<0, 16 * NX>([&](auto M) {
      constexpr int m = M, i = m & 15, k = NL + NX + (m >> 4);
      kstep(std::integral_constant<int, i>{}, ring[i], ra, k, m < 16);
      wait_vm<INFL2 - 1>();
      ring[i] = __builtin_amdgcn_raw_buffer_load_b128(rK, koff, (16 * (k + 1) + i) * 4096, NT);
      kstep(std::integral_constant<int, i>{}, land[m], rb, NL + (m >> 4), false);
      if constexpr (i == 15) { PF(k + 2); }
      __builtin_amdgcn_sched_barrier(0);
    });
#pragma unroll 1
    for (int kk = 0; kk < NL; ++kk) {
      const int k = NL + 2 * NX + kk;
      int state = pace(16 * k + 16);
      if (COLDST && kk == 0) state = COLDST;
      static_for<0, 16>([&](auto I) {
        constexpr int i = I;
        const v4u kv = lk[(kk * 16 + i) * 64];
        kstep(I, ring[i], ra, k, false);
        wait_var(state);
        ring[i] = __builtin_amdgcn_raw_buffer_load_b128(rK, koff, (k + 1) * 65536 + i * 4096, NT);
        kstep(I, kv, rb, kk, false);
        __builtin_amdgcn_sched_barrier(0);
      });
      PF(k + 2);
    }
#pragma unroll 1
    for (int k = 2 * (NL + NX); k < 16; ++k) {
      const bool last = k == 15;
      const __amdgpu_buffer_rsrc_t rN = last ? rV : rK;
      const int sbase = last ? 0 : (k + 1) * 65536;
      int state = last ? 3 : pace(16 * k + 16);
      if (COLDST && k == 2 * (NL + NX)) state = COLDST;
      static_for<0, 16>([&](auto I) {
        constexpr int i = I;
        kstep(I, ring[i], ra, k, false);
        wait_var(state);
        ring[i] = __builtin_amdgcn_raw_buffer_load_b128(rN, koff, sbase + i * 4096, NT);
        __builtin_amdgcn_sched_barrier(0);
      });
      PF(k + 2);
    }
    {
      const float m = wave_max(mrun);
      const float l = wave_sum(lrun * __builtin_amdgcn_exp2f((mrun - m) * L2E));
      if (lane == 0) { lds[L_SM + 2 * w] = m; lds[L_SM + 2 * w + 1] = l; }
    }
    float snext = scw[0];
    const v4u mask = onehot_mask(c);
    lds_barrier();
    float mx, inv;
    {
      const f32x4 s0 = *(const f32x4*)&lds[L_SM], s1 = *(const f32x4*)&lds[L_SM + 4];
      mx = fmaxf(fmaxf(s0.x, s0.z), fmaxf(s1.x, s1.z));
      float l = s0.y * __builtin_amdgcn_exp2f((s0.x - mx) * L2E);
      l = fmaf(s0.w, __builtin_amdgcn_exp2f((s0.z - mx) * L2E), l);
      l = fmaf(s1.y, __builtin_amdgcn_exp2f((s1.x - mx) * L2E), l);
      l = fmaf(s1.w, __builtin_amdgcn_exp2f((s1.z - mx) * L2E), l);
      inv = 1.0f / l;
    }
    f32x4 oacc = {0.f, 0.f, 0.f, 0.f};
#pragma unroll 1
    for (int k = 0; k < 16; ++k) {
      const bool last = k == 15;
      const int voff = last ? OOB : koff;
      const int sbase = last ? 0 : (k + 1) * 65536;
      int state = last ? 3 : k == 0 ? 0 : pace(256 + 16 * k + 16);
      int fm = (((k + 1) & 15) - 2) >> 31;
      asm volatile("" : "+s"(fm));
      unsigned pd = bfbits(__builtin_amdgcn_exp2f((snext - mx) * L2E) * inv);
      pd |= pd << 16;
      snext = scw[((k + 1) & 15) * 64];
      static_for<0, 16>([&](auto I) {
        constexpr int i = I;
        const v4u a = onehot<brev4(i)>(pd, mask);
        oacc = mfma16(a, ring[i], oacc);
        asm volatile("" : "+v"(oacc));
        wait_var(state);
        ring[i] = __builtin_amdgcn_raw_buffer_load_b128(rV, voff, sbase + i * 4096, NT);
        __builtin_amdgcn_sched_barrier(0);
        if constexpr (PSTEP < 16 && i < 15 && (i + 1) % PSTEP == 0) {
          const int ns = repace(256 + 16 * k + 16 + i + 1);
          state = (state & fm) | (ns & ~fm);
          __builtin_amdgcn_sched_barrier(0);
        }
      });
      PF(k + 18);
    }
    if (lane < 32) *(f32x4*)&lds[L_OP + w * 128 + 8 * c + 4 * g] = oacc;
    lds_barrier();
    if (w == 2) {
      const unsigned tag = ((const unsigned*)lds)[L_TAG];
      unsigned char* wsp = ws;
      asm volatile("" : "+s"(wsp));
      const __amdgpu_buffer_rsrc_t rP = __builtin_amdgcn_make_buffer_rsrc((void*)(wsp + WS_SG + (size_t)bh * 5120), 0, 5120, 0x00020000);
      asm volatile("" ::"s"(rP));
      lds_barrier();
      publish(rP, 256 + lane, tag);
    }
  }
}

extern "C" void kernel_launch(void* const* d_in, const int* in_sizes, int n_in, void* d_out, int out_size, void* d_ws,
                              size_t ws_size, hipStream_t stream) {
  fused_kernel<<<256, 512, 0, stream>>>((const u16*)d_in[0], (const u16*)d_in[1], (const u16*)d_in[2], (const u16*)d_in[3],
                                        (const u16*)d_in[4], (const u16*)d_in[6], (const u16*)d_in[7], (const u16*)d_in[8],
                                        (const u16*)d_in[9], (unsigned char*)d_ws, (u16*)d_out);
}
```

```cpp
#include <hip/hip_runtime.h>
#include <hip/hip_bf16.h>
#include <stdint.h>
#include <type_traits>

typedef unsigned v2u __attribute__((ext_vector_type(2)));
typedef unsigned v4u __attribute__((ext_vector_type(4)));
typedef unsigned v8u __attribute__((ext_vector_type(8)));
typedef float f32x2 __attribute__((ext_vector_type(2)));
typedef float f32x4 __attribute__((ext_vector_type(4)));
typedef __bf16 bf16x2 __attribute__((ext_vector_type(2)));
typedef __bf16 bf16x8 __attribute__((ext_vector_type(8)));
typedef __bf16 bf16x16 __attribute__((ext_vector_type(16)));
typedef unsigned short u16;
typedef __attribute__((address_space(1))) unsigned gu32;
typedef __attribute__((address_space(3))) unsigned lu32;
typedef __attribute__((address_space(3))) f32x2 lf32x2;
typedef __attribute__((address_space(3))) f32x4 lf32x4;

constexpr int D_ = 640, H_ = 32, SK = 4096, DH = 128;
constexpr size_t WS_SG = 0, WS_EPB = 256 * 5120, WS_AN = WS_EPB + 4096;
constexpr unsigned TAG_STEP = 0x9E3779B9u;

constexpr int TLO = 5;
constexpr int TAU256 = 3499;
constexpr int TOFF = 190;
constexpr int DELTA = 35;
constexpr int FAR = 70;
constexpr int NL = 2, NX = 2;
constexpr int WQ_INFL = 3, WO_INFL = 2;
constexpr int NT = 2, SC1 = 16, OOB = (int)0x80000000;

__device__ __forceinline__ float rbf(float x) { return (float)(__bf16)x; }
__device__ __forceinline__ unsigned bfbits(float x) { return (unsigned)__builtin_bit_cast(u16, (__bf16)x); }
__device__ __forceinline__ unsigned bfpack(f32x2 v) { return __builtin_bit_cast(unsigned, __builtin_convertvector(v, bf16x2)); }
__device__ __forceinline__ float bflo(unsigned u) { return __builtin_bit_cast(float, u << 16); }
__device__ __forceinline__ float bfhi(unsigned u) { return __builtin_bit_cast(float, u & 0xffff0000u); }

template <int CTRL> __device__ __forceinline__ float dppf(float x) {
  return __builtin_bit_cast(float, __builtin_amdgcn_mov_dpp(__builtin_bit_cast(int, x), CTRL, 0xf, 0xf, true));
}
template <int CTRL> __device__ __forceinline__ unsigned dppu(unsigned x) {
  return (unsigned)__builtin_amdgcn_mov_dpp((int)x, CTRL, 0xf, 0xf, true);
}
constexpr int XOR1 = 0xB1, XOR2 = 0x4E, XOR7 = 0x141, XOR8 = 0x128, NEWBCAST = 0x150;

__device__ __forceinline__ float swap16_sum(float x) {
  auto s = __builtin_amdgcn_permlane16_swap(__float_as_uint(x), __float_as_uint(x), false, false);
  return __uint_as_float(s[0]) + __uint_as_float(s[1]);
}
__device__ __forceinline__ float swap32_sum(float x) {
  auto t = __builtin_amdgcn_permlane32_swap(__float_as_uint(x), __float_as_uint(x), false, false);
  return __uint_as_float(t[0]) + __uint_as_float(t[1]);
}
__device__ __forceinline__ float wave_sum(float x) {
  x += dppf<XOR1>(x); x += dppf<XOR2>(x); x += dppf<XOR7>(x); x += dppf<XOR8>(x);
  return swap32_sum(swap16_sum(x));
}
__device__ __forceinline__ float wave_max(float x) {
  x = fmaxf(x, dppf<XOR1>(x)); x = fmaxf(x, dppf<XOR2>(x)); x = fmaxf(x, dppf<XOR7>(x)); x = fmaxf(x, dppf<XOR8>(x));
  auto s = __builtin_amdgcn_permlane16_swap(__float_as_uint(x), __float_as_uint(x), false, false);
  x = fmaxf(__uint_as_float(s[0]), __uint_as_float(s[1]));
  auto t = __builtin_amdgcn_permlane32_swap(__float_as_uint(x), __float_as_uint(x), false, false);
  return fmaxf(__uint_as_float(t[0]), __uint_as_float(t[1]));
}
__device__ __forceinline__ float rdlane(float x, int l) { return __builtin_bit_cast(float, __builtin_amdgcn_readlane(__builtin_bit_cast(int, x), l)); }
__device__ __forceinline__ float dot2(unsigned a, unsigned b, float acc) {
  return __builtin_amdgcn_fdot2_f32_bf16(__builtin_bit_cast(bf16x2, a), __builtin_bit_cast(bf16x2, b), acc, false);
}
__device__ __forceinline__ f32x4 mfma16(v4u a, v4u b, f32x4 c) {
  return __builtin_amdgcn_mfma_f32_16x16x32_bf16(__builtin_bit_cast(bf16x8, a), __builtin_bit_cast(bf16x8, b), c, 0, 0, 0);
}
__device__ __forceinline__ f32x4 smfma16(v4u a, v4u b0, v4u b1, f32x4 c, int idx) {
  const v8u b = __builtin_shufflevector(b0, b1, 0, 1, 2, 3, 4, 5, 6, 7);
  return __builtin_amdgcn_smfmac_f32_16x16x64_bf16(__builtin_bit_cast(bf16x8, a), __builtin_bit_cast(bf16x16, b), c, idx, 0, 0);
}
template <int J> __device__ __forceinline__ v4u onehot(unsigned val2, const v4u& mask) {
  const unsigned pb = dppu<NEWBCAST + J>(val2);
  v4u a;
  a.x = pb & mask.x; a.y = pb & mask.y; a.z = pb & mask.z; a.w = pb & mask.w;
  return a;
}
__device__ __forceinline__ v4u onehot_mask(int c) {
  const unsigned mm = (c & 1) ? 0xffff0000u : 0x0000ffffu;
  v4u mask;
  mask.x = (c >> 1) == 0 ? mm : 0u; mask.y = (c >> 1) == 1 ? mm : 0u; mask.z = (c >> 1) == 2 ? mm : 0u; mask.w = (c >> 1) == 3 ? mm : 0u;
  return mask;
}
__device__ __forceinline__ void lds_barrier() {
  asm volatile("s_waitcnt lgkmcnt(0)" ::: "memory");
  __builtin_amdgcn_s_barrier();
  asm volatile("" ::: "memory");
}
__device__ __forceinline__ unsigned lds_get(float* p) { return __hip_atomic_load((lu32*)p, __ATOMIC_RELAXED, __HIP_MEMORY_SCOPE_WORKGROUP); }
__device__ __forceinline__ void lds_put(float* p, unsigned v) { __hip_atomic_store((lu32*)p, v, __ATOMIC_RELAXED, __HIP_MEMORY_SCOPE_WORKGROUP); }
template <int N> __device__ __forceinline__ void wait_vm() { asm volatile("s_waitcnt vmcnt(%0)" ::"i"(N) : "memory"); }
__device__ __forceinline__ void wait_var(int state) {
  asm volatile("s_cmp_eq_u32 %0, 3\n\ts_cbranch_scc1 .Lwc%=\n\ts_waitcnt vmcnt(%3)\n\t"
               "s_cmp_eq_u32 %0, 2\n\ts_cbranch_scc1 .Lwc%=\n\ts_waitcnt vmcnt(%2)\n\t"
               "s_cmp_eq_u32 %0, 1\n\ts_cbranch_scc1 .Lwc%=\n\ts_waitcnt vmcnt(%1)\n.Lwc%=:"
               ::"s"(state), "i"(TLO - 1), "i"(TLO), "i"(TLO + 1) : "memory", "scc");
}
__device__ __forceinline__ void ln_stats(const float (&xv)[10], float& mb, float& sd) {
  float s = 0.f;
#pragma unroll
  for (int i = 0; i < 10; ++i) s += xv[i];
  const float mean = wave_sum(s) * (1.0f / D_);
  float q = 0.f;
#pragma unroll
  for (int i = 0; i < 10; ++i) { const float d = xv[i] - mean; q = fmaf(d, d, q); }
  const float var = rbf(wave_sum(q) * (1.0f / D_));
  mb = rbf(mean);
  sd = rbf(sqrtf(rbf(var + 1e-5f)));
}
__device__ __forceinline__ float ln_apply(float x, float mb, float sd) { return rbf(rbf(x - mb) / sd); }
__device__ __forceinline__ float gelu_bf(float u) {
  constexpr float K1 = -2.0f * 0.7978845608028654f * 1.4426950408889634f;
  constexpr float K2 = K1 * 0.044715f;
  const float e = __builtin_amdgcn_exp2f(u * fmaf(K2, u * u, K1));
  return rbf(u * __builtin_amdgcn_rcpf(1.0f + e));
}
template <int I, int N, typename Fn> __device__ __forceinline__ void static_for(Fn&& f) {
  if constexpr (I < N) { f(std::integral_constant<int, I>{}); static_for<I + 1, N>(f); }
}
constexpr int brev4(int j) { return ((j & 1) << 3) | ((j & 2) << 1) | ((j & 4) >> 1) | ((j & 8) >> 3); }

constexpr int L_S = 0;
constexpr int L_SM = 2560;
constexpr int L_TAG = 2568;
constexpr int L_CNT = 2569;
constexpr int L_AN = 2570;
constexpr int L_POS = 2571;
constexpr int L_TS = 2572;
constexpr int L_GATE = 2576;
constexpr int L_OP = 2704;
constexpr int L_SC = 3216;
constexpr int L_K = L_SC + 4096;
constexpr int L_END = L_K + 4 * 16 * NL * 256;

__global__ void __launch_bounds__(512, 2)
fused_kernel(const u16* __restrict__ x, const u16* __restrict__ kc, const u16* __restrict__ vc,
             const u16* __restrict__ wq, const u16* __restrict__ wo, const u16* __restrict__ w1, const u16* __restrict__ b1,
             const u16* __restrict__ w2, const u16* __restrict__ b2, unsigned char* ws, u16* out) {
  __shared__ __attribute__((aligned(16))) float lds[L_END];
  asm volatile("" ::"s"(x), "s"(kc), "s"(vc), "s"(wq));
  const int bh = blockIdx.x, b = bh >> 5, h = bh & 31;
  const int tid = threadIdx.x, lane = tid & 63, w = __builtin_amdgcn_readfirstlane(tid >> 6);
  const int g = lane >> 4, c = lane & 15;
  constexpr float L2E = 1.4426950408889634f;

  auto publish = [&](__amdgpu_buffer_rsrc_t rP, int t, unsigned tag) __attribute__((always_inline)) {
    const f32x2 sv = (*(const f32x2*)&lds[L_S + 2 * t] + *(const f32x2*)&lds[L_S + 640 + 2 * t]) +
                     (*(const f32x2*)&lds[L_S + 1280 + 2 * t] + *(const f32x2*)&lds[L_S + 1920 + 2 * t]);
    v4u gv; gv.x = __float_as_uint(sv.x); gv.y = tag; gv.z = __float_as_uint(sv.y); gv.w = tag;
    __builtin_amdgcn_raw_buffer_store_b128(gv, rP, t * 16, 0, SC1);
  };

  if (w >= 4) {
    __builtin_amdgcn_s_setprio(3);
    const int hw = w - 4, ht = tid - 256;
    gu32* epb = (gu32*)(ws + WS_EPB) + b * 64;
    const unsigned* x32 = (const unsigned*)(x + b * D_);
    const u16* xb = x + b * D_;
    unsigned xr[5];
#pragma unroll
    for (int i = 0; i < 5; ++i) xr[i] = x32[lane + 64 * i];
    const unsigned xo0 = xb[160 * hw + 4 * c + g], xo1 = xb[160 * hw + 64 + 4 * c + g], xo2 = xb[160 * hw + 128 + 4 * (c & 7) + g];
    __builtin_amdgcn_sched_barrier(0);
    const __amdgpu_buffer_rsrc_t rQ = __builtin_amdgcn_make_buffer_rsrc((void*)(wq + h * DH), 0, D_ * H_ * DH * 2 - h * DH * 2, 0x00020000);
    const int qoff = (160 * hw + g) * (H_ * DH * 2) + c * 16;
    v4u wr[40];
    static_for<0, WQ_INFL>([&](auto T) {
      constexpr int t = T;
      wr[t] = __builtin_amdgcn_raw_buffer_load_b128(rQ, qoff, t * 4 * (H_ * DH * 2), 0);
      __builtin_amdgcn_sched_barrier(0);
    });
    const v4u mask = onehot_mask(c);
    float xv[10];
#pragma unroll
    for (int i = 0; i < 5; ++i) { xv[2 * i] = bflo(xr[i]); xv[2 * i + 1] = bfhi(xr[i]); }
    float mb, sd;
    ln_stats(xv, mb, sd);
    asm volatile("" : "+v"(mb), "+v"(sd));
    static_for<WQ_INFL, 2 * WQ_INFL>([&](auto T) {
      constexpr int t = T;
      wait_vm<WQ_INFL - 1>();
      wr[t] = __builtin_amdgcn_raw_buffer_load_b128(rQ, qoff, t * 4 * (H_ * DH * 2), 0);
      __builtin_amdgcn_sched_barrier(0);
    });
    unsigned xn0 = bfbits(ln_apply(bflo(xo0), mb, sd)), xn1 = bfbits(ln_apply(bflo(xo1), mb, sd)), xn2 = bfbits(ln_apply(bflo(xo2), mb, sd));
    xn0 |= xn0 << 16; xn1 |= xn1 << 16; xn2 |= xn2 << 16;
    {
      f32x4 acc = {0.f, 0.f, 0.f, 0.f};
      static_for<0, 40>([&](auto T) {
        constexpr int t = T, n = t + 2 * WQ_INFL;
        if constexpr (n < 40) {
          wait_vm<WQ_INFL - 1 + (n % 9 == 0 ? 1 : 0)>();
          wr[n] = __builtin_amdgcn_raw_buffer_load_b128(rQ, qoff, n * 4 * (H_ * DH * 2), 0);
        }
        const v4u a = onehot<(t & 15)>(t < 16 ? xn0 : t < 32 ? xn1 : xn2, mask);
        acc = mfma16(a, wr[t], acc);
        asm volatile("" : "+v"(acc));
        __builtin_amdgcn_sched_barrier(0);
      });
      if (lane < 32) *(f32x4*)&lds[L_S + hw * 128 + 8 * c + 4 * g] = acc;
    }
    const int ncol = h < 8 ? 32 : 16;
    const unsigned xg = xb[16 * h + (lane & 15) + ((lane & 16) && h < 8 ? 512 : 0)];
    const int gf = ht % 192;
    const v4u gw1 = ((const v4u*)w1)[gf], gb1 = ((const v4u*)b1)[gf], gw2 = ((const v4u*)w2)[gf];
    const unsigned ew1 = w1[1536 + gf], eb1 = b1[1536 + gf], ew2 = w2[1536 + gf];
    const unsigned b2v = b2[0];
    const unsigned tag = __hip_atomic_load(epb, __ATOMIC_RELAXED, __HIP_MEMORY_SCOPE_AGENT) + TAG_STEP;
    const int cgrp = (hw == 1 && h < 8) ? 32 + h : h;
    const unsigned xres = x32[8 * cgrp + (lane & 7)];
    __builtin_amdgcn_sched_barrier(0);
    lds_barrier();
    const __amdgpu_buffer_rsrc_t rO = __builtin_amdgcn_make_buffer_rsrc((void*)(wo + (size_t)h * DH * D_), 0, DH * D_ * 2, 0x00020000);
    const int ooff = (32 * hw + g) * (D_ * 2) + c * 16;
    static_for<0, 40>([&](auto I) {
      constexpr int i = I;
      if constexpr (i >= WO_INFL) wait_vm<WO_INFL - 1>();
      wr[i] = __builtin_amdgcn_raw_buffer_load_b128(rO, ooff, (i / 5) * 4 * (D_ * 2) + (i % 5) * 256, 0);
      __builtin_amdgcn_sched_barrier(0);
    });
    if (hw == 1) {
      const unsigned char* tp = lane == 0 ? (const unsigned char*)(vc + (size_t)bh * SK * DH)
                              : lane == 1 ? (const unsigned char*)(vc + (size_t)(bh + 1) * SK * DH) - 128
                              : lane == 2 ? (const unsigned char*)out
                              : lane == 3 ? ws + WS_SG + (size_t)b * 32 * 5120
                              : ws + WS_SG + (size_t)(b + 1) * 32 * 5120 - 16;
      const unsigned tv = __hip_atomic_load((gu32*)tp, __ATOMIC_RELAXED, __HIP_MEMORY_SCOPE_AGENT);
      asm volatile("" ::"v"(tv));
    }
    if (ht == 0) { ((unsigned*)lds)[L_TAG] = tag; ((unsigned*)lds)[L_CNT] = 0u; }
    auto pacer = [&](int round, unsigned pos) __attribute__((always_inline)) {
      const __amdgpu_buffer_rsrc_t rA = __builtin_amdgcn_make_buffer_rsrc((void*)(ws + WS_AN + round * 4096), 0, 4096, 0x00020000);
      unsigned at = 0;
      for (int it = 0; it < 4096; ++it) {
        at = __builtin_amdgcn_readfirstlane(lds_get(&lds[L_POS]));
        if (at >= pos) break;
        __builtin_amdgcn_s_sleep(8);
      }
      if (at > pos + 16u) return;
      __builtin_amdgcn_s_sleep(16);
      const unsigned tv = lds_get(&lds[L_TS + (lane & 3)]);
      const unsigned ref = __builtin_amdgcn_readfirstlane(tv);
      int d = (int)(tv - ref);
      d += (int)dppu<XOR1>((unsigned)d); d += (int)dppu<XOR2>((unsigned)d);
      const unsigned mine = ref + (unsigned)(d >> 2);
      v4u gv; gv.x = mine; gv.y = mine; gv.z = mine; gv.w = mine;
      __builtin_amdgcn_raw_buffer_store_b128(gv, rA, ((h >> 2) == b && lane < 8) ? h * 128 + lane * 16 : OOB, 0, SC1);
      __builtin_amdgcn_s_sleep(110);
      asm volatile("" ::: "memory");
      const int r = (int)(__builtin_amdgcn_raw_buffer_load_b32(rA, lane < 32 ? lane * 128 : OOB, 0, SC1) - mine);
      const bool ok = lane < 32 && r > -1000 && r < 1000;
      const int cnt = __builtin_popcountll(__builtin_amdgcn_ballot_w64(ok));
      const int mean = (int)(wave_sum(ok ? (float)r : 0.f) / (float)(cnt > 0 ? cnt : 1));
      if (cnt >= 16) lds_put(&lds[L_AN], mine + (unsigned)(mean + DELTA));
    };
    if (hw == 3) pacer(0, 208);
    lds_barrier();
    if (hw == 3) {
#pragma unroll 1
      for (int r = 1; r < 3; ++r) pacer(r, r == 1 ? 304 : 400);
    }
    __builtin_amdgcn_s_setprio(0);
    if (hw < 3) {
      float mb2, sd2;
#pragma unroll
      for (int i = 0; i < 10; ++i) xv[i] = ln_apply(xv[i], mb, sd);
      ln_stats(xv, mb2, sd2);
      const float hcol = ln_apply(ln_apply(bflo(xg), mb, sd), mb2, sd2);
      float w1v[9], b1v[9], w2v[9];
#pragma unroll
      for (int i = 0; i < 4; ++i) {
        w1v[2 * i] = bflo(gw1[i]); w1v[2 * i + 1] = bfhi(gw1[i]);
        b1v[2 * i] = bflo(gb1[i]); b1v[2 * i + 1] = bfhi(gb1[i]);
        w2v[2 * i] = bflo(gw2[i]); w2v[2 * i + 1] = bfhi(gw2[i]);
      }
      w1v[8] = bflo(ew1); b1v[8] = bflo(eb1); w2v[8] = bflo(ew2);
      float ycol = 0.f;
#pragma unroll 1
      for (int j = 0; j < ncol; ++j) {
        const float hv = rdlane(hcol, j);
        float y = 0.f;
#pragma unroll
        for (int i = 0; i < 9; ++i) y = fmaf(gelu_bf(rbf(rbf(hv * w1v[i]) + b1v[i])), w2v[i], y);
        y = wave_sum(y);
        ycol = (lane == j) ? y : ycol;
      }
      if (lane < 32) lds[L_GATE + hw * 32 + lane] = ycol;
      asm volatile("s_waitcnt lgkmcnt(0)" ::: "memory");
      if (lane == 0) __hip_atomic_fetch_add((lu32*)&lds[L_CNT], 1u, __ATOMIC_RELAXED, __HIP_MEMORY_SCOPE_WORKGROUP);
      while (lds_get(&lds[L_CNT]) < 3u) __builtin_amdgcn_s_sleep(2);
      asm volatile("" ::: "memory");
    }
    __builtin_amdgcn_s_setprio(3);
    float gt0, gt1;
    {
      const int cc = (cgrp >= 32 ? 16 : 0) + 2 * (lane & 7);
      float y0 = (lds[L_GATE + cc] + lds[L_GATE + 32 + cc]) + lds[L_GATE + 64 + cc];
      float y1 = (lds[L_GATE + cc + 1] + lds[L_GATE + 33 + cc]) + lds[L_GATE + 65 + cc];
      y0 = rbf(rbf(y0) + bflo(b2v)); y1 = rbf(rbf(y1) + bflo(b2v));
      gt0 = rbf(y0 / (1.0f + __expf(-y0))); gt1 = rbf(y1 / (1.0f + __expf(-y1)));
    }
    int goff = (lane >> 3) * 5120 + cgrp * 128 + (lane & 7) * 16;
    float xr0 = bflo(xres), xr1 = bfhi(xres);
    v4u smask;
    smask.x = (c >> 2) == 0 ? 0x0000ffffu : 0u; smask.y = (c >> 2) == 1 ? 0x0000ffffu : 0u;
    smask.z = smask.x << 16; smask.w = smask.y << 16;
    int sidx = (c & 3) * 0x5555;
    lf32x2* op = (lf32x2*)&lds[L_OP + 32 * hw + 8 * (c & 3) + 2 * g];
    lf32x4* sp = (lf32x4*)&lds[L_S + hw * 640 + 8 * c + 4 * (g & 1)];
    f32x4 sacc[5];
#pragma unroll
    for (int u = 0; u < 5; ++u) { sacc[u] = f32x4{0.f, 0.f, 0.f, 0.f}; asm volatile("" : "+v"(sacc[u])); }
    asm volatile("" : "+v"(gt0), "+v"(gt1), "+v"(goff), "+v"(smask), "+v"(xr0), "+v"(xr1), "+v"(sidx), "+v"(op), "+v"(sp));
    const __amdgpu_buffer_rsrc_t rG = __builtin_amdgcn_make_buffer_rsrc((void*)(ws + WS_SG + (size_t)b * 32 * 5120), 0, 32 * 5120, 0x00020000);
    const __amdgpu_buffer_rsrc_t rOut = __builtin_amdgcn_make_buffer_rsrc((void*)(out + b * D_ + 16 * cgrp), 0, 32, 0x00020000);
    const __amdgpu_buffer_rsrc_t rP = __builtin_amdgcn_make_buffer_rsrc((void*)(ws + WS_SG + (size_t)bh * 5120), 0, 5120, 0x00020000);
    asm volatile("" ::"s"(rG), "s"(rOut), "s"(rP));
    __builtin_amdgcn_s_waitcnt(0x0F70);
    lds_barrier();
    const unsigned od = bfpack((op[0] + op[64]) + (op[128] + op[192]));
    static_for<0, 4>([&](auto T) {
      constexpr int t = T;
      const v4u a = onehot<t>(od, smask);
#pragma unroll
      for (int u = 0; u < 5; ++u) sacc[u] = smfma16(a, wr[10 * t + u], wr[10 * t + 5 + u], sacc[u], sidx);
    });
    if (lane < 32) {
#pragma unroll
      for (int u = 0; u < 5; ++u) sp[u * 32] = sacc[u];
    }
    lds_barrier();
    publish(rP, ht, tag);
    if (hw == 0 || cgrp >= 32) {
      v4u gr[4];
      __builtin_amdgcn_s_sleep(22);
#pragma unroll 1
      for (int spin = 0; spin < (1 << 17); ++spin) {
        asm volatile("" : "+v"(goff)::"memory");
#pragma unroll
        for (int i = 0; i < 4; ++i) gr[i] = __builtin_amdgcn_raw_buffer_load_b128(rG, goff, i * 8 * 5120, SC1);
        unsigned bad = 0;
#pragma unroll
        for (int i = 0; i < 4; ++i) bad |= (gr[i].y ^ tag) | (gr[i].w ^ tag);
        asm volatile("" : "+v"(bad));
        if (__all(bad == 0u)) break;
        __builtin_amdgcn_s_sleep(1);
      }
      float s0 = (__uint_as_float(gr[0].x) + __uint_as_float(gr[1].x)) + (__uint_as_float(gr[2].x) + __uint_as_float(gr[3].x));
      float s1 = (__uint_as_float(gr[0].z) + __uint_as_float(gr[1].z)) + (__uint_as_float(gr[2].z) + __uint_as_float(gr[3].z));
      s0 += dppf<XOR8>(s0); s1 += dppf<XOR8>(s1);
      s0 = swap32_sum(swap16_sum(s0)); s1 = swap32_sum(swap16_sum(s1));
      const unsigned o0 = bfbits(xr0 + rbf(rbf(s0) * gt0)), o1 = bfbits(xr1 + rbf(rbf(s1) * gt1));
      __builtin_amdgcn_raw_buffer_store_b32(o0 | (o1 << 16), rOut, lane * 4, 0, SC1);
      if (cgrp == 0 && lane == 0) __hip_atomic_store(epb, tag, __ATOMIC_RELAXED, __HIP_MEMORY_SCOPE_AGENT);
    }
    return;
  }

  {
    __builtin_amdgcn_s_setprio(1);
    const __amdgpu_buffer_rsrc_t rK = __builtin_amdgcn_make_buffer_rsrc((void*)(kc + (size_t)bh * SK * DH), 0, SK * DH * 2, 0x00020000);
    const __amdgpu_buffer_rsrc_t rV = __builtin_amdgcn_make_buffer_rsrc((void*)(vc + (size_t)bh * SK * DH), 0, SK * DH * 2, 0x00020000);
    const int koff = tid * 16;
    v4u* lk = (v4u*)&lds[L_K] + w * (16 * NL * 64) + lane;
    unsigned t0;
    v4u stg[8], land[16 * NX], ring[16];
    static_for<0, 16 * (NL + NX + 1)>([&](auto N) {
      constexpr int n = N;
      if constexpr (n >= TLO && n < 16 * (NL + NX + 1) - 2) wait_vm<TLO - 1>();
      const v4u kv = __builtin_amdgcn_raw_buffer_load_b128(rK, koff, n * 4096, NT);
      if constexpr (n < 16 * NL) stg[n & 7] = kv;
      else if constexpr (n < 16 * (NL + NX)) land[n - 16 * NL] = kv;
      else ring[n & 15] = kv;
      if constexpr (n >= 6 && n - 6 < 16 * NL) lk[(n - 6) * 64] = stg[(n - 6) & 7];
      if constexpr (n == TLO - 1) t0 = (unsigned)__builtin_amdgcn_s_memrealtime();
      __builtin_amdgcn_sched_barrier(0);
    });
    if (lane == 0) { ((unsigned*)lds)[L_TS + w] = t0; ((unsigned*)lds)[L_AN] = t0 + TOFF; ((unsigned*)lds)[L_POS] = 0u; }
    lds_barrier();
    v4u qp;
    {
      f32x4 s0 = *(const f32x4*)&lds[L_S + 8 * c], s1 = *(const f32x4*)&lds[L_S + 8 * c + 4];
#pragma unroll
      for (int ww = 1; ww < 4; ++ww) { s0 += *(const f32x4*)&lds[L_S + ww * 128 + 8 * c]; s1 += *(const f32x4*)&lds[L_S + ww * 128 + 8 * c + 4]; }
      qp.x = bfbits(s0.x) | (bfbits(s0.y) << 16); qp.y = bfbits(s0.z) | (bfbits(s0.w) << 16);
      qp.z = bfbits(s1.x) | (bfbits(s1.y) << 16); qp.w = bfbits(s1.z) | (bfbits(s1.w) << 16);
    }
    auto pace = [&](int n) __attribute__((always_inline)) -> int {
      const unsigned anchor = lds_get(&lds[L_AN]);
      const unsigned v = (unsigned)__builtin_amdgcn_s_memrealtime() - (unsigned)((n * TAU256) >> 8);
      lds_put(&lds[L_TS + w], v);
      lds_put(&lds[L_POS], (unsigned)n);
      const int late = (int)(v - (unsigned)__builtin_amdgcn_readfirstlane(anchor));
      return late < 0 ? 0 : late < FAR ? 1 : 2;
    };
    float* scw = &lds[L_SC + w * 1024 + lane];
    float mrun = -__builtin_inff(), lrun = 0.f;
    float ra[4], rb[4];
    auto kstep = [&](auto J, const v4u& kv, float (&r)[4], int grp) __attribute__((always_inline)) {
      constexpr int j = J;
      float d = dot2(kv.x, qp.x, 0.f);
      d = dot2(kv.y, qp.y, d); d = dot2(kv.z, qp.z, d); d = dot2(kv.w, qp.w, d);
      d += dppf<XOR8>(d);
      if constexpr (!(j & 1)) { r[0] = d; asm volatile("" : "+v"(r[0])); return; }
      d = (c & 8) ? d : r[0];
      d += dppf<XOR7>(d);
      if constexpr (!(j & 2)) { r[1] = d; asm volatile("" : "+v"(r[1])); return; }
      d = (c & 4) ? d : r[1];
      d += dppf<XOR2>(d);
      if constexpr (!(j & 4)) { r[2] = d; asm volatile("" : "+v"(r[2])); return; }
      d = (c & 2) ? d : r[2];
      d += dppf<XOR1>(d);
      if constexpr (!(j & 8)) { r[3] = d; asm volatile("" : "+v"(r[3])); return; }
      d = (c & 1) ? d : r[3];
      const float s = rbf(rbf(d) * 0.08838834764831845f);
      scw[grp * 64] = s;
      const float mn = fmaxf(fmaxf(mrun, s), -3.4e38f);
      lrun = fmaf(lrun, __builtin_amdgcn_exp2f((mrun - mn) * L2E), __builtin_amdgcn_exp2f((s - mn) * L2E));
      mrun = mn;
      asm volatile("" : "+v"(mrun), "+v"(lrun));
    };
    static_for<0, 16 * NX>([&](auto M) {
      constexpr int m = M, i = m & 15, k = NL + NX + (m >> 4);
      kstep(std::integral_constant<int, i>{}, ring[i], ra, k);
      wait_vm<TLO - 1>();
      ring[i] = __builtin_amdgcn_raw_buffer_load_b128(rK, koff, (16 * (k + 1) + i) * 4096, NT);
      kstep(std::integral_constant<int, i>{}, land[m], rb, NL + (m >> 4));
      __builtin_amdgcn_sched_barrier(0);
    });
#pragma unroll 1
    for (int kk = 0; kk < NL; ++kk) {
      const int k = NL + 2 * NX + kk;
      int state = pace(16 * k + 16);
      if (kk == 0) state = 1;
      static_for<0, 16>([&](auto I) {
        constexpr int i = I;
        const v4u kv = lk[(kk * 16 + i) * 64];
        kstep(I, ring[i], ra, k);
        wait_var(state);
        ring[i] = __builtin_amdgcn_raw_buffer_load_b128(rK, koff, (k + 1) * 65536 + i * 4096, NT);
        kstep(I, kv, rb, kk);
        __builtin_amdgcn_sched_barrier(0);
      });
    }
#pragma unroll 1
    for (int k = 2 * (NL + NX); k < 16; ++k) {
      const bool last = k == 15;
      const __amdgpu_buffer_rsrc_t rN = last ? rV : rK;
      const int sbase = last ? 0 : (k + 1) * 65536;
      int state = last ? 3 : pace(16 * k + 16);
      if (k == 2 * (NL + NX)) state = 1;
      static_for<0, 16>([&](auto I) {
        constexpr int i = I;
        kstep(I, ring[i], ra, k);
        wait_var(state);
        ring[i] = __builtin_amdgcn_raw_buffer_load_b128(rN, koff, sbase + i * 4096, NT);
        __builtin_amdgcn_sched_barrier(0);
      });
    }
    {
      const float m = wave_max(mrun);
      const float l = wave_sum(lrun * __builtin_amdgcn_exp2f((mrun - m) * L2E));
      if (lane == 0) { lds[L_SM + 2 * w] = m; lds[L_SM + 2 * w + 1] = l; }
    }
    float snext = scw[0];
    const v4u mask = onehot_mask(c);
    lds_barrier();
    float mx, inv;
    {
      const f32x4 s0 = *(const f32x4*)&lds[L_SM], s1 = *(const f32x4*)&lds[L_SM + 4];
      mx = fmaxf(fmaxf(s0.x, s0.z), fmaxf(s1.x, s1.z));
      float l = s0.y * __builtin_amdgcn_exp2f((s0.x - mx) * L2E);
      l = fmaf(s0.w, __builtin_amdgcn_exp2f((s0.z - mx) * L2E), l);
      l = fmaf(s1.y, __builtin_amdgcn_exp2f((s1.x - mx) * L2E), l);
      l = fmaf(s1.w, __builtin_amdgcn_exp2f((s1.z - mx) * L2E), l);
      inv = 1.0f / l;
    }
    f32x4 oacc = {0.f, 0.f, 0.f, 0.f};
#pragma unroll 1
    for (int k = 0; k < 16; ++k) {
      const bool last = k == 15;
      const int voff = last ? OOB : koff;
      const int sbase = last ? 0 : (k + 1) * 65536;
      const int state = last ? 3 : k == 0 ? 0 : pace(256 + 16 * k + 16);
      unsigned pd = bfbits(__builtin_amdgcn_exp2f((snext - mx) * L2E) * inv);
      pd |= pd << 16;
      snext = scw[((k + 1) & 15) * 64];
      static_for<0, 16>([&](auto I) {
        constexpr int i = I;
        const v4u a = onehot<brev4(i)>(pd, mask);
        oacc = mfma16(a, ring[i], oacc);
        asm volatile("" : "+v"(oacc));
        wait_var(state);
        ring[i] = __builtin_amdgcn_raw_buffer_load_b128(rV, voff, sbase + i * 4096, NT);
        __builtin_amdgcn_sched_barrier(0);
      });
    }
    if (lane < 32) *(f32x4*)&lds[L_OP + w * 128 + 8 * c + 4 * g] = oacc;
    lds_barrier();
    if (w == 2) {
      const unsigned tag = ((const unsigned*)lds)[L_TAG];
      unsigned char* wsp = ws;
      asm volatile("" : "+s"(wsp));
      const __amdgpu_buffer_rsrc_t rP = __builtin_amdgcn_make_buffer_rsrc((void*)(wsp + WS_SG + (size_t)bh * 5120), 0, 5120, 0x00020000);
      asm volatile("" ::"s"(rP));
      lds_barrier();
      publish(rP, 256 + lane, tag);
    }
  }
}

extern "C" void kernel_launch(void* const* d_in, const int* in_sizes, int n_in, void* d_out, int out_size, void* d_ws,
                              size_t ws_size, hipStream_t stream) {
  fused_kernel<<<256, 512, 0, stream>>>((const u16*)d_in[0], (const u16*)d_in[1], (const u16*)d_in[2], (const u16*)d_in[3],
                                        (const u16*)d_in[4], (const u16*)d_in[6], (const u16*)d_in[7], (const u16*)d_in[8],
                                        (const u16*)d_in[9], (unsigned char*)d_ws, (u16*)d_out);
}
```

```cpp
#include <hip/hip_runtime.h>
#include <hip/hip_bf16.h>
#include <stdint.h>
#include <type_traits>

typedef unsigned v2u __attribute__((ext_vector_type(2)));
typedef unsigned v4u __attribute__((ext_vector_type(4)));
typedef unsigned v8u __attribute__((ext_vector_type(8)));
typedef float f32x2 __attribute__((ext_vector_type(2)));
typedef float f32x4 __attribute__((ext_vector_type(4)));
typedef __bf16 bf16x2 __attribute__((ext_vector_type(2)));
typedef __bf16 bf16x8 __attribute__((ext_vector_type(8)));
typedef __bf16 bf16x16 __attribute__((ext_vector_type(16)));
typedef unsigned short u16;
typedef __attribute__((address_space(1))) unsigned gu32;
typedef __attribute__((address_space(3))) unsigned lu32;
typedef __attribute__((address_space(3))) f32x2 lf32x2;
typedef __attribute__((address_space(3))) f32x4 lf32x4;

constexpr int D_ = 640, H_ = 32, SK = 4096, DH = 128;
constexpr size_t WS_SG = 0, WS_EPB = 256 * 5120, WS_AN = WS_EPB + 4096;
constexpr unsigned TAG_STEP = 0x9E3779B9u;

constexpr int TLO = 5;
constexpr int TAU256 = 3499;
constexpr int TOFF = 190;
constexpr int DELTA = 35;
constexpr int FAR = 70;
constexpr int NL = 2, NX = 2;
constexpr int WQ_INFL = 3, WO_INFL = 2;
constexpr int NT = 2, SC1 = 16, OOB = (int)0x80000000;

__device__ __forceinline__ float rbf(float x) { return (float)(__bf16)x; }
__device__ __forceinline__ unsigned bfbits(float x) { return (unsigned)__builtin_bit_cast(u16, (__bf16)x); }
__device__ __forceinline__ unsigned bfpack(f32x2 v) { return __builtin_bit_cast(unsigned, __builtin_convertvector(v, bf16x2)); }
__device__ __forceinline__ float bflo(unsigned u) { return __builtin_bit_cast(float, u << 16); }
__device__ __forceinline__ float bfhi(unsigned u) { return __builtin_bit_cast(float, u & 0xffff0000u); }

template <int CTRL> __device__ __forceinline__ float dppf(float x) {
  return __builtin_bit_cast(float, __builtin_amdgcn_mov_dpp(__builtin_bit_cast(int, x), CTRL, 0xf, 0xf, true));
}
template <int CTRL> __device__ __forceinline__ unsigned dppu(unsigned x) {
  return (unsigned)__builtin_amdgcn_mov_dpp((int)x, CTRL, 0xf, 0xf, true);
}
constexpr int XOR1 = 0xB1, XOR2 = 0x4E, XOR7 = 0x141, XOR8 = 0x128, NEWBCAST = 0x150;

__device__ __forceinline__ float swap16_sum(float x) {
  auto s = __builtin_amdgcn_permlane16_swap(__float_as_uint(x), __float_as_uint(x), false, false);
  return __uint_as_float(s[0]) + __uint_as_float(s[1]);
}
__device__ __forceinline__ float swap32_sum(float x) {
  auto t = __builtin_amdgcn_permlane32_swap(__float_as_uint(x), __float_as_uint(x), false, false);
  return __uint_as_float(t[0]) + __uint_as_float(t[1]);
}
__device__ __forceinline__ float wave_sum(float x) {
  x += dppf<XOR1>(x); x += dppf<XOR2>(x); x += dppf<XOR7>(x); x += dppf<XOR8>(x);
  return swap32_sum(swap16_sum(x));
}
__device__ __forceinline__ float wave_max(float x) {
  x = fmaxf(x, dppf<XOR1>(x)); x = fmaxf(x, dppf<XOR2>(x)); x = fmaxf(x, dppf<XOR7>(x)); x = fmaxf(x, dppf<XOR8>(x));
  auto s = __builtin_amdgcn_permlane16_swap(__float_as_uint(x), __float_as_uint(x), false, false);
  x = fmaxf(__uint_as_float(s[0]), __uint_as_float(s[1]));
  auto t = __builtin_amdgcn_permlane32_swap(__float_as_uint(x), __float_as_uint(x), false, false);
  return fmaxf(__uint_as_float(t[0]), __uint_as_float(t[1]));
}
__device__ __forceinline__ float rdlane(float x, int l) { return __builtin_bit_cast(float, __builtin_amdgcn_readlane(__builtin_bit_cast(int, x), l)); }
__device__ __forceinline__ float dot2(unsigned a, unsigned b, float acc) {
  return __builtin_amdgcn_fdot2_f32_bf16(__builtin_bit_cast(bf16x2, a), __builtin_bit_cast(bf16x2, b), acc, false);
}
__device__ __forceinline__ f32x4 mfma16(v4u a, v4u b, f32x4 c) {
  return __builtin_amdgcn_mfma_f32_16x16x32_bf16(__builtin_bit_cast(bf16x8, a), __builtin_bit_cast(bf16x8, b), c, 0, 0, 0);
}
__device__ __forceinline__ f32x4 smfma16(v4u a, v4u b0, v4u b1, f32x4 c, int idx) {
  const v8u b = __builtin_shufflevector(b0, b1, 0, 1, 2, 3, 4, 5, 6, 7);
  return __builtin_amdgcn_smfmac_f32_16x16x64_bf16(__builtin_bit_cast(bf16x8, a), __builtin_bit_cast(bf16x16, b), c, idx, 0, 0);
}
template <int J> __device__ __forceinline__ v4u onehot(unsigned val2, const v4u& mask) {
  const unsigned pb = dppu<NEWBCAST + J>(val2);
  v4u a;
  a.x = pb & mask.x; a.y = pb & mask.y; a.z = pb & mask.z; a.w = pb & mask.w;
  return a;
}
__device__ __forceinline__ v4u onehot_mask(int c) {
  const unsigned mm = (c & 1) ? 0xffff0000u : 0x0000ffffu;
  v4u mask;
  mask.x = (c >> 1) == 0 ? mm : 0u; mask.y = (c >> 1) == 1 ? mm : 0u; mask.z = (c >> 1) == 2 ? mm : 0u; mask.w = (c >> 1) == 3 ? mm : 0u;
  return mask;
}
__device__ __forceinline__ void lds_barrier() {
  asm volatile("s_waitcnt lgkmcnt(0)" ::: "memory");
  __builtin_amdgcn_s_barrier();
  asm volatile("" ::: "memory");
}
__device__ __forceinline__ unsigned lds_get(float* p) { return __hip_atomic_load((lu32*)p, __ATOMIC_RELAXED, __HIP_MEMORY_SCOPE_WORKGROUP); }
__device__ __forceinline__ void lds_put(float* p, unsigned v) { __hip_atomic_store((lu32*)p, v, __ATOMIC_RELAXED, __HIP_MEMORY_SCOPE_WORKGROUP); }
template <int N> __device__ __forceinline__ void wait_vm() { asm volatile("s_waitcnt vmcnt(%0)" ::"i"(N) : "memory"); }
__device__ __forceinline__ void wait_var(int state) {
  asm volatile("s_cmp_eq_u32 %0, 3\n\ts_cbranch_scc1 .Lwc%=\n\ts_waitcnt vmcnt(%3)\n\t"
               "s_cmp_eq_u32 %0, 2\n\ts_cbranch_scc1 .Lwc%=\n\ts_waitcnt vmcnt(%2)\n\t"
               "s_cmp_eq_u32 %0, 1\n\ts_cbranch_scc1 .Lwc%=\n\ts_waitcnt vmcnt(%1)\n.Lwc%=:"
               ::"s"(state), "i"(TLO - 1), "i"(TLO), "i"(TLO + 1) : "memory", "scc");
}
__device__ __forceinline__ void ln_stats(const float (&xv)[10], float& mb, float& sd) {
  float s = 0.f;
#pragma unroll
  for (int i = 0; i < 10; ++i) s += xv[i];
  const float mean = wave_sum(s) * (1.0f / D_);
  float q = 0.f;
#pragma unroll
  for (int i = 0; i < 10; ++i) { const float d = xv[i] - mean; q = fmaf(d, d, q); }
  const float var = rbf(wave_sum(q) * (1.0f / D_));
  mb = rbf(mean);
  sd = rbf(sqrtf(rbf(var + 1e-5f)));
}
__device__ __forceinline__ float ln_apply(float x, float mb, float sd) { return rbf(rbf(x - mb) / sd); }
__device__ __forceinline__ float gelu_bf(float u) {
  constexpr float K1 = -2.0f * 0.7978845608028654f * 1.4426950408889634f;
  constexpr float K2 = K1 * 0.044715f;
  const float e = __builtin_amdgcn_exp2f(u * fmaf(K2, u * u, K1));
  return rbf(u * __builtin_amdgcn_rcpf(1.0f + e));
}
template <int I, int N, typename Fn> __device__ __forceinline__ void static_for(Fn&& f) {
  if constexpr (I < N) { f(std::integral_constant<int, I>{}); static_for<I + 1, N>(f); }
}
constexpr int brev4(int j) { return ((j & 1) << 3) | ((j & 2) << 1) | ((j & 4) >> 1) | ((j & 8) >> 3); }

constexpr int L_S = 0;
constexpr int L_SM = 2560;
constexpr int L_TAG = 2568;
constexpr int L_CNT = 2569;
constexpr int L_AN = 2570;
constexpr int L_POS = 2571;
constexpr int L_TS = 2572;
constexpr int L_GATE = 2576;
constexpr int L_OP = 2704;
constexpr int L_SC = 3216;
constexpr int L_K = L_SC + 4096;
constexpr int L_END = L_K + 4 * 16 * NL * 256;

__global__ void __launch_bounds__(512, 2)
fused_kernel(const u16* __restrict__ x, const u16* __restrict__ kc, const u16* __restrict__ vc,
             const u16* __restrict__ wq, const u16* __restrict__ wo, const u16* __restrict__ w1, const u16* __restrict__ b1,
             const u16* __restrict__ w2, const u16* __restrict__ b2, unsigned char* ws, u16* out) {
  __shared__ __attribute__((aligned(16))) float lds[L_END];
  asm volatile("" ::"s"(x), "s"(kc), "s"(vc), "s"(wq));
  const int bh = blockIdx.x, b = bh >> 5, h = bh & 31;
  const int tid = threadIdx.x, lane = tid & 63, w = __builtin_amdgcn_readfirstlane(tid >> 6);
  const int g = lane >> 4, c = lane & 15;
  constexpr float L2E = 1.4426950408889634f;

  auto publish = [&](__amdgpu_buffer_rsrc_t rP, int t, unsigned tag) __attribute__((always_inline)) {
    const f32x2 sv = (*(const f32x2*)&lds[L_S + 2 * t] + *(const f32x2*)&lds[L_S + 640 + 2 * t]) +
                     (*(const f32x2*)&lds[L_S + 1280 + 2 * t] + *(const f32x2*)&lds[L_S + 1920 + 2 * t]);
    v4u gv; gv.x = __float_as_uint(sv.x); gv.y = tag; gv.z = __float_as_uint(sv.y); gv.w = tag;
    __builtin_amdgcn_raw_buffer_store_b128(gv, rP, t * 16, 0, SC1);
  };

  if (w >= 4) {
    __builtin_amdgcn_s_setprio(3);
    const int hw = w - 4, ht = tid - 256;
    gu32* epb = (gu32*)(ws + WS_EPB) + b * 64;
    const unsigned* x32 = (const unsigned*)(x + b * D_);
    const u16* xb = x + b * D_;
    unsigned xr[5];
#pragma unroll
    for (int i = 0; i < 5; ++i) xr[i] = x32[lane + 64 * i];
    const unsigned xo0 = xb[160 * hw + 4 * c + g], xo1 = xb[160 * hw + 64 + 4 * c + g], xo2 = xb[160 * hw + 128 + 4 * (c & 7) + g];
    __builtin_amdgcn_sched_barrier(0);
    const __amdgpu_buffer_rsrc_t rQ = __builtin_amdgcn_make_buffer_rsrc((void*)(wq + h * DH), 0, D_ * H_ * DH * 2 - h * DH * 2, 0x00020000);
    const int qoff = (160 * hw + g) * (H_ * DH * 2) + c * 16;
    v4u wr[40];
    static_for<0, 2 * WQ_INFL>([&](auto T) {
      constexpr int t = T;
      if constexpr (t >= WQ_INFL) wait_vm<WQ_INFL - 1>();
      wr[t] = __builtin_amdgcn_raw_buffer_load_b128(rQ, qoff, t * 4 * (H_ * DH * 2), 0);
      __builtin_amdgcn_sched_barrier(0);
    });
    const v4u mask = onehot_mask(c);
    float xv[10];
#pragma unroll
    for (int i = 0; i < 5; ++i) { xv[2 * i] = bflo(xr[i]); xv[2 * i + 1] = bfhi(xr[i]); }
    float mb, sd;
    ln_stats(xv, mb, sd);
    asm volatile("" : "+v"(mb), "+v"(sd));
    static_for<2 * WQ_INFL, 3 * WQ_INFL>([&](auto T) {
      constexpr int t = T;
      wait_vm<WQ_INFL - 1>();
      wr[t] = __builtin_amdgcn_raw_buffer_load_b128(rQ, qoff, t * 4 * (H_ * DH * 2), 0);
      __builtin_amdgcn_sched_barrier(0);
    });
    unsigned xn0 = bfbits(ln_apply(bflo(xo0), mb, sd)), xn1 = bfbits(ln_apply(bflo(xo1), mb, sd)), xn2 = bfbits(ln_apply(bflo(xo2), mb, sd));
    xn0 |= xn0 << 16; xn1 |= xn1 << 16; xn2 |= xn2 << 16;
    asm volatile("" : "+v"(xn0), "+v"(xn1), "+v"(xn2));
    {
      f32x4 acc = {0.f, 0.f, 0.f, 0.f};
      static_for<0, 40>([&](auto T) {
        constexpr int t = T, n = t + 3 * WQ_INFL;
        if constexpr (n < 40) {
          wait_vm<WQ_INFL - 1 + (n % 9 == 0 ? 1 : 0)>();
          wr[n] = __builtin_amdgcn_raw_buffer_load_b128(rQ, qoff, n * 4 * (H_ * DH * 2), 0);
        }
        const v4u a = onehot<(t & 15)>(t < 16 ? xn0 : t < 32 ? xn1 : xn2, mask);
        acc = mfma16(a, wr[t], acc);
        asm volatile("" : "+v"(acc));
        __builtin_amdgcn_sched_barrier(0);
      });
      if (lane < 32) *(f32x4*)&lds[L_S + hw * 128 + 8 * c + 4 * g] = acc;
    }
    lds_barrier();
    __builtin_amdgcn_sched_barrier(0);
    const int ncol = h < 8 ? 32 : 16;
    const unsigned xg = xb[16 * h + (lane & 15) + ((lane & 16) && h < 8 ? 512 : 0)];
    const int gf = ht % 192;
    const v4u gw1 = ((const v4u*)w1)[gf], gb1 = ((const v4u*)b1)[gf], gw2 = ((const v4u*)w2)[gf];
    const unsigned ew1 = w1[1536 + gf], eb1 = b1[1536 + gf], ew2 = w2[1536 + gf];
    const unsigned b2v = b2[0];
    const unsigned tag = __hip_atomic_load(epb, __ATOMIC_RELAXED, __HIP_MEMORY_SCOPE_AGENT) + TAG_STEP;
    const int cgrp = (hw == 1 && h < 8) ? 32 + h : h;
    const unsigned xres = x32[8 * cgrp + (lane & 7)];
    __builtin_amdgcn_sched_barrier(0);
    const __amdgpu_buffer_rsrc_t rO = __builtin_amdgcn_make_buffer_rsrc((void*)(wo + (size_t)h * DH * D_), 0, DH * D_ * 2, 0x00020000);
    const int ooff = (32 * hw + g) * (D_ * 2) + c * 16;
    static_for<0, 40>([&](auto I) {
      constexpr int i = I;
      if constexpr (i >= WO_INFL) wait_vm<WO_INFL - 1>();
      wr[i] = __builtin_amdgcn_raw_buffer_load_b128(rO, ooff, (i / 5) * 4 * (D_ * 2) + (i % 5) * 256, 0);
      __builtin_amdgcn_sched_barrier(0);
    });
    if (hw == 1) {
      const unsigned char* tp = lane == 0 ? (const unsigned char*)(vc + (size_t)bh * SK * DH)
                              : lane == 1 ? (const unsigned char*)(vc + (size_t)(bh + 1) * SK * DH) - 128
                              : lane == 2 ? (const unsigned char*)out
                              : lane == 3 ? ws + WS_SG + (size_t)b * 32 * 5120
                              : ws + WS_SG + (size_t)(b + 1) * 32 * 5120 - 16;
      const unsigned tv = __hip_atomic_load((gu32*)tp, __ATOMIC_RELAXED, __HIP_MEMORY_SCOPE_AGENT);
      asm volatile("" ::"v"(tv));
    }
    if (ht == 0) { ((unsigned*)lds)[L_TAG] = tag; ((unsigned*)lds)[L_CNT] = 0u; }
    auto pacer = [&](int round, unsigned pos) __attribute__((always_inline)) {
      const __amdgpu_buffer_rsrc_t rA = __builtin_amdgcn_make_buffer_rsrc((void*)(ws + WS_AN + round * 4096), 0, 4096, 0x00020000);
      unsigned at = 0;
      for (int it = 0; it < 4096; ++it) {
        at = __builtin_amdgcn_readfirstlane(lds_get(&lds[L_POS]));
        if (at >= pos) break;
        __builtin_amdgcn_s_sleep(8);
      }
      if (at > pos + 16u) return;
      __builtin_amdgcn_s_sleep(16);
      const unsigned tv = lds_get(&lds[L_TS + (lane & 3)]);
      const unsigned ref = __builtin_amdgcn_readfirstlane(tv);
      int d = (int)(tv - ref);
      d += (int)dppu<XOR1>((unsigned)d); d += (int)dppu<XOR2>((unsigned)d);
      const unsigned mine = ref + (unsigned)(d >> 2);
      v4u gv; gv.x = mine; gv.y = mine; gv.z = mine; gv.w = mine;
      __builtin_amdgcn_raw_buffer_store_b128(gv, rA, ((h >> 2) == b && lane < 8) ? h * 128 + lane * 16 : OOB, 0, SC1);
      __builtin_amdgcn_s_sleep(110);
      asm volatile("" ::: "memory");
      const int r = (int)(__builtin_amdgcn_raw_buffer_load_b32(rA, lane < 32 ? lane * 128 : OOB, 0, SC1) - mine);
      const bool ok = lane < 32 && r > -1000 && r < 1000;
      const int cnt = __builtin_popcountll(__builtin_amdgcn_ballot_w64(ok));
      const int mean = (int)(wave_sum(ok ? (float)r : 0.f) / (float)(cnt > 0 ? cnt : 1));
      if (cnt >= 16) lds_put(&lds[L_AN], mine + (unsigned)(mean + DELTA));
    };
    if (hw == 3) pacer(0, 208);
    lds_barrier();
    if (hw == 3) {
#pragma unroll 1
      for (int r = 1; r < 3; ++r) pacer(r, r == 1 ? 304 : 400);
    }
    __builtin_amdgcn_s_setprio(0);
    if (hw < 3) {
      float mb2, sd2;
#pragma unroll
      for (int i = 0; i < 10; ++i) xv[i] = ln_apply(xv[i], mb, sd);
      ln_stats(xv, mb2, sd2);
      const float hcol = ln_apply(ln_apply(bflo(xg), mb, sd), mb2, sd2);
      float w1v[9], b1v[9], w2v[9];
#pragma unroll
      for (int i = 0; i < 4; ++i) {
        w1v[2 * i] = bflo(gw1[i]); w1v[2 * i + 1] = bfhi(gw1[i]);
        b1v[2 * i] = bflo(gb1[i]); b1v[2 * i + 1] = bfhi(gb1[i]);
        w2v[2 * i] = bflo(gw2[i]); w2v[2 * i + 1] = bfhi(gw2[i]);
      }
      w1v[8] = bflo(ew1); b1v[8] = bflo(eb1); w2v[8] = bflo(ew2);
      float ycol = 0.f;
#pragma unroll 1
      for (int j = 0; j < ncol; ++j) {
        const float hv = rdlane(hcol, j);
        float y = 0.f;
#pragma unroll
        for (int i = 0; i < 9; ++i) y = fmaf(gelu_bf(rbf(rbf(hv * w1v[i]) + b1v[i])), w2v[i], y);
        y = wave_sum(y);
        ycol = (lane == j) ? y : ycol;
      }
      if (lane < 32) lds[L_GATE + hw * 32 + lane] = ycol;
      asm volatile("s_waitcnt lgkmcnt(0)" ::: "memory");
      if (lane == 0) __hip_atomic_fetch_add((lu32*)&lds[L_CNT], 1u, __ATOMIC_RELAXED, __HIP_MEMORY_SCOPE_WORKGROUP);
      while (lds_get(&lds[L_CNT]) < 3u) __builtin_amdgcn_s_sleep(2);
      asm volatile("" ::: "memory");
    }
    __builtin_amdgcn_s_setprio(3);
    float gt0, gt1;
    {
      const int cc = (cgrp >= 32 ? 16 : 0) + 2 * (lane & 7);
      float y0 = (lds[L_GATE + cc] + lds[L_GATE + 32 + cc]) + lds[L_GATE + 64 + cc];
      float y1 = (lds[L_GATE + cc + 1] + lds[L_GATE + 33 + cc]) + lds[L_GATE + 65 + cc];
      y0 = rbf(rbf(y0) + bflo(b2v)); y1 = rbf(rbf(y1) + bflo(b2v));
      gt0 = rbf(y0 / (1.0f + __expf(-y0))); gt1 = rbf(y1 / (1.0f + __expf(-y1)));
    }
    int goff = (lane >> 3) * 5120 + cgrp * 128 + (lane & 7) * 16;
    float xr0 = bflo(xres), xr1 = bfhi(xres);
    v4u smask;
    smask.x = (c >> 2) == 0 ? 0x0000ffffu : 0u; smask.y = (c >> 2) == 1 ? 0x0000ffffu : 0u;
    smask.z = smask.x << 16; smask.w = smask.y << 16;
    int sidx = (c & 3) * 0x5555;
    lf32x2* op = (lf32x2*)&lds[L_OP + 32 * hw + 8 * (c & 3) + 2 * g];
    lf32x4* sp = (lf32x4*)&lds[L_S + hw * 640 + 8 * c + 4 * (g & 1)];
    f32x4 sacc[5];
#pragma unroll
    for (int u = 0; u < 5; ++u) { sacc[u] = f32x4{0.f, 0.f, 0.f, 0.f}; asm volatile("" : "+v"(sacc[u])); }
    asm volatile("" : "+v"(gt0), "+v"(gt1), "+v"(goff), "+v"(smask), "+v"(xr0), "+v"(xr1), "+v"(sidx), "+v"(op), "+v"(sp));
    const __amdgpu_buffer_rsrc_t rG = __builtin_amdgcn_make_buffer_rsrc((void*)(ws + WS_SG + (size_t)b * 32 * 5120), 0, 32 * 5120, 0x00020000);
    const __amdgpu_buffer_rsrc_t rOut = __builtin_amdgcn_make_buffer_rsrc((void*)(out + b * D_ + 16 * cgrp), 0, 32, 0x00020000);
    const __amdgpu_buffer_rsrc_t rP = __builtin_amdgcn_make_buffer_rsrc((void*)(ws + WS_SG + (size_t)bh * 5120), 0, 5120, 0x00020000);
    asm volatile("" ::"s"(rG), "s"(rOut), "s"(rP));
    __builtin_amdgcn_s_waitcnt(0x0F70);
    lds_barrier();
    const unsigned od = bfpack((op[0] + op[64]) + (op[128] + op[192]));
    static_for<0, 4>([&](auto T) {
      constexpr int t = T;
      const v4u a = onehot<t>(od, smask);
#pragma unroll
      for (int u = 0; u < 5; ++u) sacc[u] = smfma16(a, wr[10 * t + u], wr[10 * t + 5 + u], sacc[u], sidx);
    });
    if (lane < 32) {
#pragma unroll
      for (int u = 0; u < 5; ++u) sp[u * 32] = sacc[u];
    }
    lds_barrier();
    publish(rP, ht, tag);
    if (hw == 0 || cgrp >= 32) {
      v4u gr[4];
      __builtin_amdgcn_s_sleep(22);
#pragma unroll 1
      for (int spin = 0; spin < (1 << 17); ++spin) {
        asm volatile("" : "+v"(goff)::"memory");
#pragma unroll
        for (int i = 0; i < 4; ++i) gr[i] = __builtin_amdgcn_raw_buffer_load_b128(rG, goff, i * 8 * 5120, SC1);
        unsigned bad = 0;
#pragma unroll
        for (int i = 0; i < 4; ++i) bad |= (gr[i].y ^ tag) | (gr[i].w ^ tag);
        asm volatile("" : "+v"(bad));
        if (__all(bad == 0u)) break;
        __builtin_amdgcn_s_sleep(1);
      }
      float s0 = (__uint_as_float(gr[0].x) + __uint_as_float(gr[1].x)) + (__uint_as_float(gr[2].x) + __uint_as_float(gr[3].x));
      float s1 = (__uint_as_float(gr[0].z) + __uint_as_float(gr[1].z)) + (__uint_as_float(gr[2].z) + __uint_as_float(gr[3].z));
      s0 += dppf<XOR8>(s0); s1 += dppf<XOR8>(s1);
      s0 = swap32_sum(swap16_sum(s0)); s1 = swap32_sum(swap16_sum(s1));
      const unsigned o0 = bfbits(xr0 + rbf(rbf(s0) * gt0)), o1 = bfbits(xr1 + rbf(rbf(s1) * gt1));
      __builtin_amdgcn_raw_buffer_store_b32(o0 | (o1 << 16), rOut, lane * 4, 0, SC1);
      if (cgrp == 0 && lane == 0) __hip_atomic_store(epb, tag, __ATOMIC_RELAXED, __HIP_MEMORY_SCOPE_AGENT);
    }
    return;
  }

  {
    __builtin_amdgcn_s_setprio(1);
    const __amdgpu_buffer_rsrc_t rK = __builtin_amdgcn_make_buffer_rsrc((void*)(kc + (size_t)bh * SK * DH), 0, SK * DH * 2, 0x00020000);
    const __amdgpu_buffer_rsrc_t rV = __builtin_amdgcn_make_buffer_rsrc((void*)(vc + (size_t)bh * SK * DH), 0, SK * DH * 2, 0x00020000);
    const int koff = tid * 16;
    v4u* lk = (v4u*)&lds[L_K] + w * (16 * NL * 64) + lane;
    unsigned t0;
    v4u stg[8], land[16 * NX], ring[16];
    static_for<0, 16 * (NL + NX + 1)>([&](auto N) {
      constexpr int n = N;
      if constexpr (n >= TLO && n < 16 * (NL + NX + 1) - 2) wait_vm<TLO - 1>();
      const v4u kv = __builtin_amdgcn_raw_buffer_load_b128(rK, koff, n * 4096, NT);
      if constexpr (n < 16 * NL) stg[n & 7] = kv;
      else if constexpr (n < 16 * (NL + NX)) land[n - 16 * NL] = kv;
      else ring[n & 15] = kv;
      if constexpr (n >= 6 && n - 6 < 16 * NL) lk[(n - 6) * 64] = stg[(n - 6) & 7];
      if constexpr (n == TLO - 1) t0 = (unsigned)__builtin_amdgcn_s_memrealtime();
      __builtin_amdgcn_sched_barrier(0);
    });
    if (lane == 0) { ((unsigned*)lds)[L_TS + w] = t0; ((unsigned*)lds)[L_AN] = t0 + TOFF; ((unsigned*)lds)[L_POS] = 0u; }
    lds_barrier();
    v4u qp;
    {
      f32x4 s0 = *(const f32x4*)&lds[L_S + 8 * c], s1 = *(const f32x4*)&lds[L_S + 8 * c + 4];
#pragma unroll
      for (int ww = 1; ww < 4; ++ww) { s0 += *(const f32x4*)&lds[L_S + ww * 128 + 8 * c]; s1 += *(const f32x4*)&lds[L_S + ww * 128 + 8 * c + 4]; }
      qp.x = bfbits(s0.x) | (bfbits(s0.y) << 16); qp.y = bfbits(s0.z) | (bfbits(s0.w) << 16);
      qp.z = bfbits(s1.x) | (bfbits(s1.y) << 16); qp.w = bfbits(s1.z) | (bfbits(s1.w) << 16);
    }
    auto pace = [&](int n) __attribute__((always_inline)) -> int {
      const unsigned anchor = lds_get(&lds[L_AN]);
      const unsigned v = (unsigned)__builtin_amdgcn_s_memrealtime() - (unsigned)((n * TAU256) >> 8);
      lds_put(&lds[L_TS + w], v);
      lds_put(&lds[L_POS], (unsigned)n);
      const int late = (int)(v - (unsigned)__builtin_amdgcn_readfirstlane(anchor));
      return late < 0 ? 0 : late < FAR ? 1 : 2;
    };
    float* scw = &lds[L_SC + w * 1024 + lane];
    float mrun = -__builtin_inff(), lrun = 0.f;
    float ra[4], rb[4];
    auto kstep = [&](auto J, const v4u& kv, float (&r)[4], int grp) __attribute__((always_inline)) {
      constexpr int j = J;
      float d = dot2(kv.x, qp.x, 0.f);
      d = dot2(kv.y, qp.y, d); d = dot2(kv.z, qp.z, d); d = dot2(kv.w, qp.w, d);
      d += dppf<XOR8>(d);
      if constexpr (!(j & 1)) { r[0] = d; asm volatile("" : "+v"(r[0])); return; }
      d = (c & 8) ? d : r[0];
      d += dppf<XOR7>(d);
      if constexpr (!(j & 2)) { r[1] = d; asm volatile("" : "+v"(r[1])); return; }
      d = (c & 4) ? d : r[1];
      d += dppf<XOR2>(d);
      if constexpr (!(j & 4)) { r[2] = d; asm volatile("" : "+v"(r[2])); return; }
      d = (c & 2) ? d : r[2];
      d += dppf<XOR1>(d);
      if constexpr (!(j & 8)) { r[3] = d; asm volatile("" : "+v"(r[3])); return; }
      d = (c & 1) ? d : r[3];
      const float s = rbf(rbf(d) * 0.08838834764831845f);
      scw[grp * 64] = s;
      const float mn = fmaxf(fmaxf(mrun, s), -3.4e38f);
      lrun = fmaf(lrun, __builtin_amdgcn_exp2f((mrun - mn) * L2E), __builtin_amdgcn_exp2f((s - mn) * L2E));
      mrun = mn;
      asm volatile("" : "+v"(mrun), "+v"(lrun));
    };
    static_for<0, 16 * NX>([&](auto M) {
      constexpr int m = M, i = m & 15, k = NL + NX + (m >> 4);
      kstep(std::integral_constant<int, i>{}, ring[i], ra, k);
      wait_vm<TLO - 1>();
      ring[i] = __builtin_amdgcn_raw_buffer_load_b128(rK, koff, (16 * (k + 1) + i) * 4096, NT);
      kstep(std::integral_constant<int, i>{}, land[m], rb, NL + (m >> 4));
      __builtin_amdgcn_sched_barrier(0);
    });
#pragma unroll 1
    for (int kk = 0; kk < NL; ++kk) {
      const int k = NL + 2 * NX + kk;
      int state = pace(16 * k + 16);
      if (kk == 0) state = 1;
      static_for<0, 16>([&](auto I) {
        constexpr int i = I;
        const v4u kv = lk[(kk * 16 + i) * 64];
        kstep(I, ring[i], ra, k);
        wait_var(state);
        ring[i] = __builtin_amdgcn_raw_buffer_load_b128(rK, koff, (k + 1) * 65536 + i * 4096, NT);
        kstep(I, kv, rb, kk);
        __builtin_amdgcn_sched_barrier(0);
      });
    }
#pragma unroll 1
    for (int k = 2 * (NL + NX); k < 16; ++k) {
      const bool last = k == 15;
      const __amdgpu_buffer_rsrc_t rN = last ? rV : rK;
      const int sbase = last ? 0 : (k + 1) * 65536;
      int state = last ? 3 : pace(16 * k + 16);
      if (k == 2 * (NL + NX)) state = 1;
      static_for<0, 16>([&](auto I) {
        constexpr int i = I;
        kstep(I, ring[i], ra, k);
        wait_var(state);
        ring[i] = __builtin_amdgcn_raw_buffer_load_b128(rN, koff, sbase + i * 4096, NT);
        __builtin_amdgcn_sched_barrier(0);
      });
    }
    {
      const float m = wave_max(mrun);
      const float l = wave_sum(lrun * __builtin_amdgcn_exp2f((mrun - m) * L2E));
      if (lane == 0) { lds[L_SM + 2 * w] = m; lds[L_SM + 2 * w + 1] = l; }
    }
    float snext = scw[0];
    const v4u mask = onehot_mask(c);
    lds_barrier();
    float mx, inv;
    {
      const f32x4 s0 = *(const f32x4*)&lds[L_SM], s1 = *(const f32x4*)&lds[L_SM + 4];
      mx = fmaxf(fmaxf(s0.x, s0.z), fmaxf(s1.x, s1.z));
      float l = s0.y * __builtin_amdgcn_exp2f((s0.x - mx) * L2E);
      l = fmaf(s0.w, __builtin_amdgcn_exp2f((s0.z - mx) * L2E), l);
      l = fmaf(s1.y, __builtin_amdgcn_exp2f((s1.x - mx) * L2E), l);
      l = fmaf(s1.w, __builtin_amdgcn_exp2f((s1.z - mx) * L2E), l);
      inv = 1.0f / l;
    }
    f32x4 oacc = {0.f, 0.f, 0.f, 0.f};
#pragma unroll 1
    for (int k = 0; k < 16; ++k) {
      const bool last = k == 15;
      const int voff = last ? OOB : koff;
      const int sbase = last ? 0 : (k + 1) * 65536;
      const int state = last ? 3 : k == 0 ? 0 : pace(256 + 16 * k + 16);
      unsigned pd = bfbits(__builtin_amdgcn_exp2f((snext - mx) * L2E) * inv);
      pd |= pd << 16;
      snext = scw[((k + 1) & 15) * 64];
      static_for<0, 16>([&](auto I) {
        constexpr int i = I;
        const v4u a = onehot<brev4(i)>(pd, mask);
        oacc = mfma16(a, ring[i], oacc);
        asm volatile("" : "+v"(oacc));
        wait_var(state);
        ring[i] = __builtin_amdgcn_raw_buffer_load_b128(rV, voff, sbase + i * 4096, NT);
        __builtin_amdgcn_sched_barrier(0);
      });
    }
    if (lane < 32) *(f32x4*)&lds[L_OP + w * 128 + 8 * c + 4 * g] = oacc;
    lds_barrier();
    if (w == 2) {
      const unsigned tag = ((const unsigned*)lds)[L_TAG];
      unsigned char* wsp = ws;
      asm volatile("" : "+s"(wsp));
      const __amdgpu_buffer_rsrc_t rP = __builtin_amdgcn_make_buffer_rsrc((void*)(wsp + WS_SG + (size_t)bh * 5120), 0, 5120, 0x00020000);
      asm volatile("" ::"s"(rP));
      lds_barrier();
      publish(rP, 256 + lane, tag);
    }
  }
}

extern "C" void kernel_launch(void* const* d_in, const int* in_sizes, int n_in, void* d_out, int out_size, void* d_ws,
                              size_t ws_size, hipStream_t stream) {
  fused_kernel<<<256, 512, 0, stream>>>((const u16*)d_in[0], (const u16*)d_in[1], (const u16*)d_in[2], (const u16*)d_in[3],
                                        (const u16*)d_in[4], (const u16*)d_in[6], (const u16*)d_in[7], (const u16*)d_in[8],
                                        (const u16*)d_in[9], (unsigned char*)d_ws, (u16*)d_out);
}
```

```cpp
#include <hip/hip_runtime.h>
#include <hip/hip_bf16.h>
#include <stdint.h>
#include <type_traits>

typedef unsigned v2u __attribute__((ext_vector_type(2)));
typedef unsigned v4u __attribute__((ext_vector_type(4)));
typedef unsigned v8u __attribute__((ext_vector_type(8)));
typedef float f32x2 __attribute__((ext_vector_type(2)));
typedef float f32x4 __attribute__((ext_vector_type(4)));
typedef __bf16 bf16x2 __attribute__((ext_vector_type(2)));
typedef __bf16 bf16x8 __attribute__((ext_vector_type(8)));
typedef __bf16 bf16x16 __attribute__((ext_vector_type(16)));
typedef unsigned short u16;
typedef __attribute__((address_space(1))) unsigned gu32;
typedef __attribute__((address_space(3))) unsigned lu32;
typedef __attribute__((address_space(3))) f32x2 lf32x2;
typedef __attribute__((address_space(3))) f32x4 lf32x4;

constexpr int D_ = 640, H_ = 32, SK = 4096, DH = 128;
constexpr size_t WS_SG = 0, WS_EPB = 256 * 5120, WS_AN = WS_EPB + 4096;
constexpr unsigned TAG_STEP = 0x9E3779B9u;

constexpr int TLO = 5;
constexpr int TAU256 = 3499;
constexpr int TOFF = 190;
constexpr int DELTA = 35;
constexpr int FAR = 70;
constexpr int NL = 2, NX = 2;
constexpr int WQ_INFL = 3, WO_INFL = 2;
constexpr int NT = 2, SC1 = 16, OOB = (int)0x80000000;

__device__ __forceinline__ float rbf(float x) { return (float)(__bf16)x; }
__device__ __forceinline__ unsigned bfbits(float x) { return (unsigned)__builtin_bit_cast(u16, (__bf16)x); }
__device__ __forceinline__ unsigned bfpack(f32x2 v) { return __builtin_bit_cast(unsigned, __builtin_convertvector(v, bf16x2)); }
__device__ __forceinline__ float bflo(unsigned u) { return __builtin_bit_cast(float, u << 16); }
__device__ __forceinline__ float bfhi(unsigned u) { return __builtin_bit_cast(float, u & 0xffff0000u); }

template <int CTRL> __device__ __forceinline__ float dppf(float x) {
  return __builtin_bit_cast(float, __builtin_amdgcn_mov_dpp(__builtin_bit_cast(int, x), CTRL, 0xf, 0xf, true));
}
template <int CTRL> __device__ __forceinline__ unsigned dppu(unsigned x) {
  return (unsigned)__builtin_amdgcn_mov_dpp((int)x, CTRL, 0xf, 0xf, true);
}
constexpr int XOR1 = 0xB1, XOR2 = 0x4E, XOR7 = 0x141, XOR8 = 0x128, NEWBCAST = 0x150;

__device__ __forceinline__ float swap16_sum(float x) {
  auto s = __builtin_amdgcn_permlane16_swap(__float_as_uint(x), __float_as_uint(x), false, false);
  return __uint_as_float(s[0]) + __uint_as_float(s[1]);
}
__device__ __forceinline__ float swap32_sum(float x) {
  auto t = __builtin_amdgcn_permlane32_swap(__float_as_uint(x), __float_as_uint(x), false, false);
  return __uint_as_float(t[0]) + __uint_as_float(t[1]);
}
__device__ __forceinline__ float wave_sum(float x) {
  x += dppf<XOR1>(x); x += dppf<XOR2>(x); x += dppf<XOR7>(x); x += dppf<XOR8>(x);
  return swap32_sum(swap16_sum(x));
}
__device__ __forceinline__ float wave_max(float x) {
  x = fmaxf(x, dppf<XOR1>(x)); x = fmaxf(x, dppf<XOR2>(x)); x = fmaxf(x, dppf<XOR7>(x)); x = fmaxf(x, dppf<XOR8>(x));
  auto s = __builtin_amdgcn_permlane16_swap(__float_as_uint(x), __float_as_uint(x), false, false);
  x = fmaxf(__uint_as_float(s[0]), __uint_as_float(s[1]));
  auto t = __builtin_amdgcn_permlane32_swap(__float_as_uint(x), __float_as_uint(x), false, false);
  return fmaxf(__uint_as_float(t[0]), __uint_as_float(t[1]));
}
__device__ __forceinline__ float rdlane(float x, int l) { return __builtin_bit_cast(float, __builtin_amdgcn_readlane(__builtin_bit_cast(int, x), l)); }
__device__ __forceinline__ float dot2(unsigned a, unsigned b, float acc) {
  return __builtin_amdgcn_fdot2_f32_bf16(__builtin_bit_cast(bf16x2, a), __builtin_bit_cast(bf16x2, b), acc, false);
}
__device__ __forceinline__ f32x4 mfma16(v4u a, v4u b, f32x4 c) {
  return __builtin_amdgcn_mfma_f32_16x16x32_bf16(__builtin_bit_cast(bf16x8, a), __builtin_bit_cast(bf16x8, b), c, 0, 0, 0);
}
__device__ __forceinline__ f32x4 smfma16(v4u a, v4u b0, v4u b1, f32x4 c, int idx) {
  const v8u b = __builtin_shufflevector(b0, b1, 0, 1, 2, 3, 4, 5, 6, 7);
  return __builtin_amdgcn_smfmac_f32_16x16x64_bf16(__builtin_bit_cast(bf16x8, a), __builtin_bit_cast(bf16x16, b), c, idx, 0, 0);
}
template <int J> __device__ __forceinline__ v4u onehot(unsigned val2, const v4u& mask) {
  const unsigned pb = dppu<NEWBCAST + J>(val2);
  v4u a;
  a.x = pb & mask.x; a.y = pb & mask.y; a.z = pb & mask.z; a.w = pb & mask.w;
  return a;
}
__device__ __forceinline__ v4u onehot_mask(int c) {
  const unsigned mm = (c & 1) ? 0xffff0000u : 0x0000ffffu;
  v4u mask;
  mask.x = (c >> 1) == 0 ? mm : 0u; mask.y = (c >> 1) == 1 ? mm : 0u; mask.z = (c >> 1) == 2 ? mm : 0u; mask.w = (c >> 1) == 3 ? mm : 0u;
  return mask;
}
__device__ __forceinline__ void lds_barrier() {
  asm volatile("s_waitcnt lgkmcnt(0)" ::: "memory");
  __builtin_amdgcn_s_barrier();
  asm volatile("" ::: "memory");
}
__device__ __forceinline__ unsigned lds_get(float* p) { return __hip_atomic_load((lu32*)p, __ATOMIC_RELAXED, __HIP_MEMORY_SCOPE_WORKGROUP); }
__device__ __forceinline__ void lds_put(float* p, unsigned v) { __hip_atomic_store((lu32*)p, v, __ATOMIC_RELAXED, __HIP_MEMORY_SCOPE_WORKGROUP); }
template <int N> __device__ __forceinline__ void wait_vm() { asm volatile("s_waitcnt vmcnt(%0)" ::"i"(N) : "memory"); }
__device__ __forceinline__ void wait_var(int state) {
  asm volatile("s_cmp_eq_u32 %0, 3\n\ts_cbranch_scc1 .Lwc%=\n\ts_waitcnt vmcnt(%3)\n\t"
               "s_cmp_eq_u32 %0, 2\n\ts_cbranch_scc1 .Lwc%=\n\ts_waitcnt vmcnt(%2)\n\t"
               "s_cmp_eq_u32 %0, 1\n\ts_cbranch_scc1 .Lwc%=\n\ts_waitcnt vmcnt(%1)\n.Lwc%=:"
               ::"s"(state), "i"(TLO - 1), "i"(TLO), "i"(TLO + 1) : "memory", "scc");
}
__device__ __forceinline__ void ln_stats(const float (&xv)[10], float& mb, float& sd) {
  float s = 0.f;
#pragma unroll
  for (int i = 0; i < 10; ++i) s += xv[i];
  const float mean = wave_sum(s) * (1.0f / D_);
  float q = 0.f;
#pragma unroll
  for (int i = 0; i < 10; ++i) { const float d = xv[i] - mean; q = fmaf(d, d, q); }
  const float var = rbf(wave_sum(q) * (1.0f / D_));
  mb = rbf(mean);
  sd = rbf(sqrtf(rbf(var + 1e-5f)));
}
__device__ __forceinline__ float ln_apply(float x, float mb, float sd) { return rbf(rbf(x - mb) / sd); }
__device__ __forceinline__ float gelu_bf(float u) {
  constexpr float K1 = -2.0f * 0.7978845608028654f * 1.4426950408889634f;
  constexpr float K2 = K1 * 0.044715f;
  const float e = __builtin_amdgcn_exp2f(u * fmaf(K2, u * u, K1));
  return rbf(u * __builtin_amdgcn_rcpf(1.0f + e));
}
template <int I, int N, typename Fn> __device__ __forceinline__ void static_for(Fn&& f) {
  if constexpr (I < N) { f(std::integral_constant<int, I>{}); static_for<I + 1, N>(f); }
}
constexpr int CL = 3;
constexpr int soff(int n) { return (n >> CL) * (4096 << CL) + (n & ((1 << CL) - 1)) * 1024; }
constexpr int brev4(int j) { return ((j & 1) << 3) | ((j & 2) << 1) | ((j & 4) >> 1) | ((j & 8) >> 3); }

constexpr int L_S = 0;
constexpr int L_SM = 2560;
constexpr int L_TAG = 2568;
constexpr int L_CNT = 2569;
constexpr int L_AN = 2570;
constexpr int L_POS = 2571;
constexpr int L_TS = 2572;
constexpr int L_GATE = 2576;
constexpr int L_OP = 2704;
constexpr int L_SC = 3216;
constexpr int L_K = L_SC + 4096;
constexpr int L_END = L_K + 4 * 16 * NL * 256;

__global__ void __launch_bounds__(512, 2)
fused_kernel(const u16* __restrict__ x, const u16* __restrict__ kc, const u16* __restrict__ vc,
             const u16* __restrict__ wq, const u16* __restrict__ wo, const u16* __restrict__ w1, const u16* __restrict__ b1,
             const u16* __restrict__ w2, const u16* __restrict__ b2, unsigned char* ws, u16* out) {
  __shared__ __attribute__((aligned(16))) float lds[L_END];
  asm volatile("" ::"s"(x), "s"(kc), "s"(vc), "s"(wq));
  const int bh = blockIdx.x, b = bh >> 5, h = bh & 31;
  const int tid = threadIdx.x, lane = tid & 63, w = __builtin_amdgcn_readfirstlane(tid >> 6);
  const int g = lane >> 4, c = lane & 15;
  constexpr float L2E = 1.4426950408889634f;

  auto publish = [&](__amdgpu_buffer_rsrc_t rP, int t, unsigned tag) __attribute__((always_inline)) {
    const f32x2 sv = (*(const f32x2*)&lds[L_S + 2 * t] + *(const f32x2*)&lds[L_S + 640 + 2 * t]) +
                     (*(const f32x2*)&lds[L_S + 1280 + 2 * t] + *(const f32x2*)&lds[L_S + 1920 + 2 * t]);
    v4u gv; gv.x = __float_as_uint(sv.x); gv.y = tag; gv.z = __float_as_uint(sv.y); gv.w = tag;
    __builtin_amdgcn_raw_buffer_store_b128(gv, rP, t * 16, 0, SC1);
  };

  if (w >= 4) {
    __builtin_amdgcn_s_setprio(3);
    const int hw = w - 4, ht = tid - 256;
    gu32* epb = (gu32*)(ws + WS_EPB) + b * 64;
    const unsigned* x32 = (const unsigned*)(x + b * D_);
    const u16* xb = x + b * D_;
    unsigned xr[5];
#pragma unroll
    for (int i = 0; i < 5; ++i) xr[i] = x32[lane + 64 * i];
    const unsigned xo0 = xb[160 * hw + 4 * c + g], xo1 = xb[160 * hw + 64 + 4 * c + g], xo2 = xb[160 * hw + 128 + 4 * (c & 7) + g];
    __builtin_amdgcn_sched_barrier(0);
    const __amdgpu_buffer_rsrc_t rQ = __builtin_amdgcn_make_buffer_rsrc((void*)(wq + h * DH), 0, D_ * H_ * DH * 2 - h * DH * 2, 0x00020000);
    const int qoff = (160 * hw + g) * (H_ * DH * 2) + c * 16;
    v4u wr[40];
    static_for<0, 2 * WQ_INFL>([&](auto T) {
      constexpr int t = T;
      if constexpr (t >= WQ_INFL) wait_vm<WQ_INFL - 1>();
      wr[t] = __builtin_amdgcn_raw_buffer_load_b128(rQ, qoff, t * 4 * (H_ * DH * 2), 0);
      __builtin_amdgcn_sched_barrier(0);
    });
    const v4u mask = onehot_mask(c);
    float xv[10];
#pragma unroll
    for (int i = 0; i < 5; ++i) { xv[2 * i] = bflo(xr[i]); xv[2 * i + 1] = bfhi(xr[i]); }
    float mb, sd;
    ln_stats(xv, mb, sd);
    asm volatile("" : "+v"(mb), "+v"(sd));
    static_for<2 * WQ_INFL, 3 * WQ_INFL>([&](auto T) {
      constexpr int t = T;
      wait_vm<WQ_INFL - 1>();
      wr[t] = __builtin_amdgcn_raw_buffer_load_b128(rQ, qoff, t * 4 * (H_ * DH * 2), 0);
      __builtin_amdgcn_sched_barrier(0);
    });
    unsigned xn0 = bfbits(ln_apply(bflo(xo0), mb, sd)), xn1 = bfbits(ln_apply(bflo(xo1), mb, sd)), xn2 = bfbits(ln_apply(bflo(xo2), mb, sd));
    xn0 |= xn0 << 16; xn1 |= xn1 << 16; xn2 |= xn2 << 16;
    asm volatile("" : "+v"(xn0), "+v"(xn1), "+v"(xn2));
    {
      f32x4 acc = {0.f, 0.f, 0.f, 0.f};
      static_for<0, 40>([&](auto T) {
        constexpr int t = T, n = t + 3 * WQ_INFL;
        if constexpr (n < 40) {
          wait_vm<WQ_INFL - 1 + (n % 9 == 0 ? 1 : 0)>();
          wr[n] = __builtin_amdgcn_raw_buffer_load_b128(rQ, qoff, n * 4 * (H_ * DH * 2), 0);
        }
        const v4u a = onehot<(t & 15)>(t < 16 ? xn0 : t < 32 ? xn1 : xn2, mask);
        acc = mfma16(a, wr[t], acc);
        asm volatile("" : "+v"(acc));
        __builtin_amdgcn_sched_barrier(0);
      });
      if (lane < 32) *(f32x4*)&lds[L_S + hw * 128 + 8 * c + 4 * g] = acc;
    }
    lds_barrier();
    __builtin_amdgcn_sched_barrier(0);
    const int ncol = h < 8 ? 32 : 16;
    const unsigned xg = xb[16 * h + (lane & 15) + ((lane & 16) && h < 8 ? 512 : 0)];
    const int gf = ht % 192;
    const v4u gw1 = ((const v4u*)w1)[gf], gb1 = ((const v4u*)b1)[gf], gw2 = ((const v4u*)w2)[gf];
    const unsigned ew1 = w1[1536 + gf], eb1 = b1[1536 + gf], ew2 = w2[1536 + gf];
    const unsigned b2v = b2[0];
    const unsigned tag = __hip_atomic_load(epb, __ATOMIC_RELAXED, __HIP_MEMORY_SCOPE_AGENT) + TAG_STEP;
    const int cgrp = (hw == 1 && h < 8) ? 32 + h : h;
    const unsigned xres = x32[8 * cgrp + (lane & 7)];
    __builtin_amdgcn_sched_barrier(0);
    const __amdgpu_buffer_rsrc_t rO = __builtin_amdgcn_make_buffer_rsrc((void*)(wo + (size_t)h * DH * D_), 0, DH * D_ * 2, 0x00020000);
    const int ooff = (32 * hw + g) * (D_ * 2) + c * 16;
    static_for<0, 40>([&](auto I) {
      constexpr int i = I;
      if constexpr (i >= WO_INFL) wait_vm<WO_INFL - 1>();
      wr[i] = __builtin_amdgcn_raw_buffer_load_b128(rO, ooff, (i / 5) * 4 * (D_ * 2) + (i % 5) * 256, 0);
      __builtin_amdgcn_sched_barrier(0);
    });
    if (hw == 1) {
      const unsigned char* tp = lane == 0 ? (const unsigned char*)(vc + (size_t)bh * SK * DH)
                              : lane == 1 ? (const unsigned char*)(vc + (size_t)(bh + 1) * SK * DH) - 128
                              : lane == 2 ? (const unsigned char*)out
                              : lane == 3 ? ws + WS_SG + (size_t)b * 32 * 5120
                              : ws + WS_SG + (size_t)(b + 1) * 32 * 5120 - 16;
      const unsigned tv = __hip_atomic_load((gu32*)tp, __ATOMIC_RELAXED, __HIP_MEMORY_SCOPE_AGENT);
      asm volatile("" ::"v"(tv));
    }
    if (ht == 0) { ((unsigned*)lds)[L_TAG] = tag; ((unsigned*)lds)[L_CNT] = 0u; }
    auto pacer = [&](int round, unsigned pos) __attribute__((always_inline)) {
      const __amdgpu_buffer_rsrc_t rA = __builtin_amdgcn_make_buffer_rsrc((void*)(ws + WS_AN + round * 4096), 0, 4096, 0x00020000);
      unsigned at = 0;
      for (int it = 0; it < 4096; ++it) {
        at = __builtin_amdgcn_readfirstlane(lds_get(&lds[L_POS]));
        if (at >= pos) break;
        __builtin_amdgcn_s_sleep(8);
      }
      if (at > pos + 16u) return;
      __builtin_amdgcn_s_sleep(16);
      const unsigned tv = lds_get(&lds[L_TS + (lane & 3)]);
      const unsigned ref = __builtin_amdgcn_readfirstlane(tv);
      int d = (int)(tv - ref);
      d += (int)dppu<XOR1>((unsigned)d); d += (int)dppu<XOR2>((unsigned)d);
      const unsigned mine = ref + (unsigned)(d >> 2);
      v4u gv; gv.x = mine; gv.y = mine; gv.z = mine; gv.w = mine;
      __builtin_amdgcn_raw_buffer_store_b128(gv, rA, ((h >> 2) == b && lane < 8) ? h * 128 + lane * 16 : OOB, 0, SC1);
      __builtin_amdgcn_s_sleep(110);
      asm volatile("" ::: "memory");
      const int r = (int)(__builtin_amdgcn_raw_buffer_load_b32(rA, lane < 32 ? lane * 128 : OOB, 0, SC1) - mine);
      const bool ok = lane < 32 && r > -1000 && r < 1000;
      const int cnt = __builtin_popcountll(__builtin_amdgcn_ballot_w64(ok));
      const int mean = (int)(wave_sum(ok ? (float)r : 0.f) / (float)(cnt > 0 ? cnt : 1));
      if (cnt >= 16) lds_put(&lds[L_AN], mine + (unsigned)(mean + DELTA));
    };
    if (hw == 3) pacer(0, 208);
    lds_barrier();
    if (hw == 3) {
#pragma unroll 1
      for (int r = 1; r < 3; ++r) pacer(r, r == 1 ? 304 : 400);
    }
    __builtin_amdgcn_s_setprio(0);
    if (hw < 3) {
      float mb2, sd2;
#pragma unroll
      for (int i = 0; i < 10; ++i) xv[i] = ln_apply(xv[i], mb, sd);
      ln_stats(xv, mb2, sd2);
      const float hcol = ln_apply(ln_apply(bflo(xg), mb, sd), mb2, sd2);
      float w1v[9], b1v[9], w2v[9];
#pragma unroll
      for (int i = 0; i < 4; ++i) {
        w1v[2 * i] = bflo(gw1[i]); w1v[2 * i + 1] = bfhi(gw1[i]);
        b1v[2 * i] = bflo(gb1[i]); b1v[2 * i + 1] = bfhi(gb1[i]);
        w2v[2 * i] = bflo(gw2[i]); w2v[2 * i + 1] = bfhi(gw2[i]);
      }
      w1v[8] = bflo(ew1); b1v[8] = bflo(eb1); w2v[8] = bflo(ew2);
      float ycol = 0.f;
#pragma unroll 1
      for (int j = 0; j < ncol; ++j) {
        const float hv = rdlane(hcol, j);
        float y = 0.f;
#pragma unroll
        for (int i = 0; i < 9; ++i) y = fmaf(gelu_bf(rbf(rbf(hv * w1v[i]) + b1v[i])), w2v[i], y);
        y = wave_sum(y);
        ycol = (lane == j) ? y : ycol;
      }
      if (lane < 32) lds[L_GATE + hw * 32 + lane] = ycol;
      asm volatile("s_waitcnt lgkmcnt(0)" ::: "memory");
      if (lane == 0) __hip_atomic_fetch_add((lu32*)&lds[L_CNT], 1u, __ATOMIC_RELAXED, __HIP_MEMORY_SCOPE_WORKGROUP);
      while (lds_get(&lds[L_CNT]) < 3u) __builtin_amdgcn_s_sleep(2);
      asm volatile("" ::: "memory");
    }
    __builtin_amdgcn_s_setprio(3);
    float gt0, gt1;
    {
      const int cc = (cgrp >= 32 ? 16 : 0) + 2 * (lane & 7);
      float y0 = (lds[L_GATE + cc] + lds[L_GATE + 32 + cc]) + lds[L_GATE + 64 + cc];
      float y1 = (lds[L_GATE + cc + 1] + lds[L_GATE + 33 + cc]) + lds[L_GATE + 65 + cc];
      y0 = rbf(rbf(y0) + bflo(b2v)); y1 = rbf(rbf(y1) + bflo(b2v));
      gt0 = rbf(y0 / (1.0f + __expf(-y0))); gt1 = rbf(y1 / (1.0f + __expf(-y1)));
    }
    int goff = (lane >> 3) * 5120 + cgrp * 128 + (lane & 7) * 16;
    float xr0 = bflo(xres), xr1 = bfhi(xres);
    v4u smask;
    smask.x = (c >> 2) == 0 ? 0x0000ffffu : 0u; smask.y = (c >> 2) == 1 ? 0x0000ffffu : 0u;
    smask.z = smask.x << 16; smask.w = smask.y << 16;
    int sidx = (c & 3) * 0x5555;
    lf32x2* op = (lf32x2*)&lds[L_OP + 32 * hw + 8 * (c & 3) + 2 * g];
    lf32x4* sp = (lf32x4*)&lds[L_S + hw * 640 + 8 * c + 4 * (g & 1)];
    f32x4 sacc[5];
#pragma unroll
    for (int u = 0; u < 5; ++u) { sacc[u] = f32x4{0.f, 0.f, 0.f, 0.f}; asm volatile("" : "+v"(sacc[u])); }
    asm volatile("" : "+v"(gt0), "+v"(gt1), "+v"(goff), "+v"(smask), "+v"(xr0), "+v"(xr1), "+v"(sidx), "+v"(op), "+v"(sp));
    const __amdgpu_buffer_rsrc_t rG = __builtin_amdgcn_make_buffer_rsrc((void*)(ws + WS_SG + (size_t)b * 32 * 5120), 0, 32 * 5120, 0x00020000);
    const __amdgpu_buffer_rsrc_t rOut = __builtin_amdgcn_make_buffer_rsrc((void*)(out + b * D_ + 16 * cgrp), 0, 32, 0x00020000);
    const __amdgpu_buffer_rsrc_t rP = __builtin_amdgcn_make_buffer_rsrc((void*)(ws + WS_SG + (size_t)bh * 5120), 0, 5120, 0x00020000);
    asm volatile("" ::"s"(rG), "s"(rOut), "s"(rP));
    __builtin_amdgcn_s_waitcnt(0x0F70);
    lds_barrier();
    const unsigned od = bfpack((op[0] + op[64]) + (op[128] + op[192]));
    static_for<0, 4>([&](auto T) {
      constexpr int t = T;
      const v4u a = onehot<t>(od, smask);
#pragma unroll
      for (int u = 0; u < 5; ++u) sacc[u] = smfma16(a, wr[10 * t + u], wr[10 * t + 5 + u], sacc[u], sidx);
    });
    if (lane < 32) {
#pragma unroll
      for (int u = 0; u < 5; ++u) sp[u * 32] = sacc[u];
    }
    lds_barrier();
    publish(rP, ht, tag);
    if (hw == 0 || cgrp >= 32) {
      v4u gr[4];
      __builtin_amdgcn_s_sleep(22);
#pragma unroll 1
      for (int spin = 0; spin < (1 << 17); ++spin) {
        asm volatile("" : "+v"(goff)::"memory");
#pragma unroll
        for (int i = 0; i < 4; ++i) gr[i] = __builtin_amdgcn_raw_buffer_load_b128(rG, goff, i * 8 * 5120, SC1);
        unsigned bad = 0;
#pragma unroll
        for (int i = 0; i < 4; ++i) bad |= (gr[i].y ^ tag) | (gr[i].w ^ tag);
        asm volatile("" : "+v"(bad));
        if (__all(bad == 0u)) break;
        __builtin_amdgcn_s_sleep(1);
      }
      float s0 = (__uint_as_float(gr[0].x) + __uint_as_float(gr[1].x)) + (__uint_as_float(gr[2].x) + __uint_as_float(gr[3].x));
      float s1 = (__uint_as_float(gr[0].z) + __uint_as_float(gr[1].z)) + (__uint_as_float(gr[2].z) + __uint_as_float(gr[3].z));
      s0 += dppf<XOR8>(s0); s1 += dppf<XOR8>(s1);
      s0 = swap32_sum(swap16_sum(s0)); s1 = swap32_sum(swap16_sum(s1));
      const unsigned o0 = bfbits(xr0 + rbf(rbf(s0) * gt0)), o1 = bfbits(xr1 + rbf(rbf(s1) * gt1));
      __builtin_amdgcn_raw_buffer_store_b32(o0 | (o1 << 16), rOut, lane * 4, 0, SC1);
      if (cgrp == 0 && lane == 0) __hip_atomic_store(epb, tag, __ATOMIC_RELAXED, __HIP_MEMORY_SCOPE_AGENT);
    }
    return;
  }

  {
    __builtin_amdgcn_s_setprio(1);
    const __amdgpu_buffer_rsrc_t rK = __builtin_amdgcn_make_buffer_rsrc((void*)(kc + (size_t)bh * SK * DH), 0, SK * DH * 2, 0x00020000);
    const __amdgpu_buffer_rsrc_t rV = __builtin_amdgcn_make_buffer_rsrc((void*)(vc + (size_t)bh * SK * DH), 0, SK * DH * 2, 0x00020000);
    const int koff = w * (1024 << CL) + lane * 16;
    v4u* lk = (v4u*)&lds[L_K] + w * (16 * NL * 64) + lane;
    unsigned t0;
    v4u stg[8], land[16 * NX], ring[16];
    static_for<0, 16 * (NL + NX + 1)>([&](auto N) {
      constexpr int n = N;
      if constexpr (n >= TLO && n < 16 * (NL + NX + 1) - 2) wait_vm<TLO - 1>();
      const v4u kv = __builtin_amdgcn_raw_buffer_load_b128(rK, koff, soff(n), NT);
      if constexpr (n < 16 * NL) stg[n & 7] = kv;
      else if constexpr (n < 16 * (NL + NX)) land[n - 16 * NL] = kv;
      else ring[n & 15] = kv;
      if constexpr (n >= 6 && n - 6 < 16 * NL) lk[(n - 6) * 64] = stg[(n - 6) & 7];
      if constexpr (n == TLO - 1) t0 = (unsigned)__builtin_amdgcn_s_memrealtime();
      __builtin_amdgcn_sched_barrier(0);
    });
    if (lane == 0) { ((unsigned*)lds)[L_TS + w] = t0; ((unsigned*)lds)[L_AN] = t0 + TOFF; ((unsigned*)lds)[L_POS] = 0u; }
    lds_barrier();
    v4u qp;
    {
      f32x4 s0 = *(const f32x4*)&lds[L_S + 8 * c], s1 = *(const f32x4*)&lds[L_S + 8 * c + 4];
#pragma unroll
      for (int ww = 1; ww < 4; ++ww) { s0 += *(const f32x4*)&lds[L_S + ww * 128 + 8 * c]; s1 += *(const f32x4*)&lds[L_S + ww * 128 + 8 * c + 4]; }
      qp.x = bfbits(s0.x) | (bfbits(s0.y) << 16); qp.y = bfbits(s0.z) | (bfbits(s0.w) << 16);
      qp.z = bfbits(s1.x) | (bfbits(s1.y) << 16); qp.w = bfbits(s1.z) | (bfbits(s1.w) << 16);
    }
    auto pace = [&](int n) __attribute__((always_inline)) -> int {
      const unsigned anchor = lds_get(&lds[L_AN]);
      const unsigned v = (unsigned)__builtin_amdgcn_s_memrealtime() - (unsigned)((n * TAU256) >> 8);
      lds_put(&lds[L_TS + w], v);
      lds_put(&lds[L_POS], (unsigned)n);
      const int late = (int)(v - (unsigned)__builtin_amdgcn_readfirstlane(anchor));
      return late < 0 ? 0 : late < FAR ? 1 : 2;
    };
    float* scw = &lds[L_SC + w * 1024 + lane];
    float mrun = -__builtin_inff(), lrun = 0.f;
    float ra[4], rb[4];
    auto kstep = [&](auto J, const v4u& kv, float (&r)[4], int grp) __attribute__((always_inline)) {
      constexpr int j = J;
      float d = dot2(kv.x, qp.x, 0.f);
      d = dot2(kv.y, qp.y, d); d = dot2(kv.z, qp.z, d); d = dot2(kv.w, qp.w, d);
      d += dppf<XOR8>(d);
      if constexpr (!(j & 1)) { r[0] = d; asm volatile("" : "+v"(r[0])); return; }
      d = (c & 8) ? d : r[0];
      d += dppf<XOR7>(d);
      if constexpr (!(j & 2)) { r[1] = d; asm volatile("" : "+v"(r[1])); return; }
      d = (c & 4) ? d : r[1];
      d += dppf<XOR2>(d);
      if constexpr (!(j & 4)) { r[2] = d; asm volatile("" : "+v"(r[2])); return; }
      d = (c & 2) ? d : r[2];
      d += dppf<XOR1>(d);
      if constexpr (!(j & 8)) { r[3] = d; asm volatile("" : "+v"(r[3])); return; }
      d = (c & 1) ? d : r[3];
      const float s = rbf(rbf(d) * 0.08838834764831845f);
      scw[grp * 64] = s;
      const float mn = fmaxf(fmaxf(mrun, s), -3.4e38f);
      lrun = fmaf(lrun, __builtin_amdgcn_exp2f((mrun - mn) * L2E), __builtin_amdgcn_exp2f((s - mn) * L2E));
      mrun = mn;
      asm volatile("" : "+v"(mrun), "+v"(lrun));
    };
    static_for<0, 16 * NX>([&](auto M) {
      constexpr int m = M, i = m & 15, k = NL + NX + (m >> 4);
      kstep(std::integral_constant<int, i>{}, ring[i], ra, k);
      wait_vm<TLO - 1>();
      ring[i] = __builtin_amdgcn_raw_buffer_load_b128(rK, koff, soff(16 * (k + 1) + i), NT);
      kstep(std::integral_constant<int, i>{}, land[m], rb, NL + (m >> 4));
      __builtin_amdgcn_sched_barrier(0);
    });
#pragma unroll 1
    for (int kk = 0; kk < NL; ++kk) {
      const int k = NL + 2 * NX + kk;
      int state = pace(16 * k + 16);
      if (kk == 0) state = 1;
      static_for<0, 16>([&](auto I) {
        constexpr int i = I;
        const v4u kv = lk[(kk * 16 + i) * 64];
        kstep(I, ring[i], ra, k);
        wait_var(state);
        ring[i] = __builtin_amdgcn_raw_buffer_load_b128(rK, koff, (k + 1) * 65536 + soff(i), NT);
        kstep(I, kv, rb, kk);
        __builtin_amdgcn_sched_barrier(0);
      });
    }
#pragma unroll 1
    for (int k = 2 * (NL + NX); k < 16; ++k) {
      const bool last = k == 15;
      const __amdgpu_buffer_rsrc_t rN = last ? rV : rK;
      const int sbase = last ? 0 : (k + 1) * 65536;
      int state = last ? 3 : pace(16 * k + 16);
      if (k == 2 * (NL + NX)) state = 1;
      static_for<0, 16>([&](auto I) {
        constexpr int i = I;
        kstep(I, ring[i], ra, k);
        wait_var(state);
        ring[i] = __builtin_amdgcn_raw_buffer_load_b128(rN, koff, sbase + soff(i), NT);
        __builtin_amdgcn_sched_barrier(0);
      });
    }
    {
      const float m = wave_max(mrun);
      const float l = wave_sum(lrun * __builtin_amdgcn_exp2f((mrun - m) * L2E));
      if (lane == 0) { lds[L_SM + 2 * w] = m; lds[L_SM + 2 * w + 1] = l; }
    }
    float snext = scw[0];
    const v4u mask = onehot_mask(c);
    lds_barrier();
    float mx, inv;
    {
      const f32x4 s0 = *(const f32x4*)&lds[L_SM], s1 = *(const f32x4*)&lds[L_SM + 4];
      mx = fmaxf(fmaxf(s0.x, s0.z), fmaxf(s1.x, s1.z));
      float l = s0.y * __builtin_amdgcn_exp2f((s0.x - mx) * L2E);
      l = fmaf(s0.w, __builtin_amdgcn_exp2f((s0.z - mx) * L2E), l);
      l = fmaf(s1.y, __builtin_amdgcn_exp2f((s1.x - mx) * L2E), l);
      l = fmaf(s1.w, __builtin_amdgcn_exp2f((s1.z - mx) * L2E), l);
      inv = 1.0f / l;
    }
    f32x4 oacc = {0.f, 0.f, 0.f, 0.f};
#pragma unroll 1
    for (int k = 0; k < 16; ++k) {
      const bool last = k == 15;
      const int voff = last ? OOB : koff;
      const int sbase = last ? 0 : (k + 1) * 65536;
      const int state = last ? 3 : k == 0 ? 0 : pace(256 + 16 * k + 16);
      unsigned pd = bfbits(__builtin_amdgcn_exp2f((snext - mx) * L2E) * inv);
      pd |= pd << 16;
      snext = scw[((k + 1) & 15) * 64];
      static_for<0, 16>([&](auto I) {
        constexpr int i = I;
        const v4u a = onehot<brev4(i)>(pd, mask);
        oacc = mfma16(a, ring[i], oacc);
        asm volatile("" : "+v"(oacc));
        wait_var(state);
        ring[i] = __builtin_amdgcn_raw_buffer_load_b128(rV, voff, sbase + soff(i), NT);
        __builtin_amdgcn_sched_barrier(0);
      });
    }
    if (lane < 32) *(f32x4*)&lds[L_OP + w * 128 + 8 * c + 4 * g] = oacc;
    lds_barrier();
    if (w == 2) {
      const unsigned tag = ((const unsigned*)lds)[L_TAG];
      unsigned char* wsp = ws;
      asm volatile("" : "+s"(wsp));
      const __amdgpu_buffer_rsrc_t rP = __builtin_amdgcn_make_buffer_rsrc((void*)(wsp + WS_SG + (size_t)bh * 5120), 0, 5120, 0x00020000);
      asm volatile("" ::"s"(rP));
      lds_barrier();
      publish(rP, 256 + lane, tag);
    }
  }
}

extern "C" void kernel_launch(void* const* d_in, const int* in_sizes, int n_in, void* d_out, int out_size, void* d_ws,
                              size_t ws_size, hipStream_t stream) {
  fused_kernel<<<256, 512, 0, stream>>>((const u16*)d_in[0], (const u16*)d_in[1], (const u16*)d_in[2], (const u16*)d_in[3],
                                        (const u16*)d_in[4], (const u16*)d_in[6], (const u16*)d_in[7], (const u16*)d_in[8],
                                        (const u16*)d_in[9], (unsigned char*)d_ws, (u16*)d_out);
}
```

```cpp
#include <hip/hip_runtime.h>
#include <hip/hip_bf16.h>
#include <stdint.h>
#include <type_traits>

typedef unsigned v2u __attribute__((ext_vector_type(2)));
typedef unsigned v4u __attribute__((ext_vector_type(4)));
typedef unsigned v8u __attribute__((ext_vector_type(8)));
typedef float f32x2 __attribute__((ext_vector_type(2)));
typedef float f32x4 __attribute__((ext_vector_type(4)));
typedef __bf16 bf16x2 __attribute__((ext_vector_type(2)));
typedef __bf16 bf16x8 __attribute__((ext_vector_type(8)));
typedef __bf16 bf16x16 __attribute__((ext_vector_type(16)));
typedef unsigned short u16;
typedef __attribute__((address_space(1))) unsigned gu32;
typedef __attribute__((address_space(3))) unsigned lu32;
typedef __attribute__((address_space(3))) f32x2 lf32x2;
typedef __attribute__((address_space(3))) f32x4 lf32x4;

constexpr int D_ = 640, H_ = 32, SK = 4096, DH = 128;
constexpr size_t WS_SG = 0, WS_EPB = 256 * 5120, WS_AN = WS_EPB + 4096;
constexpr unsigned TAG_STEP = 0x9E3779B9u;

constexpr int TLO = 5;
constexpr int TAU256 = 3430;
constexpr int TOFF = 190;
constexpr int DELTA = 35;
constexpr int FAR = 70;
constexpr int NL = 2, NX = 2;
constexpr int WQ_INFL = 3, WO_INFL = 2;
constexpr int NT = 2, SC1 = 16, OOB = (int)0x80000000;

__device__ __forceinline__ float rbf(float x) { return (float)(__bf16)x; }
__device__ __forceinline__ unsigned bfbits(float x) { return (unsigned)__builtin_bit_cast(u16, (__bf16)x); }
__device__ __forceinline__ unsigned bfpack(f32x2 v) { return __builtin_bit_cast(unsigned, __builtin_convertvector(v, bf16x2)); }
__device__ __forceinline__ float bflo(unsigned u) { return __builtin_bit_cast(float, u << 16); }
__device__ __forceinline__ float bfhi(unsigned u) { return __builtin_bit_cast(float, u & 0xffff0000u); }

template <int CTRL> __device__ __forceinline__ float dppf(float x) {
  return __builtin_bit_cast(float, __builtin_amdgcn_mov_dpp(__builtin_bit_cast(int, x), CTRL, 0xf, 0xf, true));
}
template <int CTRL> __device__ __forceinline__ unsigned dppu(unsigned x) {
  return (unsigned)__builtin_amdgcn_mov_dpp((int)x, CTRL, 0xf, 0xf, true);
}
constexpr int XOR1 = 0xB1, XOR2 = 0x4E, XOR7 = 0x141, XOR8 = 0x128, NEWBCAST = 0x150;

__device__ __forceinline__ float swap16_sum(float x) {
  auto s = __builtin_amdgcn_permlane16_swap(__float_as_uint(x), __float_as_uint(x), false, false);
  return __uint_as_float(s[0]) + __uint_as_float(s[1]);
}
__device__ __forceinline__ float swap32_sum(float x) {
  auto t = __builtin_amdgcn_permlane32_swap(__float_as_uint(x), __float_as_uint(x), false, false);
  return __uint_as_float(t[0]) + __uint_as_float(t[1]);
}
__device__ __forceinline__ float wave_sum(float x) {
  x += dppf<XOR1>(x); x += dppf<XOR2>(x); x += dppf<XOR7>(x); x += dppf<XOR8>(x);
  return swap32_sum(swap16_sum(x));
}
__device__ __forceinline__ float wave_max(float x) {
  x = fmaxf(x, dppf<XOR1>(x)); x = fmaxf(x, dppf<XOR2>(x)); x = fmaxf(x, dppf<XOR7>(x)); x = fmaxf(x, dppf<XOR8>(x));
  auto s = __builtin_amdgcn_permlane16_swap(__float_as_uint(x), __float_as_uint(x), false, false);
  x = fmaxf(__uint_as_float(s[0]), __uint_as_float(s[1]));
  auto t = __builtin_amdgcn_permlane32_swap(__float_as_uint(x), __float_as_uint(x), false, false);
  return fmaxf(__uint_as_float(t[0]), __uint_as_float(t[1]));
}
__device__ __forceinline__ float rdlane(float x, int l) { return __builtin_bit_cast(float, __builtin_amdgcn_readlane(__builtin_bit_cast(int, x), l)); }
__device__ __forceinline__ float dot2(unsigned a, unsigned b, float acc) {
  return __builtin_amdgcn_fdot2_f32_bf16(__builtin_bit_cast(bf16x2, a), __builtin_bit_cast(bf16x2, b), acc, false);
}
__device__ __forceinline__ f32x4 mfma16(v4u a, v4u b, f32x4 c) {
  return __builtin_amdgcn_mfma_f32_16x16x32_bf16(__builtin_bit_cast(bf16x8, a), __builtin_bit_cast(bf16x8, b), c, 0, 0, 0);
}
__device__ __forceinline__ f32x4 smfma16(v4u a, v4u b0, v4u b1, f32x4 c, int idx) {
  const v8u b = __builtin_shufflevector(b0, b1, 0, 1, 2, 3, 4, 5, 6, 7);
  return __builtin_amdgcn_smfmac_f32_16x16x64_bf16(__builtin_bit_cast(bf16x8, a), __builtin_bit_cast(bf16x16, b), c, idx, 0, 0);
}
template <int J> __device__ __forceinline__ v4u onehot(unsigned val2, const v4u& mask) {
  const unsigned pb = dppu<NEWBCAST + J>(val2);
  v4u a;
  a.x = pb & mask.x; a.y = pb & mask.y; a.z = pb & mask.z; a.w = pb & mask.w;
  return a;
}
__device__ __forceinline__ v4u onehot_mask(int c) {
  const unsigned mm = (c & 1) ? 0xffff0000u : 0x0000ffffu;
  v4u mask;
  mask.x = (c >> 1) == 0 ? mm : 0u; mask.y = (c >> 1) == 1 ? mm : 0u; mask.z = (c >> 1) == 2 ? mm : 0u; mask.w = (c >> 1) == 3 ? mm : 0u;
  return mask;
}
__device__ __forceinline__ void lds_barrier() {
  asm volatile("s_waitcnt lgkmcnt(0)" ::: "memory");
  __builtin_amdgcn_s_barrier();
  asm volatile("" ::: "memory");
}
__device__ __forceinline__ unsigned lds_get(float* p) { return __hip_atomic_load((lu32*)p, __ATOMIC_RELAXED, __HIP_MEMORY_SCOPE_WORKGROUP); }
__device__ __forceinline__ void lds_put(float* p, unsigned v) { __hip_atomic_store((lu32*)p, v, __ATOMIC_RELAXED, __HIP_MEMORY_SCOPE_WORKGROUP); }
template <int N> __device__ __forceinline__ void wait_vm() { asm volatile("s_waitcnt vmcnt(%0)" ::"i"(N) : "memory"); }
__device__ __forceinline__ void wait_var(int state) {
  asm volatile("s_cmp_eq_u32 %0, 3\n\ts_cbranch_scc1 .Lwc%=\n\ts_waitcnt vmcnt(%3)\n\t"
               "s_cmp_eq_u32 %0, 2\n\ts_cbranch_scc1 .Lwc%=\n\ts_waitcnt vmcnt(%2)\n\t"
               "s_cmp_eq_u32 %0, 1\n\ts_cbranch_scc1 .Lwc%=\n\ts_waitcnt vmcnt(%1)\n.Lwc%=:"
               ::"s"(state), "i"(TLO - 1), "i"(TLO), "i"(TLO + 1) : "memory", "scc");
}
__device__ __forceinline__ void ln_stats(const float (&xv)[10], float& mb, float& sd) {
  float s = 0.f;
#pragma unroll
  for (int i = 0; i < 10; ++i) s += xv[i];
  const float mean = wave_sum(s) * (1.0f / D_);
  float q = 0.f;
#pragma unroll
  for (int i = 0; i < 10; ++i) { const float d = xv[i] - mean; q = fmaf(d, d, q); }
  const float var = rbf(wave_sum(q) * (1.0f / D_));
  mb = rbf(mean);
  sd = rbf(sqrtf(rbf(var + 1e-5f)));
}
__device__ __forceinline__ float ln_apply(float x, float mb, float sd) { return rbf(rbf(x - mb) / sd); }
__device__ __forceinline__ float gelu_bf(float u) {
  constexpr float K1 = -2.0f * 0.7978845608028654f * 1.4426950408889634f;
  constexpr float K2 = K1 * 0.044715f;
  const float e = __builtin_amdgcn_exp2f(u * fmaf(K2, u * u, K1));
  return rbf(u * __builtin_amdgcn_rcpf(1.0f + e));
}
template <int I, int N, typename Fn> __device__ __forceinline__ void static_for(Fn&& f) {
  if constexpr (I < N) { f(std::integral_constant<int, I>{}); static_for<I + 1, N>(f); }
}
constexpr unsigned long long PERM = 0x3502784b1a96ull;
constexpr int ppos(int i) { return (int)((PERM >> (4 * (11 - i))) & 15); }
constexpr int scat4(int v, int i) {
  return (((v & 1) << ppos(i)) | (((v >> 1) & 1) << ppos(i + 1)) | (((v >> 2) & 1) << ppos(i + 2)) | (((v >> 3) & 1) << ppos(i + 3))) * 256;
}
constexpr int soff(int n) { return scat4(n & 15, 4) + scat4(n >> 4, 8); }
constexpr int brev4(int j) { return ((j & 1) << 3) | ((j & 2) << 1) | ((j & 4) >> 1) | ((j & 8) >> 3); }

constexpr int L_S = 0;
constexpr int L_SM = 2560;
constexpr int L_TAG = 2568;
constexpr int L_CNT = 2569;
constexpr int L_AN = 2570;
constexpr int L_POS = 2571;
constexpr int L_TS = 2572;
constexpr int L_GATE = 2576;
constexpr int L_OP = 2704;
constexpr int L_SC = 3216;
constexpr int L_K = L_SC + 4096;
constexpr int L_END = L_K + 4 * 16 * NL * 256;

__global__ void __launch_bounds__(512, 2)
fused_kernel(const u16* __restrict__ x, const u16* __restrict__ kc, const u16* __restrict__ vc,
             const u16* __restrict__ wq, const u16* __restrict__ wo, const u16* __restrict__ w1, const u16* __restrict__ b1,
             const u16* __restrict__ w2, const u16* __restrict__ b2, unsigned char* ws, u16* out) {
  __shared__ __attribute__((aligned(16))) float lds[L_END];
  asm volatile("" ::"s"(x), "s"(kc), "s"(vc), "s"(wq));
  const int bh = blockIdx.x, b = bh >> 5, h = bh & 31;
  const int tid = threadIdx.x, lane = tid & 63, w = __builtin_amdgcn_readfirstlane(tid >> 6);
  const int g = lane >> 4, c = lane & 15;
  constexpr float L2E = 1.4426950408889634f;

  auto publish = [&](__amdgpu_buffer_rsrc_t rP, int t, unsigned tag) __attribute__((always_inline)) {
    const f32x2 sv = (*(const f32x2*)&lds[L_S + 2 * t] + *(const f32x2*)&lds[L_S + 640 + 2 * t]) +
                     (*(const f32x2*)&lds[L_S + 1280 + 2 * t] + *(const f32x2*)&lds[L_S + 1920 + 2 * t]);
    v4u gv; gv.x = __float_as_uint(sv.x); gv.y = tag; gv.z = __float_as_uint(sv.y); gv.w = tag;
    __builtin_amdgcn_raw_buffer_store_b128(gv, rP, t * 16, 0, SC1);
  };

  if (w >= 4) {
    __builtin_amdgcn_s_setprio(3);
    const int hw = w - 4, ht = tid - 256;
    gu32* epb = (gu32*)(ws + WS_EPB) + b * 64;
    const unsigned* x32 = (const unsigned*)(x + b * D_);
    const u16* xb = x + b * D_;
    unsigned xr[5];
#pragma unroll
    for (int i = 0; i < 5; ++i) xr[i] = x32[lane + 64 * i];
    const unsigned xo0 = xb[160 * hw + 4 * c + g], xo1 = xb[160 * hw + 64 + 4 * c + g], xo2 = xb[160 * hw + 128 + 4 * (c & 7) + g];
    __builtin_amdgcn_sched_barrier(0);
    const __amdgpu_buffer_rsrc_t rQ = __builtin_amdgcn_make_buffer_rsrc((void*)(wq + h * DH), 0, D_ * H_ * DH * 2 - h * DH * 2, 0x00020000);
    const int qoff = (160 * hw + g) * (H_ * DH * 2) + c * 16;
    v4u wr[40];
    static_for<0, 2 * WQ_INFL>([&](auto T) {
      constexpr int t = T;
      if constexpr (t >= WQ_INFL) wait_vm<WQ_INFL - 1>();
      wr[t] = __builtin_amdgcn_raw_buffer_load_b128(rQ, qoff, t * 4 * (H_ * DH * 2), 0);
      __builtin_amdgcn_sched_barrier(0);
    });
    const v4u mask = onehot_mask(c);
    float xv[10];
#pragma unroll
    for (int i = 0; i < 5; ++i) { xv[2 * i] = bflo(xr[i]); xv[2 * i + 1] = bfhi(xr[i]); }
    float mb, sd;
    ln_stats(xv, mb, sd);
    asm volatile("" : "+v"(mb), "+v"(sd));
    static_for<2 * WQ_INFL, 3 * WQ_INFL>([&](auto T) {
      constexpr int t = T;
      wait_vm<WQ_INFL - 1>();
      wr[t] = __builtin_amdgcn_raw_buffer_load_b128(rQ, qoff, t * 4 * (H_ * DH * 2), 0);
      __builtin_amdgcn_sched_barrier(0);
    });
    unsigned xn0 = bfbits(ln_apply(bflo(xo0), mb, sd)), xn1 = bfbits(ln_apply(bflo(xo1), mb, sd)), xn2 = bfbits(ln_apply(bflo(xo2), mb, sd));
    xn0 |= xn0 << 16; xn1 |= xn1 << 16; xn2 |= xn2 << 16;
    asm volatile("" : "+v"(xn0), "+v"(xn1), "+v"(xn2));
    {
      f32x4 acc = {0.f, 0.f, 0.f, 0.f};
      static_for<0, 40>([&](auto T) {
        constexpr int t = T, n = t + 3 * WQ_INFL;
        if constexpr (n < 40) {
          wait_vm<WQ_INFL - 1 + (n % 9 == 0 ? 1 : 0)>();
          wr[n] = __builtin_amdgcn_raw_buffer_load_b128(rQ, qoff, n * 4 * (H_ * DH * 2), 0);
        }
        const v4u a = onehot<(t & 15)>(t < 16 ? xn0 : t < 32 ? xn1 : xn2, mask);
        acc = mfma16(a, wr[t], acc);
        asm volatile("" : "+v"(acc));
        __builtin_amdgcn_sched_barrier(0);
      });
      if (lane < 32) *(f32x4*)&lds[L_S + hw * 128 + 8 * c + 4 * g] = acc;
    }
    lds_barrier();
    __builtin_amdgcn_sched_barrier(0);
    const int ncol = h < 8 ? 32 : 16;
    const unsigned xg = xb[16 * h + (lane & 15) + ((lane & 16) && h < 8 ? 512 : 0)];
    const int gf = ht % 192;
    const v4u gw1 = ((const v4u*)w1)[gf], gb1 = ((const v4u*)b1)[gf], gw2 = ((const v4u*)w2)[gf];
    const unsigned ew1 = w1[1536 + gf], eb1 = b1[1536 + gf], ew2 = w2[1536 + gf];
    const unsigned b2v = b2[0];
    const unsigned tag = __hip_atomic_load(epb, __ATOMIC_RELAXED, __HIP_MEMORY_SCOPE_AGENT) + TAG_STEP;
    const int cgrp = (hw == 1 && h < 8) ? 32 + h : h;
    const unsigned xres = x32[8 * cgrp + (lane & 7)];
    __builtin_amdgcn_sched_barrier(0);
    const __amdgpu_buffer_rsrc_t rO = __builtin_amdgcn_make_buffer_rsrc((void*)(wo + (size_t)h * DH * D_), 0, DH * D_ * 2, 0x00020000);
    const int ooff = (32 * hw + g) * (D_ * 2) + c * 16;
    static_for<0, 40>([&](auto I) {
      constexpr int i = I;
      if constexpr (i >= WO_INFL) wait_vm<WO_INFL - 1>();
      wr[i] = __builtin_amdgcn_raw_buffer_load_b128(rO, ooff, (i / 5) * 4 * (D_ * 2) + (i % 5) * 256, 0);
      __builtin_amdgcn_sched_barrier(0);
    });
    if (hw == 1) {
      const unsigned char* tp = lane == 0 ? (const unsigned char*)(vc + (size_t)bh * SK * DH)
                              : lane == 1 ? (const unsigned char*)(vc + (size_t)(bh + 1) * SK * DH) - 128
                              : lane == 2 ? (const unsigned char*)out
                              : lane == 3 ? ws + WS_SG + (size_t)b * 32 * 5120
                              : ws + WS_SG + (size_t)(b + 1) * 32 * 5120 - 16;
      const unsigned tv = __hip_atomic_load((gu32*)tp, __ATOMIC_RELAXED, __HIP_MEMORY_SCOPE_AGENT);
      asm volatile("" ::"v"(tv));
    }
    if (ht == 0) { ((unsigned*)lds)[L_TAG] = tag; ((unsigned*)lds)[L_CNT] = 0u; }
    auto pacer = [&](int round, unsigned pos) __attribute__((always_inline)) {
      const __amdgpu_buffer_rsrc_t rA = __builtin_amdgcn_make_buffer_rsrc((void*)(ws + WS_AN + round * 4096), 0, 4096, 0x00020000);
      unsigned at = 0;
      for (int it = 0; it < 4096; ++it) {
        at = __builtin_amdgcn_readfirstlane(lds_get(&lds[L_POS]));
        if (at >= pos) break;
        __builtin_amdgcn_s_sleep(8);
      }
      if (at > pos + 16u) return;
      __builtin_amdgcn_s_sleep(16);
      const unsigned tv = lds_get(&lds[L_TS + (lane & 3)]);
      const unsigned ref = __builtin_amdgcn_readfirstlane(tv);
      int d = (int)(tv - ref);
      d += (int)dppu<XOR1>((unsigned)d); d += (int)dppu<XOR2>((unsigned)d);
      const unsigned mine = ref + (unsigned)(d >> 2);
      v4u gv; gv.x = mine; gv.y = mine; gv.z = mine; gv.w = mine;
      __builtin_amdgcn_raw_buffer_store_b128(gv, rA, ((h >> 2) == b && lane < 8) ? h * 128 + lane * 16 : OOB, 0, SC1);
      __builtin_amdgcn_s_sleep(110);
      asm volatile("" ::: "memory");
      const int r = (int)(__builtin_amdgcn_raw_buffer_load_b32(rA, lane < 32 ? lane * 128 : OOB, 0, SC1) - mine);
      const bool ok = lane < 32 && r > -1000 && r < 1000;
      const int cnt = __builtin_popcountll(__builtin_amdgcn_ballot_w64(ok));
      const int mean = (int)(wave_sum(ok ? (float)r : 0.f) / (float)(cnt > 0 ? cnt : 1));
      if (cnt >= 16) lds_put(&lds[L_AN], mine + (unsigned)(mean + DELTA));
    };
    if (hw == 3) pacer(0, 208);
    lds_barrier();
    if (hw == 3) {
#pragma unroll 1
      for (int r = 1; r < 3; ++r) pacer(r, r == 1 ? 304 : 400);
    }
    __builtin_amdgcn_s_setprio(0);
    if (hw < 3) {
      float mb2, sd2;
#pragma unroll
      for (int i = 0; i < 10; ++i) xv[i] = ln_apply(xv[i], mb, sd);
      ln_stats(xv, mb2, sd2);
      const float hcol = ln_apply(ln_apply(bflo(xg), mb, sd), mb2, sd2);
      float w1v[9], b1v[9], w2v[9];
#pragma unroll
      for (int i = 0; i < 4; ++i) {
        w1v[2 * i] = bflo(gw1[i]); w1v[2 * i + 1] = bfhi(gw1[i]);
        b1v[2 * i] = bflo(gb1[i]); b1v[2 * i + 1] = bfhi(gb1[i]);
        w2v[2 * i] = bflo(gw2[i]); w2v[2 * i + 1] = bfhi(gw2[i]);
      }
      w1v[8] = bflo(ew1); b1v[8] = bflo(eb1); w2v[8] = bflo(ew2);
      float ycol = 0.f;
#pragma unroll 1
      for (int j = 0; j < ncol; ++j) {
        const float hv = rdlane(hcol, j);
        float y = 0.f;
#pragma unroll
        for (int i = 0; i < 9; ++i) y = fmaf(gelu_bf(rbf(rbf(hv * w1v[i]) + b1v[i])), w2v[i], y);
        y = wave_sum(y);
        ycol = (lane == j) ? y : ycol;
      }
      if (lane < 32) lds[L_GATE + hw * 32 + lane] = ycol;
      asm volatile("s_waitcnt lgkmcnt(0)" ::: "memory");
      if (lane == 0) __hip_atomic_fetch_add((lu32*)&lds[L_CNT], 1u, __ATOMIC_RELAXED, __HIP_MEMORY_SCOPE_WORKGROUP);
      while (lds_get(&lds[L_CNT]) < 3u) __builtin_amdgcn_s_sleep(2);
      asm volatile("" ::: "memory");
    }
    __builtin_amdgcn_s_setprio(3);
    float gt0, gt1;
    {
      const int cc = (cgrp >= 32 ? 16 : 0) + 2 * (lane & 7);
      float y0 = (lds[L_GATE + cc] + lds[L_GATE + 32 + cc]) + lds[L_GATE + 64 + cc];
      float y1 = (lds[L_GATE + cc + 1] + lds[L_GATE + 33 + cc]) + lds[L_GATE + 65 + cc];
      y0 = rbf(rbf(y0) + bflo(b2v)); y1 = rbf(rbf(y1) + bflo(b2v));
      gt0 = rbf(y0 / (1.0f + __expf(-y0))); gt1 = rbf(y1 / (1.0f + __expf(-y1)));
    }
    int goff = (lane >> 3) * 5120 + cgrp * 128 + (lane & 7) * 16;
    float xr0 = bflo(xres), xr1 = bfhi(xres);
    v4u smask;
    smask.x = (c >> 2) == 0 ? 0x0000ffffu : 0u; smask.y = (c >> 2) == 1 ? 0x0000ffffu : 0u;
    smask.z = smask.x << 16; smask.w = smask.y << 16;
    int sidx = (c & 3) * 0x5555;
    lf32x2* op = (lf32x2*)&lds[L_OP + 32 * hw + 8 * (c & 3) + 2 * g];
    lf32x4* sp = (lf32x4*)&lds[L_S + hw * 640 + 8 * c + 4 * (g & 1)];
    f32x4 sacc[5];
#pragma unroll
    for (int u = 0; u < 5; ++u) { sacc[u] = f32x4{0.f, 0.f, 0.f, 0.f}; asm volatile("" : "+v"(sacc[u])); }
    asm volatile("" : "+v"(gt0), "+v"(gt1), "+v"(goff), "+v"(smask), "+v"(xr0), "+v"(xr1), "+v"(sidx), "+v"(op), "+v"(sp));
    const __amdgpu_buffer_rsrc_t rG = __builtin_amdgcn_make_buffer_rsrc((void*)(ws + WS_SG + (size_t)b * 32 * 5120), 0, 32 * 5120, 0x00020000);
    const __amdgpu_buffer_rsrc_t rOut = __builtin_amdgcn_make_buffer_rsrc((void*)(out + b * D_ + 16 * cgrp), 0, 32, 0x00020000);
    const __amdgpu_buffer_rsrc_t rP = __builtin_amdgcn_make_buffer_rsrc((void*)(ws + WS_SG + (size_t)bh * 5120), 0, 5120, 0x00020000);
    asm volatile("" ::"s"(rG), "s"(rOut), "s"(rP));
    __builtin_amdgcn_s_waitcnt(0x0F70);
    lds_barrier();
    const unsigned od = bfpack((op[0] + op[64]) + (op[128] + op[192]));
    static_for<0, 4>([&](auto T) {
      constexpr int t = T;
      const v4u a = onehot<t>(od, smask);
#pragma unroll
      for (int u = 0; u < 5; ++u) sacc[u] = smfma16(a, wr[10 * t + u], wr[10 * t + 5 + u], sacc[u], sidx);
    });
    if (lane < 32) {
#pragma unroll
      for (int u = 0; u < 5; ++u) sp[u * 32] = sacc[u];
    }
    lds_barrier();
    publish(rP, ht, tag);
    if (hw == 0 || cgrp >= 32) {
      v4u gr[4];
      __builtin_amdgcn_s_sleep(22);
#pragma unroll 1
      for (int spin = 0; spin < (1 << 17); ++spin) {
        asm volatile("" : "+v"(goff)::"memory");
#pragma unroll
        for (int i = 0; i < 4; ++i) gr[i] = __builtin_amdgcn_raw_buffer_load_b128(rG, goff, i * 8 * 5120, SC1);
        unsigned bad = 0;
#pragma unroll
        for (int i = 0; i < 4; ++i) bad |= (gr[i].y ^ tag) | (gr[i].w ^ tag);
        asm volatile("" : "+v"(bad));
        if (__all(bad == 0u)) break;
        __builtin_amdgcn_s_sleep(1);
      }
      float s0 = (__uint_as_float(gr[0].x) + __uint_as_float(gr[1].x)) + (__uint_as_float(gr[2].x) + __uint_as_float(gr[3].x));
      float s1 = (__uint_as_float(gr[0].z) + __uint_as_float(gr[1].z)) + (__uint_as_float(gr[2].z) + __uint_as_float(gr[3].z));
      s0 += dppf<XOR8>(s0); s1 += dppf<XOR8>(s1);
      s0 = swap32_sum(swap16_sum(s0)); s1 = swap32_sum(swap16_sum(s1));
      const unsigned o0 = bfbits(xr0 + rbf(rbf(s0) * gt0)), o1 = bfbits(xr1 + rbf(rbf(s1) * gt1));
      __builtin_amdgcn_raw_buffer_store_b32(o0 | (o1 << 16), rOut, lane * 4, 0, SC1);
      if (cgrp == 0 && lane == 0) __hip_atomic_store(epb, tag, __ATOMIC_RELAXED, __HIP_MEMORY_SCOPE_AGENT);
    }
    return;
  }

  {
    __builtin_amdgcn_s_setprio(1);
    const __amdgpu_buffer_rsrc_t rK = __builtin_amdgcn_make_buffer_rsrc((void*)(kc + (size_t)bh * SK * DH), 0, SK * DH * 2, 0x00020000);
    const __amdgpu_buffer_rsrc_t rV = __builtin_amdgcn_make_buffer_rsrc((void*)(vc + (size_t)bh * SK * DH), 0, SK * DH * 2, 0x00020000);
    const int koff = scat4(tid >> 4, 0) + c * 16;
    v4u* lk = (v4u*)&lds[L_K] + w * (16 * NL * 64) + lane;
    unsigned t0;
    v4u stg[8], land[16 * NX], ring[16];
    static_for<0, 16 * (NL + NX + 1)>([&](auto N) {
      constexpr int n = N;
      if constexpr (n >= TLO && n < 16 * (NL + NX + 1) - 2) wait_vm<TLO - 1>();
      const v4u kv = __builtin_amdgcn_raw_buffer_load_b128(rK, koff, soff(n), NT);
      if constexpr (n < 16 * NL) stg[n & 7] = kv;
      else if constexpr (n < 16 * (NL + NX)) land[n - 16 * NL] = kv;
      else ring[n & 15] = kv;
      if constexpr (n >= 6 && n - 6 < 16 * NL) lk[(n - 6) * 64] = stg[(n - 6) & 7];
      if constexpr (n == TLO - 1) t0 = (unsigned)__builtin_amdgcn_s_memrealtime();
      __builtin_amdgcn_sched_barrier(0);
    });
    if (lane == 0) { ((unsigned*)lds)[L_TS + w] = t0; ((unsigned*)lds)[L_AN] = t0 + TOFF; ((unsigned*)lds)[L_POS] = 0u; }
    lds_barrier();
    v4u qp;
    {
      f32x4 s0 = *(const f32x4*)&lds[L_S + 8 * c], s1 = *(const f32x4*)&lds[L_S + 8 * c + 4];
#pragma unroll
      for (int ww = 1; ww < 4; ++ww) { s0 += *(const f32x4*)&lds[L_S + ww * 128 + 8 * c]; s1 += *(const f32x4*)&lds[L_S + ww * 128 + 8 * c + 4]; }
      qp.x = bfbits(s0.x) | (bfbits(s0.y) << 16); qp.y = bfbits(s0.z) | (bfbits(s0.w) << 16);
      qp.z = bfbits(s1.x) | (bfbits(s1.y) << 16); qp.w = bfbits(s1.z) | (bfbits(s1.w) << 16);
    }
    auto pace = [&](int n) __attribute__((always_inline)) -> int {
      const unsigned anchor = lds_get(&lds[L_AN]);
      const unsigned v = (unsigned)__builtin_amdgcn_s_memrealtime() - (unsigned)((n * TAU256) >> 8);
      lds_put(&lds[L_TS + w], v);
      lds_put(&lds[L_POS], (unsigned)n);
      const int late = (int)(v - (unsigned)__builtin_amdgcn_readfirstlane(anchor));
      return late < 0 ? 0 : late < FAR ? 1 : 2;
    };
    float* scw = &lds[L_SC + w * 1024 + lane];
    float mrun = -__builtin_inff(), lrun = 0.f;
    float ra[4], rb[4];
    auto kstep = [&](auto J, const v4u& kv, float (&r)[4], int grp) __attribute__((always_inline)) {
      constexpr int j = J;
      float d = dot2(kv.x, qp.x, 0.f);
      d = dot2(kv.y, qp.y, d); d = dot2(kv.z, qp.z, d); d = dot2(kv.w, qp.w, d);
      d += dppf<XOR8>(d);
      if constexpr (!(j & 1)) { r[0] = d; asm volatile("" : "+v"(r[0])); return; }
      d = (c & 8) ? d : r[0];
      d += dppf<XOR7>(d);
      if constexpr (!(j & 2)) { r[1] = d; asm volatile("" : "+v"(r[1])); return; }
      d = (c & 4) ? d : r[1];
      d += dppf<XOR2>(d);
      if constexpr (!(j & 4)) { r[2] = d; asm volatile("" : "+v"(r[2])); return; }
      d = (c & 2) ? d : r[2];
      d += dppf<XOR1>(d);
      if constexpr (!(j & 8)) { r[3] = d; asm volatile("" : "+v"(r[3])); return; }
      d = (c & 1) ? d : r[3];
      const float s = rbf(rbf(d) * 0.08838834764831845f);
      scw[grp * 64] = s;
      const float mn = fmaxf(fmaxf(mrun, s), -3.4e38f);
      lrun = fmaf(lrun, __builtin_amdgcn_exp2f((mrun - mn) * L2E), __builtin_amdgcn_exp2f((s - mn) * L2E));
      mrun = mn;
      asm volatile("" : "+v"(mrun), "+v"(lrun));
    };
    static_for<0, 16 * NX>([&](auto M) {
      constexpr int m = M, i = m & 15, k = NL + NX + (m >> 4);
      kstep(std::integral_constant<int, i>{}, ring[i], ra, k);
      wait_vm<TLO - 1>();
      ring[i] = __builtin_amdgcn_raw_buffer_load_b128(rK, koff, soff(16 * (k + 1) + i), NT);
      kstep(std::integral_constant<int, i>{}, land[m], rb, NL + (m >> 4));
      __builtin_amdgcn_sched_barrier(0);
    });
#pragma unroll 1
    for (int kk = 0; kk < NL; ++kk) {
      const int k = NL + 2 * NX + kk;
      int state = pace(16 * k + 16);
      if (kk == 0) state = 1;
      static_for<0, 16>([&](auto I) {
        constexpr int i = I;
        const v4u kv = lk[(kk * 16 + i) * 64];
        kstep(I, ring[i], ra, k);
        wait_var(state);
        ring[i] = __builtin_amdgcn_raw_buffer_load_b128(rK, koff, scat4(k + 1, 8) + soff(i), NT);
        kstep(I, kv, rb, kk);
        __builtin_amdgcn_sched_barrier(0);
      });
    }
#pragma unroll 1
    for (int k = 2 * (NL + NX); k < 16; ++k) {
      const bool last = k == 15;
      const __amdgpu_buffer_rsrc_t rN = last ? rV : rK;
      const int sbase = scat4((k + 1) & 15, 8);
      int state = last ? 3 : pace(16 * k + 16);
      if (k == 2 * (NL + NX)) state = 1;
      static_for<0, 16>([&](auto I) {
        constexpr int i = I;
        kstep(I, ring[i], ra, k);
        wait_var(state);
        ring[i] = __builtin_amdgcn_raw_buffer_load_b128(rN, koff, sbase + soff(i), NT);
        __builtin_amdgcn_sched_barrier(0);
      });
    }
    {
      const float m = wave_max(mrun);
      const float l = wave_sum(lrun * __builtin_amdgcn_exp2f((mrun - m) * L2E));
      if (lane == 0) { lds[L_SM + 2 * w] = m; lds[L_SM + 2 * w + 1] = l; }
    }
    float snext = scw[0];
    const v4u mask = onehot_mask(c);
    lds_barrier();
    float mx, inv;
    {
      const f32x4 s0 = *(const f32x4*)&lds[L_SM], s1 = *(const f32x4*)&lds[L_SM + 4];
      mx = fmaxf(fmaxf(s0.x, s0.z), fmaxf(s1.x, s1.z));
      float l = s0.y * __builtin_amdgcn_exp2f((s0.x - mx) * L2E);
      l = fmaf(s0.w, __builtin_amdgcn_exp2f((s0.z - mx) * L2E), l);
      l = fmaf(s1.y, __builtin_amdgcn_exp2f((s1.x - mx) * L2E), l);
      l = fmaf(s1.w, __builtin_amdgcn_exp2f((s1.z - mx) * L2E), l);
      inv = 1.0f / l;
    }
    f32x4 oacc = {0.f, 0.f, 0.f, 0.f};
#pragma unroll 1
    for (int k = 0; k < 16; ++k) {
      const bool last = k == 15;
      const int voff = last ? OOB : koff;
      const int sbase = scat4((k + 1) & 15, 8);
      const int state = last ? 3 : k == 0 ? 0 : pace(256 + 16 * k + 16);
      unsigned pd = bfbits(__builtin_amdgcn_exp2f((snext - mx) * L2E) * inv);
      pd |= pd << 16;
      snext = scw[((k + 1) & 15) * 64];
      static_for<0, 16>([&](auto I) {
        constexpr int i = I;
        const v4u a = onehot<brev4(i)>(pd, mask);
        oacc = mfma16(a, ring[i], oacc);
        asm volatile("" : "+v"(oacc));
        wait_var(state);
        ring[i] = __builtin_amdgcn_raw_buffer_load_b128(rV, voff, sbase + soff(i), NT);
        __builtin_amdgcn_sched_barrier(0);
      });
    }
    if (lane < 32) *(f32x4*)&lds[L_OP + w * 128 + 8 * c + 4 * g] = oacc;
    lds_barrier();
    if (w == 2) {
      const unsigned tag = ((const unsigned*)lds)[L_TAG];
      unsigned char* wsp = ws;
      asm volatile("" : "+s"(wsp));
      const __amdgpu_buffer_rsrc_t rP = __builtin_amdgcn_make_buffer_rsrc((void*)(wsp + WS_SG + (size_t)bh * 5120), 0, 5120, 0x00020000);
      asm volatile("" ::"s"(rP));
      lds_barrier();
      publish(rP, 256 + lane, tag);
    }
  }
}

extern "C" void kernel_launch(void* const* d_in, const int* in_sizes, int n_in, void* d_out, int out_size, void* d_ws,
                              size_t ws_size, hipStream_t stream) {
  fused_kernel<<<256, 512, 0, stream>>>((const u16*)d_in[0], (const u16*)d_in[1], (const u16*)d_in[2], (const u16*)d_in[3],
                                        (const u16*)d_in[4], (const u16*)d_in[6], (const u16*)d_in[7], (const u16*)d_in[8],
                                        (const u16*)d_in[9], (unsigned char*)d_ws, (u16*)d_out);
}
```

```cpp
#include <hip/hip_runtime.h>
#include <hip/hip_bf16.h>
#include <stdint.h>
#include <type_traits>

typedef unsigned v2u __attribute__((ext_vector_type(2)));
typedef unsigned v4u __attribute__((ext_vector_type(4)));
typedef unsigned v8u __attribute__((ext_vector_type(8)));
typedef float f32x2 __attribute__((ext_vector_type(2)));
typedef float f32x4 __attribute__((ext_vector_type(4)));
typedef __bf16 bf16x2 __attribute__((ext_vector_type(2)));
typedef __bf16 bf16x8 __attribute__((ext_vector_type(8)));
typedef __bf16 bf16x16 __attribute__((ext_vector_type(16)));
typedef unsigned short u16;
typedef __attribute__((address_space(1))) unsigned gu32;
typedef __attribute__((address_space(3))) unsigned lu32;
typedef __attribute__((address_space(3))) f32x2 lf32x2;
typedef __attribute__((address_space(3))) f32x4 lf32x4;

constexpr int D_ = 640, H_ = 32, SK = 4096, DH = 128;
constexpr size_t WS_SG = 0, WS_EPB = 256 * 5120, WS_AN = WS_EPB + 4096;
constexpr unsigned TAG_STEP = 0x9E3779B9u;

constexpr int TLO = 5;
constexpr int TAU256 = 3430;
constexpr int TOFF = 190;
constexpr int DELTA = 35;
constexpr int FAR = 70;
constexpr int NL = 2, NX = 2;
constexpr int WQ_INFL = 3, WO_INFL = 2;
constexpr int NT = 2, SC1 = 16, OOB = (int)0x80000000;

__device__ __forceinline__ float rbf(float x) { return (float)(__bf16)x; }
__device__ __forceinline__ unsigned bfbits(float x) { return (unsigned)__builtin_bit_cast(u16, (__bf16)x); }
__device__ __forceinline__ unsigned bfpack(f32x2 v) { return __builtin_bit_cast(unsigned, __builtin_convertvector(v, bf16x2)); }
__device__ __forceinline__ float bflo(unsigned u) { return __builtin_bit_cast(float, u << 16); }
__device__ __forceinline__ float bfhi(unsigned u) { return __builtin_bit_cast(float, u & 0xffff0000u); }

template <int CTRL> __device__ __forceinline__ float dppf(float x) {
  return __builtin_bit_cast(float, __builtin_amdgcn_mov_dpp(__builtin_bit_cast(int, x), CTRL, 0xf, 0xf, true));
}
template <int CTRL> __device__ __forceinline__ unsigned dppu(unsigned x) {
  return (unsigned)__builtin_amdgcn_mov_dpp((int)x, CTRL, 0xf, 0xf, true);
}
constexpr int XOR1 = 0xB1, XOR2 = 0x4E, XOR7 = 0x141, XOR8 = 0x128, NEWBCAST = 0x150;

__device__ __forceinline__ float swap16_sum(float x) {
  auto s = __builtin_amdgcn_permlane16_swap(__float_as_uint(x), __float_as_uint(x), false, false);
  return __uint_as_float(s[0]) + __uint_as_float(s[1]);
}
__device__ __forceinline__ float swap32_sum(float x) {
  auto t = __builtin_amdgcn_permlane32_swap(__float_as_uint(x), __float_as_uint(x), false, false);
  return __uint_as_float(t[0]) + __uint_as_float(t[1]);
}
__device__ __forceinline__ float wave_sum(float x) {
  x += dppf<XOR1>(x); x += dppf<XOR2>(x); x += dppf<XOR7>(x); x += dppf<XOR8>(x);
  return swap32_sum(swap16_sum(x));
}
__device__ __forceinline__ float wave_max(float x) {
  x = fmaxf(x, dppf<XOR1>(x)); x = fmaxf(x, dppf<XOR2>(x)); x = fmaxf(x, dppf<XOR7>(x)); x = fmaxf(x, dppf<XOR8>(x));
  auto s = __builtin_amdgcn_permlane16_swap(__float_as_uint(x), __float_as_uint(x), false, false);
  x = fmaxf(__uint_as_float(s[0]), __uint_as_float(s[1]));
  auto t = __builtin_amdgcn_permlane32_swap(__float_as_uint(x), __float_as_uint(x), false, false);
  return fmaxf(__uint_as_float(t[0]), __uint_as_float(t[1]));
}
__device__ __forceinline__ float rdlane(float x, int l) { return __builtin_bit_cast(float, __builtin_amdgcn_readlane(__builtin_bit_cast(int, x), l)); }
__device__ __forceinline__ float dot2(unsigned a, unsigned b, float acc) {
  return __builtin_amdgcn_fdot2_f32_bf16(__builtin_bit_cast(bf16x2, a), __builtin_bit_cast(bf16x2, b), acc, false);
}
__device__ __forceinline__ f32x4 mfma16(v4u a, v4u b, f32x4 c) {
  return __builtin_amdgcn_mfma_f32_16x16x32_bf16(__builtin_bit_cast(bf16x8, a), __builtin_bit_cast(bf16x8, b), c, 0, 0, 0);
}
__device__ __forceinline__ f32x4 smfma16(v4u a, v4u b0, v4u b1, f32x4 c, int idx) {
  const v8u b = __builtin_shufflevector(b0, b1, 0, 1, 2, 3, 4, 5, 6, 7);
  return __builtin_amdgcn_smfmac_f32_16x16x64_bf16(__builtin_bit_cast(bf16x8, a), __builtin_bit_cast(bf16x16, b), c, idx, 0, 0);
}
template <int J> __device__ __forceinline__ v4u onehot(unsigned val2, const v4u& mask) {
  const unsigned pb = dppu<NEWBCAST + J>(val2);
  v4u a;
  a.x = pb & mask.x; a.y = pb & mask.y; a.z = pb & mask.z; a.w = pb & mask.w;
  return a;
}
__device__ __forceinline__ v4u onehot_mask(int c) {
  const unsigned mm = (c & 1) ? 0xffff0000u : 0x0000ffffu;
  v4u mask;
  mask.x = (c >> 1) == 0 ? mm : 0u; mask.y = (c >> 1) == 1 ? mm : 0u; mask.z = (c >> 1) == 2 ? mm : 0u; mask.w = (c >> 1) == 3 ? mm : 0u;
  return mask;
}
__device__ __forceinline__ void lds_barrier() {
  asm volatile("s_waitcnt lgkmcnt(0)" ::: "memory");
  __builtin_amdgcn_s_barrier();
  asm volatile("" ::: "memory");
}
__device__ __forceinline__ unsigned lds_get(float* p) { return __hip_atomic_load((lu32*)p, __ATOMIC_RELAXED, __HIP_MEMORY_SCOPE_WORKGROUP); }
__device__ __forceinline__ void lds_put(float* p, unsigned v) { __hip_atomic_store((lu32*)p, v, __ATOMIC_RELAXED, __HIP_MEMORY_SCOPE_WORKGROUP); }
template <int N> __device__ __forceinline__ void wait_vm() { asm volatile("s_waitcnt vmcnt(%0)" ::"i"(N) : "memory"); }
__device__ __forceinline__ void wait_var(int state) {
  asm volatile("s_cmp_eq_u32 %0, 3\n\ts_cbranch_scc1 .Lwc%=\n\ts_waitcnt vmcnt(%3)\n\t"
               "s_cmp_eq_u32 %0, 2\n\ts_cbranch_scc1 .Lwc%=\n\ts_waitcnt vmcnt(%2)\n\t"
               "s_cmp_eq_u32 %0, 1\n\ts_cbranch_scc1 .Lwc%=\n\ts_waitcnt vmcnt(%1)\n.Lwc%=:"
               ::"s"(state), "i"(TLO - 1), "i"(TLO), "i"(TLO + 1) : "memory", "scc");
}
__device__ __forceinline__ void ln_stats(const float (&xv)[10], float& mb, float& sd) {
  float s = 0.f;
#pragma unroll
  for (int i = 0; i < 10; ++i) s += xv[i];
  const float mean = wave_sum(s) * (1.0f / D_);
  float q = 0.f;
#pragma unroll
  for (int i = 0; i < 10; ++i) { const float d = xv[i] - mean; q = fmaf(d, d, q); }
  const float var = rbf(wave_sum(q) * (1.0f / D_));
  mb = rbf(mean);
  sd = rbf(sqrtf(rbf(var + 1e-5f)));
}
__device__ __forceinline__ float ln_apply(float x, float mb, float sd) { return rbf(rbf(x - mb) / sd); }
__device__ __forceinline__ float gelu_bf(float u) {
  constexpr float K1 = -2.0f * 0.7978845608028654f * 1.4426950408889634f;
  constexpr float K2 = K1 * 0.044715f;
  const float e = __builtin_amdgcn_exp2f(u * fmaf(K2, u * u, K1));
  return rbf(u * __builtin_amdgcn_rcpf(1.0f + e));
}
template <int I, int N, typename Fn> __device__ __forceinline__ void static_for(Fn&& f) {
  if constexpr (I < N) { f(std::integral_constant<int, I>{}); static_for<I + 1, N>(f); }
}
constexpr unsigned long long PERM = 0x3502784b1a96ull;
constexpr int ppos(int i) { return (int)((PERM >> (4 * (11 - i))) & 15); }
constexpr int scat4(int v, int i) {
  return (((v & 1) << ppos(i)) | (((v >> 1) & 1) << ppos(i + 1)) | (((v >> 2) & 1) << ppos(i + 2)) | (((v >> 3) & 1) << ppos(i + 3))) * 256;
}
constexpr int soff(int n) { return scat4(n & 15, 4) + scat4(n >> 4, 8); }
constexpr int brev4(int j) { return ((j & 1) << 3) | ((j & 2) << 1) | ((j & 4) >> 1) | ((j & 8) >> 3); }

constexpr int L_S = 0;
constexpr int L_SM = 2560;
constexpr int L_TAG = 2568;
constexpr int L_CNT = 2569;
constexpr int L_AN = 2570;
constexpr int L_POS = 2571;
constexpr int L_TS = 2572;
constexpr int L_GATE = 2576;
constexpr int L_OP = 2704;
constexpr int L_SC = 3216;
constexpr int L_K = L_SC + 4096;
constexpr int L_END = L_K + 4 * 16 * NL * 256;

__global__ void __launch_bounds__(512, 2)
fused_kernel(const u16* __restrict__ x, const u16* __restrict__ kc, const u16* __restrict__ vc,
             const u16* __restrict__ wq, const u16* __restrict__ wo, const u16* __restrict__ w1, const u16* __restrict__ b1,
             const u16* __restrict__ w2, const u16* __restrict__ b2, unsigned char* ws, u16* out) {
  __shared__ __attribute__((aligned(16))) float lds[L_END];
  asm volatile("" ::"s"(x), "s"(kc), "s"(vc), "s"(wq));
  const int bh = blockIdx.x, b = bh >> 5, h = bh & 31;
  const int tid = threadIdx.x, lane = tid & 63, w = __builtin_amdgcn_readfirstlane(tid >> 6);
  const int g = lane >> 4, c = lane & 15;
  constexpr float L2E = 1.4426950408889634f;

  auto publish = [&](__amdgpu_buffer_rsrc_t rP, int t, unsigned tag) __attribute__((always_inline)) {
    const f32x2 sv = (*(const f32x2*)&lds[L_S + 2 * t] + *(const f32x2*)&lds[L_S + 640 + 2 * t]) +
                     (*(const f32x2*)&lds[L_S + 1280 + 2 * t] + *(const f32x2*)&lds[L_S + 1920 + 2 * t]);
    v4u gv; gv.x = __float_as_uint(sv.x); gv.y = tag; gv.z = __float_as_uint(sv.y); gv.w = tag;
    __builtin_amdgcn_raw_buffer_store_b128(gv, rP, t * 16, 0, SC1);
  };

  if (w >= 4) {
    __builtin_amdgcn_s_setprio(3);
    const int hw = w - 4, ht = tid - 256;
    gu32* epb = (gu32*)(ws + WS_EPB) + b * 64;
    const unsigned* x32 = (const unsigned*)(x + b * D_);
    const u16* xb = x + b * D_;
    unsigned xr[5];
#pragma unroll
    for (int i = 0; i < 5; ++i) xr[i] = x32[lane + 64 * i];
    const unsigned xo0 = xb[160 * hw + 4 * c + g], xo1 = xb[160 * hw + 64 + 4 * c + g], xo2 = xb[160 * hw + 128 + 4 * (c & 7) + g];
    __builtin_amdgcn_sched_barrier(0);
    v4u pre[5];
    {
      const __amdgpu_buffer_rsrc_t rW = __builtin_amdgcn_make_buffer_rsrc((void*)wq, 0, D_ * H_ * DH * 2, 0x00020000);
      const int pi = (bh >> 3) * 4 + hw;
      const int po = ((pi >> 1) * 2 + (g >> 1)) * 8192 + ((h & 7) + 16 * (pi & 1) + 8 * (g & 1)) * 256 + c * 16;
#pragma unroll
      for (int j = 0; j < 5; ++j) { pre[j] = __builtin_amdgcn_raw_buffer_load_b128(rW, po, j * 128 * 8192, 0); __builtin_amdgcn_sched_barrier(0); }
    }
    const __amdgpu_buffer_rsrc_t rQ = __builtin_amdgcn_make_buffer_rsrc((void*)(wq + h * DH), 0, D_ * H_ * DH * 2 - h * DH * 2, 0x00020000);
    const int qoff = (160 * hw + g) * (H_ * DH * 2) + c * 16;
    v4u wr[40];
    static_for<0, 2 * WQ_INFL>([&](auto T) {
      constexpr int t = T;
      if constexpr (t >= WQ_INFL) wait_vm<WQ_INFL - 1>();
      wr[t] = __builtin_amdgcn_raw_buffer_load_b128(rQ, qoff, t * 4 * (H_ * DH * 2), 0);
      __builtin_amdgcn_sched_barrier(0);
    });
    const v4u mask = onehot_mask(c);
    float xv[10];
#pragma unroll
    for (int i = 0; i < 5; ++i) { xv[2 * i] = bflo(xr[i]); xv[2 * i + 1] = bfhi(xr[i]); }
    float mb, sd;
    ln_stats(xv, mb, sd);
    asm volatile("" : "+v"(mb), "+v"(sd) : "v"(pre[0]), "v"(pre[1]), "v"(pre[2]), "v"(pre[3]), "v"(pre[4]));
    static_for<2 * WQ_INFL, 3 * WQ_INFL>([&](auto T) {
      constexpr int t = T;
      wait_vm<WQ_INFL - 1>();
      wr[t] = __builtin_amdgcn_raw_buffer_load_b128(rQ, qoff, t * 4 * (H_ * DH * 2), 0);
      __builtin_amdgcn_sched_barrier(0);
    });
    unsigned xn0 = bfbits(ln_apply(bflo(xo0), mb, sd)), xn1 = bfbits(ln_apply(bflo(xo1), mb, sd)), xn2 = bfbits(ln_apply(bflo(xo2), mb, sd));
    xn0 |= xn0 << 16; xn1 |= xn1 << 16; xn2 |= xn2 << 16;
    asm volatile("" : "+v"(xn0), "+v"(xn1), "+v"(xn2));
    {
      f32x4 acc = {0.f, 0.f, 0.f, 0.f};
      static_for<0, 40>([&](auto T) {
        constexpr int t = T, n = t + 3 * WQ_INFL;
        if constexpr (n < 40) {
          wait_vm<WQ_INFL - 1 + (n % 9 == 0 ? 1 : 0)>();
          wr[n] = __builtin_amdgcn_raw_buffer_load_b128(rQ, qoff, n * 4 * (H_ * DH * 2), 0);
        }
        const v4u a = onehot<(t & 15)>(t < 16 ? xn0 : t < 32 ? xn1 : xn2, mask);
        acc = mfma16(a, wr[t], acc);
        asm volatile("" : "+v"(acc));
        __builtin_amdgcn_sched_barrier(0);
      });
      if (lane < 32) *(f32x4*)&lds[L_S + hw * 128 + 8 * c + 4 * g] = acc;
    }
    lds_barrier();
    __builtin_amdgcn_sched_barrier(0);
    const int ncol = h < 8 ? 32 : 16;
    const unsigned xg = xb[16 * h + (lane & 15) + ((lane & 16) && h < 8 ? 512 : 0)];
    const int gf = ht % 192;
    const v4u gw1 = ((const v4u*)w1)[gf], gb1 = ((const v4u*)b1)[gf], gw2 = ((const v4u*)w2)[gf];
    const unsigned ew1 = w1[1536 + gf], eb1 = b1[1536 + gf], ew2 = w2[1536 + gf];
    const unsigned b2v = b2[0];
    const unsigned tag = __hip_atomic_load(epb, __ATOMIC_RELAXED, __HIP_MEMORY_SCOPE_AGENT) + TAG_STEP;
    const int cgrp = (hw == 1 && h < 8) ? 32 + h : h;
    const unsigned xres = x32[8 * cgrp + (lane & 7)];
    __builtin_amdgcn_sched_barrier(0);
    const __amdgpu_buffer_rsrc_t rO = __builtin_amdgcn_make_buffer_rsrc((void*)(wo + (size_t)h * DH * D_), 0, DH * D_ * 2, 0x00020000);
    const int ooff = (32 * hw + g) * (D_ * 2) + c * 16;
    {
      const int pi = b * 4 + hw, pg = pi & 15;
      const int po = (pi >> 4) * 16384 + ((pg & 7) + ((pg >> 3) << 4) + ((g & 1) << 3) + ((g >> 1) << 5)) * 256 + c * 16;
#pragma unroll
      for (int j = 0; j < 5; ++j) { pre[j] = __builtin_amdgcn_raw_buffer_load_b128(rO, po, j * 32768, 0); __builtin_amdgcn_sched_barrier(0); }
      asm volatile("" ::"v"(pre[0]), "v"(pre[1]), "v"(pre[2]), "v"(pre[3]), "v"(pre[4]));
      __builtin_amdgcn_s_sleep(40);
      __builtin_amdgcn_sched_barrier(0);
    }
    static_for<0, 40>([&](auto I) {
      constexpr int i = I;
      if constexpr (i >= WO_INFL) wait_vm<WO_INFL - 1>();
      wr[i] = __builtin_amdgcn_raw_buffer_load_b128(rO, ooff, (i / 5) * 4 * (D_ * 2) + (i % 5) * 256, 0);
      __builtin_amdgcn_sched_barrier(0);
    });
    if (hw == 1) {
      const unsigned char* tp = lane == 0 ? (const unsigned char*)(vc + (size_t)bh * SK * DH)
                              : lane == 1 ? (const unsigned char*)(vc + (size_t)(bh + 1) * SK * DH) - 128
                              : lane == 2 ? (const unsigned char*)out
                              : lane == 3 ? ws + WS_SG + (size_t)b * 32 * 5120
                              : ws + WS_SG + (size_t)(b + 1) * 32 * 5120 - 16;
      const unsigned tv = __hip_atomic_load((gu32*)tp, __ATOMIC_RELAXED, __HIP_MEMORY_SCOPE_AGENT);
      asm volatile("" ::"v"(tv));
    }
    if (ht == 0) { ((unsigned*)lds)[L_TAG] = tag; ((unsigned*)lds)[L_CNT] = 0u; }
    auto pacer = [&](int round, unsigned pos) __attribute__((always_inline)) {
      const __amdgpu_buffer_rsrc_t rA = __builtin_amdgcn_make_buffer_rsrc((void*)(ws + WS_AN + round * 4096), 0, 4096, 0x00020000);
      unsigned at = 0;
      for (int it = 0; it < 4096; ++it) {
        at = __builtin_amdgcn_readfirstlane(lds_get(&lds[L_POS]));
        if (at >= pos) break;
        __builtin_amdgcn_s_sleep(8);
      }
      if (at > pos + 16u) return;
      __builtin_amdgcn_s_sleep(16);
      const unsigned tv = lds_get(&lds[L_TS + (lane & 3)]);
      const unsigned ref = __builtin_amdgcn_readfirstlane(tv);
      int d = (int)(tv - ref);
      d += (int)dppu<XOR1>((unsigned)d); d += (int)dppu<XOR2>((unsigned)d);
      const unsigned mine = ref + (unsigned)(d >> 2);
      v4u gv; gv.x = mine; gv.y = mine; gv.z = mine; gv.w = mine;
      __builtin_amdgcn_raw_buffer_store_b128(gv, rA, ((h >> 2) == b && lane < 8) ? h * 128 + lane * 16 : OOB, 0, SC1);
      __builtin_amdgcn_s_sleep(110);
      asm volatile("" ::: "memory");
      const int r = (int)(__builtin_amdgcn_raw_buffer_load_b32(rA, lane < 32 ? lane * 128 : OOB, 0, SC1) - mine);
      const bool ok = lane < 32 && r > -1000 && r < 1000;
      const int cnt = __builtin_popcountll(__builtin_amdgcn_ballot_w64(ok));
      const int mean = (int)(wave_sum(ok ? (float)r : 0.f) / (float)(cnt > 0 ? cnt : 1));
      if (cnt >= 16) lds_put(&lds[L_AN], mine + (unsigned)(mean + DELTA));
    };
    if (hw == 3) pacer(0, 208);
    lds_barrier();
    if (hw == 3) {
#pragma unroll 1
      for (int r = 1; r < 3; ++r) pacer(r, r == 1 ? 304 : 400);
    }
    __builtin_amdgcn_s_setprio(0);
    if (hw < 3) {
      float mb2, sd2;
#pragma unroll
      for (int i = 0; i < 10; ++i) xv[i] = ln_apply(xv[i], mb, sd);
      ln_stats(xv, mb2, sd2);
      const float hcol = ln_apply(ln_apply(bflo(xg), mb, sd), mb2, sd2);
      float w1v[9], b1v[9], w2v[9];
#pragma unroll
      for (int i = 0; i < 4; ++i) {
        w1v[2 * i] = bflo(gw1[i]); w1v[2 * i + 1] = bfhi(gw1[i]);
        b1v[2 * i] = bflo(gb1[i]); b1v[2 * i + 1] = bfhi(gb1[i]);
        w2v[2 * i] = bflo(gw2[i]); w2v[2 * i + 1] = bfhi(gw2[i]);
      }
      w1v[8] = bflo(ew1); b1v[8] = bflo(eb1); w2v[8] = bflo(ew2);
      float ycol = 0.f;
#pragma unroll 1
      for (int j = 0; j < ncol; ++j) {
        const float hv = rdlane(hcol, j);
        float y = 0.f;
#pragma unroll
        for (int i = 0; i < 9; ++i) y = fmaf(gelu_bf(rbf(rbf(hv * w1v[i]) + b1v[i])), w2v[i], y);
        y = wave_sum(y);
        ycol = (lane == j) ? y : ycol;
      }
      if (lane < 32) lds[L_GATE + hw * 32 + lane] = ycol;
      asm volatile("s_waitcnt lgkmcnt(0)" ::: "memory");
      if (lane == 0) __hip_atomic_fetch_add((lu32*)&lds[L_CNT], 1u, __ATOMIC_RELAXED, __HIP_MEMORY_SCOPE_WORKGROUP);
      while (lds_get(&lds[L_CNT]) < 3u) __builtin_amdgcn_s_sleep(2);
      asm volatile("" ::: "memory");
    }
    __builtin_amdgcn_s_setprio(3);
    float gt0, gt1;
    {
      const int cc = (cgrp >= 32 ? 16 : 0) + 2 * (lane & 7);
      float y0 = (lds[L_GATE + cc] + lds[L_GATE + 32 + cc]) + lds[L_GATE + 64 + cc];
      float y1 = (lds[L_GATE + cc + 1] + lds[L_GATE + 33 + cc]) + lds[L_GATE + 65 + cc];
      y0 = rbf(rbf(y0) + bflo(b2v)); y1 = rbf(rbf(y1) + bflo(b2v));
      gt0 = rbf(y0 / (1.0f + __expf(-y0))); gt1 = rbf(y1 / (1.0f + __expf(-y1)));
    }
    int goff = (lane >> 3) * 5120 + cgrp * 128 + (lane & 7) * 16;
    float xr0 = bflo(xres), xr1 = bfhi(xres);
    v4u smask;
    smask.x = (c >> 2) == 0 ? 0x0000ffffu : 0u; smask.y = (c >> 2) == 1 ? 0x0000ffffu : 0u;
    smask.z = smask.x << 16; smask.w = smask.y << 16;
    int sidx = (c & 3) * 0x5555;
    lf32x2* op = (lf32x2*)&lds[L_OP + 32 * hw + 8 * (c & 3) + 2 * g];
    lf32x4* sp = (lf32x4*)&lds[L_S + hw * 640 + 8 * c + 4 * (g & 1)];
    f32x4 sacc[5];
#pragma unroll
    for (int u = 0; u < 5; ++u) { sacc[u] = f32x4{0.f, 0.f, 0.f, 0.f}; asm volatile("" : "+v"(sacc[u])); }
    asm volatile("" : "+v"(gt0), "+v"(gt1), "+v"(goff), "+v"(smask), "+v"(xr0), "+v"(xr1), "+v"(sidx), "+v"(op), "+v"(sp));
    const __amdgpu_buffer_rsrc_t rG = __builtin_amdgcn_make_buffer_rsrc((void*)(ws + WS_SG + (size_t)b * 32 * 5120), 0, 32 * 5120, 0x00020000);
    const __amdgpu_buffer_rsrc_t rOut = __builtin_amdgcn_make_buffer_rsrc((void*)(out + b * D_ + 16 * cgrp), 0, 32, 0x00020000);
    const __amdgpu_buffer_rsrc_t rP = __builtin_amdgcn_make_buffer_rsrc((void*)(ws + WS_SG + (size_t)bh * 5120), 0, 5120, 0x00020000);
    asm volatile("" ::"s"(rG), "s"(rOut), "s"(rP));
    __builtin_amdgcn_s_waitcnt(0x0F70);
    lds_barrier();
    const unsigned od = bfpack((op[0] + op[64]) + (op[128] + op[192]));
    static_for<0, 4>([&](auto T) {
      constexpr int t = T;
      const v4u a = onehot<t>(od, smask);
#pragma unroll
      for (int u = 0; u < 5; ++u) sacc[u] = smfma16(a, wr[10 * t + u], wr[10 * t + 5 + u], sacc[u], sidx);
    });
    if (lane < 32) {
#pragma unroll
      for (int u = 0; u < 5; ++u) sp[u * 32] = sacc[u];
    }
    lds_barrier();
    publish(rP, ht, tag);
    if (hw == 0 || cgrp >= 32) {
      v4u gr[4];
      __builtin_amdgcn_s_sleep(22);
#pragma unroll 1
      for (int spin = 0; spin < (1 << 17); ++spin) {
        asm volatile("" : "+v"(goff)::"memory");
#pragma unroll
        for (int i = 0; i < 4; ++i) gr[i] = __builtin_amdgcn_raw_buffer_load_b128(rG, goff, i * 8 * 5120, SC1);
        unsigned bad = 0;
#pragma unroll
        for (int i = 0; i < 4; ++i) bad |= (gr[i].y ^ tag) | (gr[i].w ^ tag);
        asm volatile("" : "+v"(bad));
        if (__all(bad == 0u)) break;
        __builtin_amdgcn_s_sleep(1);
      }
      float s0 = (__uint_as_float(gr[0].x) + __uint_as_float(gr[1].x)) + (__uint_as_float(gr[2].x) + __uint_as_float(gr[3].x));
      float s1 = (__uint_as_float(gr[0].z) + __uint_as_float(gr[1].z)) + (__uint_as_float(gr[2].z) + __uint_as_float(gr[3].z));
      s0 += dppf<XOR8>(s0); s1 += dppf<XOR8>(s1);
      s0 = swap32_sum(swap16_sum(s0)); s1 = swap32_sum(swap16_sum(s1));
      const unsigned o0 = bfbits(xr0 + rbf(rbf(s0) * gt0)), o1 = bfbits(xr1 + rbf(rbf(s1) * gt1));
      __builtin_amdgcn_raw_buffer_store_b32(o0 | (o1 << 16), rOut, lane * 4, 0, SC1);
      if (cgrp == 0 && lane == 0) __hip_atomic_store(epb, tag, __ATOMIC_RELAXED, __HIP_MEMORY_SCOPE_AGENT);
    }
    return;
  }

  {
    __builtin_amdgcn_s_setprio(1);
    const __amdgpu_buffer_rsrc_t rK = __builtin_amdgcn_make_buffer_rsrc((void*)(kc + (size_t)bh * SK * DH), 0, SK * DH * 2, 0x00020000);
    const __amdgpu_buffer_rsrc_t rV = __builtin_amdgcn_make_buffer_rsrc((void*)(vc + (size_t)bh * SK * DH), 0, SK * DH * 2, 0x00020000);
    const int koff = scat4(tid >> 4, 0) + c * 16;
    v4u* lk = (v4u*)&lds[L_K] + w * (16 * NL * 64) + lane;
    unsigned t0;
    v4u stg[8], land[16 * NX], ring[16];
    static_for<0, 16 * (NL + NX + 1)>([&](auto N) {
      constexpr int n = N;
      if constexpr (n >= TLO && n < 16 * (NL + NX + 1) - 2) wait_vm<TLO - 1>();
      const v4u kv = __builtin_amdgcn_raw_buffer_load_b128(rK, koff, soff(n), NT);
      if constexpr (n < 16 * NL) stg[n & 7] = kv;
      else if constexpr (n < 16 * (NL + NX)) land[n - 16 * NL] = kv;
      else ring[n & 15] = kv;
      if constexpr (n >= 6 && n - 6 < 16 * NL) lk[(n - 6) * 64] = stg[(n - 6) & 7];
      if constexpr (n == TLO - 1) t0 = (unsigned)__builtin_amdgcn_s_memrealtime();
      __builtin_amdgcn_sched_barrier(0);
    });
    if (lane == 0) { ((unsigned*)lds)[L_TS + w] = t0; ((unsigned*)lds)[L_AN] = t0 + TOFF; ((unsigned*)lds)[L_POS] = 0u; }
    lds_barrier();
    v4u qp;
    {
      f32x4 s0 = *(const f32x4*)&lds[L_S + 8 * c], s1 = *(const f32x4*)&lds[L_S + 8 * c + 4];
#pragma unroll
      for (int ww = 1; ww < 4; ++ww) { s0 += *(const f32x4*)&lds[L_S + ww * 128 + 8 * c]; s1 += *(const f32x4*)&lds[L_S + ww * 128 + 8 * c + 4]; }
      qp.x = bfbits(s0.x) | (bfbits(s0.y) << 16); qp.y = bfbits(s0.z) | (bfbits(s0.w) << 16);
      qp.z = bfbits(s1.x) | (bfbits(s1.y) << 16); qp.w = bfbits(s1.z) | (bfbits(s1.w) << 16);
    }
    auto pace = [&](int n) __attribute__((always_inline)) -> int {
      const unsigned anchor = lds_get(&lds[L_AN]);
      const unsigned v = (unsigned)__builtin_amdgcn_s_memrealtime() - (unsigned)((n * TAU256) >> 8);
      lds_put(&lds[L_TS + w], v);
      lds_put(&lds[L_POS], (unsigned)n);
      const int late = (int)(v - (unsigned)__builtin_amdgcn_readfirstlane(anchor));
      return late < 0 ? 0 : late < FAR ? 1 : 2;
    };
    float* scw = &lds[L_SC + w * 1024 + lane];
    float mrun = -__builtin_inff(), lrun = 0.f;
    float ra[4], rb[4];
    auto kstep = [&](auto J, const v4u& kv, float (&r)[4], int grp) __attribute__((always_inline)) {
      constexpr int j = J;
      float d = dot2(kv.x, qp.x, 0.f);
      d = dot2(kv.y, qp.y, d); d = dot2(kv.z, qp.z, d); d = dot2(kv.w, qp.w, d);
      d += dppf<XOR8>(d);
      if constexpr (!(j & 1)) { r[0] = d; asm volatile("" : "+v"(r[0])); return; }
      d = (c & 8) ? d : r[0];
      d += dppf<XOR7>(d);
      if constexpr (!(j & 2)) { r[1] = d; asm volatile("" : "+v"(r[1])); return; }
      d = (c & 4) ? d : r[1];
      d += dppf<XOR2>(d);
      if constexpr (!(j & 4)) { r[2] = d; asm volatile("" : "+v"(r[2])); return; }
      d = (c & 2) ? d : r[2];
      d += dppf<XOR1>(d);
      if constexpr (!(j & 8)) { r[3] = d; asm volatile("" : "+v"(r[3])); return; }
      d = (c & 1) ? d : r[3];
      const float s = rbf(rbf(d) * 0.08838834764831845f);
      scw[grp * 64] = s;
      const float mn = fmaxf(fmaxf(mrun, s), -3.4e38f);
      lrun = fmaf(lrun, __builtin_amdgcn_exp2f((mrun - mn) * L2E), __builtin_amdgcn_exp2f((s - mn) * L2E));
      mrun = mn;
      asm volatile("" : "+v"(mrun), "+v"(lrun));
    };
    static_for<0, 16 * NX>([&](auto M) {
      constexpr int m = M, i = m & 15, k = NL + NX + (m >> 4);
      kstep(std::integral_constant<int, i>{}, ring[i], ra, k);
      wait_vm<TLO - 1>();
      ring[i] = __builtin_amdgcn_raw_buffer_load_b128(rK, koff, soff(16 * (k + 1) + i), NT);
      kstep(std::integral_constant<int, i>{}, land[m], rb, NL + (m >> 4));
      __builtin_amdgcn_sched_barrier(0);
    });
#pragma unroll 1
    for (int kk = 0; kk < NL; ++kk) {
      const int k = NL + 2 * NX + kk;
      int state = pace(16 * k + 16);
      if (kk == 0) state = 1;
      static_for<0, 16>([&](auto I) {
        constexpr int i = I;
        const v4u kv = lk[(kk * 16 + i) * 64];
        kstep(I, ring[i], ra, k);
        wait_var(state);
        ring[i] = __builtin_amdgcn_raw_buffer_load_b128(rK, koff, scat4(k + 1, 8) + soff(i), NT);
        kstep(I, kv, rb, kk);
        __builtin_amdgcn_sched_barrier(0);
      });
    }
#pragma unroll 1
    for (int k = 2 * (NL + NX); k < 16; ++k) {
      const bool last = k == 15;
      const __amdgpu_buffer_rsrc_t rN = last ? rV : rK;
      const int sbase = scat4((k + 1) & 15, 8);
      int state = last ? 3 : pace(16 * k + 16);
      if (k == 2 * (NL + NX)) state = 1;
      static_for<0, 16>([&](auto I) {
        constexpr int i = I;
        kstep(I, ring[i], ra, k);
        wait_var(state);
        ring[i] = __builtin_amdgcn_raw_buffer_load_b128(rN, koff, sbase + soff(i), NT);
        __builtin_amdgcn_sched_barrier(0);
      });
    }
    {
      const float m = wave_max(mrun);
      const float l = wave_sum(lrun * __builtin_amdgcn_exp2f((mrun - m) * L2E));
      if (lane == 0) { lds[L_SM + 2 * w] = m; lds[L_SM + 2 * w + 1] = l; }
    }
    float snext = scw[0];
    const v4u mask = onehot_mask(c);
    lds_barrier();
    float mx, inv;
    {
      const f32x4 s0 = *(const f32x4*)&lds[L_SM], s1 = *(const f32x4*)&lds[L_SM + 4];
      mx = fmaxf(fmaxf(s0.x, s0.z), fmaxf(s1.x, s1.z));
      float l = s0.y * __builtin_amdgcn_exp2f((s0.x - mx) * L2E);
      l = fmaf(s0.w, __builtin_amdgcn_exp2f((s0.z - mx) * L2E), l);
      l = fmaf(s1.y, __builtin_amdgcn_exp2f((s1.x - mx) * L2E), l);
      l = fmaf(s1.w, __builtin_amdgcn_exp2f((s1.z - mx) * L2E), l);
      inv = 1.0f / l;
    }
    f32x4 oacc = {0.f, 0.f, 0.f, 0.f};
#pragma unroll 1
    for (int k = 0; k < 16; ++k) {
      const bool last = k == 15;
      const int voff = last ? OOB : koff;
      const int sbase = scat4((k + 1) & 15, 8);
      const int state = last ? 3 : k == 0 ? 0 : pace(256 + 16 * k + 16);
      unsigned pd = bfbits(__builtin_amdgcn_exp2f((snext - mx) * L2E) * inv);
      pd |= pd << 16;
      snext = scw[((k + 1) & 15) * 64];
      static_for<0, 16>([&](auto I) {
        constexpr int i = I;
        const v4u a = onehot<brev4(i)>(pd, mask);
        oacc = mfma16(a, ring[i], oacc);
        asm volatile("" : "+v"(oacc));
        wait_var(state);
        ring[i] = __builtin_amdgcn_raw_buffer_load_b128(rV, voff, sbase + soff(i), NT);
        __builtin_amdgcn_sched_barrier(0);
      });
    }
    if (lane < 32) *(f32x4*)&lds[L_OP + w * 128 + 8 * c + 4 * g] = oacc;
    lds_barrier();
    if (w == 2) {
      const unsigned tag = ((const unsigned*)lds)[L_TAG];
      unsigned char* wsp = ws;
      asm volatile("" : "+s"(wsp));
      const __amdgpu_buffer_rsrc_t rP = __builtin_amdgcn_make_buffer_rsrc((void*)(wsp + WS_SG + (size_t)bh * 5120), 0, 5120, 0x00020000);
      asm volatile("" ::"s"(rP));
      lds_barrier();
      publish(rP, 256 + lane, tag);
    }
  }
}

extern "C" void kernel_launch(void* const* d_in, const int* in_sizes, int n_in, void* d_out, int out_size, void* d_ws,
                              size_t ws_size, hipStream_t stream) {
  fused_kernel<<<256, 512, 0, stream>>>((const u16*)d_in[0], (const u16*)d_in[1], (const u16*)d_in[2], (const u16*)d_in[3],
                                        (const u16*)d_in[4], (const u16*)d_in[6], (const u16*)d_in[7], (const u16*)d_in[8],
                                        (const u16*)d_in[9], (unsigned char*)d_ws, (u16*)d_out);
}
```

```cpp
#include <hip/hip_runtime.h>
#include <hip/hip_bf16.h>
#include <stdint.h>

typedef unsigned v4u __attribute__((ext_vector_type(4)));
typedef unsigned v2u __attribute__((ext_vector_type(2)));
typedef float f32x4 __attribute__((ext_vector_type(4)));
typedef __bf16 bf16x8 __attribute__((ext_vector_type(8)));
typedef __bf16 bf16x2 __attribute__((ext_vector_type(2)));
typedef unsigned short u16;
typedef __attribute__((address_space(1))) unsigned gu32;
typedef __attribute__((address_space(1))) unsigned long long gu64;
constexpr size_t WS_EPOCH = 0, WS_GRAN = 65536;

constexpr int B_ = 8, D_ = 640, H_ = 32, SK = 4096, DH = 128, F_ = 1728;
#define RING 32
#define K_AUX 2
#define V_AUX 2
#define W_AUX 0
#define VTHR 256
#define WO_INFL 4
#define HPRIO 3

__device__ __forceinline__ float rbf(float x) { return (float)(__bf16)x; }
__device__ __forceinline__ unsigned bfbits(float x) { return (unsigned)__builtin_bit_cast(u16, (__bf16)x); }
__device__ __forceinline__ float bflo(unsigned u) { return __builtin_bit_cast(float, u << 16); }
__device__ __forceinline__ float bfhi(unsigned u) { return __builtin_bit_cast(float, u & 0xffff0000u); }

template <int CTRL> __device__ __forceinline__ float dppf(float x) {
  return __builtin_bit_cast(float, __builtin_amdgcn_mov_dpp(__builtin_bit_cast(int, x), CTRL, 0xf, 0xf, true));
}
template <int CTRL> __device__ __forceinline__ unsigned dppu(unsigned x) {
  return (unsigned)__builtin_amdgcn_mov_dpp((int)x, CTRL, 0xf, 0xf, true);
}
constexpr int XOR1 = 0xB1, XOR2 = 0x4E, XOR7 = 0x141, XOR8 = 0x128, NEWBCAST = 0x150;

__device__ __forceinline__ float wave_sum(float x) {
  x += dppf<XOR1>(x); x += dppf<XOR2>(x); x += dppf<XOR7>(x); x += dppf<XOR8>(x);
  auto s = __builtin_amdgcn_permlane16_swap(__float_as_uint(x), __float_as_uint(x), false, false);
  x = __uint_as_float(s[0]) + __uint_as_float(s[1]);
  auto t = __builtin_amdgcn_permlane32_swap(__float_as_uint(x), __float_as_uint(x), false, false);
  return __uint_as_float(t[0]) + __uint_as_float(t[1]);
}
__device__ __forceinline__ float rows_sum(float x) {
  auto s = __builtin_amdgcn_permlane16_swap(__float_as_uint(x), __float_as_uint(x), false, false);
  x = __uint_as_float(s[0]) + __uint_as_float(s[1]);
  auto t = __builtin_amdgcn_permlane32_swap(__float_as_uint(x), __float_as_uint(x), false, false);
  return __uint_as_float(t[0]) + __uint_as_float(t[1]);
}
__device__ __forceinline__ float wave_max(float x) {
  x = fmaxf(x, dppf<XOR1>(x)); x = fmaxf(x, dppf<XOR2>(x)); x = fmaxf(x, dppf<XOR7>(x)); x = fmaxf(x, dppf<XOR8>(x));
  auto s = __builtin_amdgcn_permlane16_swap(__float_as_uint(x), __float_as_uint(x), false, false);
  x = fmaxf(__uint_as_float(s[0]), __uint_as_float(s[1]));
  auto t = __builtin_amdgcn_permlane32_swap(__float_as_uint(x), __float_as_uint(x), false, false);
  return fmaxf(__uint_as_float(t[0]), __uint_as_float(t[1]));
}
__device__ __forceinline__ float dot2(unsigned a, unsigned b, float acc) {
  return __builtin_amdgcn_fdot2_f32_bf16(__builtin_bit_cast(bf16x2, a), __builtin_bit_cast(bf16x2, b), acc, false);
}
__device__ __forceinline__ f32x4 mfma16(v4u a, v4u b, f32x4 c) {
  return __builtin_amdgcn_mfma_f32_16x16x32_bf16(__builtin_bit_cast(bf16x8, a), __builtin_bit_cast(bf16x8, b), c, 0, 0, 0);
}
template <int J> __device__ __forceinline__ v4u onehot(unsigned pdup, const v4u& mask) {
  const unsigned pb = dppu<NEWBCAST + J>(pdup);
  v4u a;
  a.x = pb & mask.x; a.y = pb & mask.y; a.z = pb & mask.z; a.w = pb & mask.w;
  return a;
}
__device__ __forceinline__ void lds_barrier() {
  asm volatile("s_waitcnt lgkmcnt(0)" ::: "memory");
  __builtin_amdgcn_s_barrier();
  asm volatile("" ::: "memory");
}
__device__ __forceinline__ void ln_stats(const float (&xv)[10], float& mb, float& sd) {
  float s = 0.f;
#pragma unroll
  for (int i = 0; i < 10; ++i) s += xv[i];
  s = wave_sum(s);
  const float mean = s * (1.0f / D_);
  float q = 0.f;
#pragma unroll
  for (int i = 0; i < 10; ++i) { const float d = xv[i] - mean; q = fmaf(d, d, q); }
  q = wave_sum(q);
  const float var = rbf(q * (1.0f / D_));
  mb = rbf(mean);
  sd = rbf(sqrtf(rbf(var + 1e-5f)));
}
__device__ __forceinline__ float ln_apply(float x, float mb, float sd) { return rbf(rbf(x - mb) / sd); }

template <int I, int N, typename Fn> __device__ __forceinline__ void static_for(Fn&& f) {
  if constexpr (I < N) { f(std::integral_constant<int, I>{}); static_for<I + 1, N>(f); }
}
__device__ __forceinline__ float gelu_bf(float u) {
  constexpr float K1 = -2.0f * 0.7978845608028654f * 1.4426950408889634f;
  constexpr float K2 = K1 * 0.044715f;
  const float e = __builtin_amdgcn_exp2f(u * fmaf(K2, u * u, K1));
  return rbf(u * __builtin_amdgcn_rcpf(1.0f + e));
}

#define ST(i) do {} while (0)

constexpr int L_SM = 2560, L_GATE = 2576, L_OP = 2688, L_END = L_OP + 512;

__global__ void __launch_bounds__(512, 2)
attn_kernel(const u16* __restrict__ x, const u16* __restrict__ kc, const u16* __restrict__ vc,
            const u16* __restrict__ wq, const u16* __restrict__ wo, const u16* __restrict__ w1, const u16* __restrict__ b1,
            const u16* __restrict__ w2, const u16* __restrict__ b2, unsigned char* ws, u16* __restrict__ out) {
  __shared__ __attribute__((aligned(16))) float lds[L_END];
  const int bh = blockIdx.x, b = bh >> 5, h = bh & 31;
  const int tid = threadIdx.x, lane = tid & 63, w = __builtin_amdgcn_readfirstlane(tid >> 6);
  const int g = lane >> 4, c = lane & 15;
  ST(0);
  const unsigned long long tstart = __builtin_amdgcn_s_memrealtime();
  v4u mask;
  {
    const unsigned m = (c & 1) ? 0xffff0000u : 0x0000ffffu;
    mask.x = (c >> 1) == 0 ? m : 0u; mask.y = (c >> 1) == 1 ? m : 0u; mask.z = (c >> 1) == 2 ? m : 0u; mask.w = (c >> 1) == 3 ? m : 0u;
  }
  constexpr float L2E = 1.4426950408889634f;

  if (w < 4) {
    auto swave = [&](auto VAUX) __attribute__((always_inline)) {
    constexpr int vaux = VAUX;
    const __amdgpu_buffer_rsrc_t rK = __builtin_amdgcn_make_buffer_rsrc((void*)(kc + (size_t)bh * SK * DH), 0, SK * DH * 2, 0x00020000);
    const __amdgpu_buffer_rsrc_t rV = __builtin_amdgcn_make_buffer_rsrc((void*)(vc + (size_t)bh * SK * DH), 0, SK * DH * 2, 0x00020000);
    v4u ring[RING];
    const int koff = w * 1024 + lane * 16;
#pragma unroll
    for (int i = 0; i < RING; ++i) { ring[i] = __builtin_amdgcn_raw_buffer_load_b128(rK, koff, i * 4096, K_AUX); __builtin_amdgcn_sched_barrier(0); }
    ST(1);
    lds_barrier();
    ST(2);
    v4u qp;
    {
      f32x4 s0 = *(const f32x4*)&lds[8 * c], s1 = *(const f32x4*)&lds[8 * c + 4];
#pragma unroll
      for (int ww = 1; ww < 4; ++ww) {
        s0 += *(const f32x4*)&lds[ww * 128 + 8 * c];
        s1 += *(const f32x4*)&lds[ww * 128 + 8 * c + 4];
      }
      qp.x = bfbits(s0.x) | (bfbits(s0.y) << 16); qp.y = bfbits(s0.z) | (bfbits(s0.w) << 16);
      qp.z = bfbits(s1.x) | (bfbits(s1.y) << 16); qp.w = bfbits(s1.z) | (bfbits(s1.w) << 16);
    }
    constexpr int NIT = 256 / RING, GPI = RING / 16;
    float sc[16];
#pragma unroll
    for (int i = 0; i < 16; ++i) sc[i] = 0.f;
    auto kbody = [&](auto LAST, int sbase) {
      constexpr bool last = LAST;
      float nsc[GPI];
#pragma unroll
      for (int gi = 0; gi < GPI; ++gi) {
        float xx[16];
#pragma unroll
        for (int j = 0; j < 16; ++j) {
          const int i = gi * 16 + j;
          float d = dot2(ring[i].x, qp.x, 0.f);
          d = dot2(ring[i].y, qp.y, d); d = dot2(ring[i].z, qp.z, d); d = dot2(ring[i].w, qp.w, d);
          asm volatile("" : "+v"(d));
          xx[j] = d;
          if constexpr (last) ring[i] = __builtin_amdgcn_raw_buffer_load_b128(rV, koff, i * 4096, vaux);
          else ring[i] = __builtin_amdgcn_raw_buffer_load_b128(rK, koff, sbase + i * 4096, K_AUX);
          __builtin_amdgcn_sched_barrier(0);
        }
#define TR_STEP(HALF, CTRL, BIT) _Pragma("unroll") for (int hh = 0; hh < (HALF); ++hh) { \
          const float lo_ = xx[hh], hi_ = xx[hh + (HALF)]; const float t1_ = lo_ + dppf<CTRL>(lo_), t2_ = hi_ + dppf<CTRL>(hi_); \
          xx[hh] = (c & (BIT)) ? t2_ : t1_; }
        TR_STEP(8, XOR8, 8) TR_STEP(4, XOR7, 4) TR_STEP(2, XOR2, 2) TR_STEP(1, XOR1, 1)
#undef TR_STEP
        nsc[gi] = rbf(rbf(xx[0]) * 0.08838834764831845f);
        asm volatile("" : "+v"(nsc[gi]));
      }
#pragma unroll
      for (int i = 0; i < 16 - GPI; ++i) sc[i] = sc[i + GPI];
#pragma unroll
      for (int gi = 0; gi < GPI; ++gi) sc[16 - GPI + gi] = nsc[gi];
    };
#pragma unroll 1
    for (int it = 0; it < NIT - 1; ++it) kbody(std::false_type{}, (it + 1) * RING * 4096);
    kbody(std::true_type{}, 0);
    ST(3);
    float* sm = &lds[L_SM];
    {
      float m = sc[0];
#pragma unroll
      for (int i = 1; i < 16; ++i) m = fmaxf(m, sc[i]);
      m = wave_max(m);
      float l = 0.f;
#pragma unroll
      for (int i = 0; i < 16; ++i) l += __builtin_amdgcn_exp2f((sc[i] - m) * L2E);
      l = wave_sum(l);
      if (lane == 0) { sm[2 * w] = m; sm[2 * w + 1] = l; }
    }
    lds_barrier();
    unsigned pd[16];
    {
      const f32x4 t0 = *(const f32x4*)&sm[0], t1 = *(const f32x4*)&sm[4];
      const float m = fmaxf(fmaxf(t0.x, t0.z), fmaxf(t1.x, t1.z));
      float l = t0.y * __builtin_amdgcn_exp2f((t0.x - m) * L2E);
      l = fmaf(t0.w, __builtin_amdgcn_exp2f((t0.z - m) * L2E), l);
      l = fmaf(t1.y, __builtin_amdgcn_exp2f((t1.x - m) * L2E), l);
      l = fmaf(t1.w, __builtin_amdgcn_exp2f((t1.z - m) * L2E), l);
      const float inv = 1.0f / l;
#pragma unroll
      for (int i = 0; i < 16; ++i) { const unsigned p = bfbits(__builtin_amdgcn_exp2f((sc[i] - m) * L2E) * inv); pd[i] = p | (p << 16); }
    }
    ST(4);
    f32x4 oacc = {0.f, 0.f, 0.f, 0.f};
#pragma unroll 1
    for (int it = 0; it < NIT - 1; ++it) {
      const int sbase = (it + 1) * RING * 4096;
      static_for<0, RING>([&](auto I) {
        constexpr int i = I;
        const v4u a = onehot<(i & 15)>(pd[i >> 4], mask);
        oacc = mfma16(a, ring[i], oacc);
        asm volatile("" : "+v"(oacc));
        ring[i] = __builtin_amdgcn_raw_buffer_load_b128(rV, koff, sbase + i * 4096, vaux);
        __builtin_amdgcn_sched_barrier(0);
      });
#pragma unroll
      for (int i = 0; i < 16 - GPI; ++i) pd[i] = pd[i + GPI];
    }
    static_for<0, RING>([&](auto I) {
      constexpr int i = I;
      const v4u a = onehot<(i & 15)>(pd[i >> 4], mask);
      oacc = mfma16(a, ring[i], oacc);
      asm volatile("" : "+v"(oacc));
      __builtin_amdgcn_sched_barrier(0);
    });
    ST(5);
    if (lane < 32) *(f32x4*)&lds[L_OP + w * 128 + 8 * c + 4 * g] = oacc;
    lds_barrier();
    ST(6);
    };
    swave(std::integral_constant<int, V_AUX>{});
    return;
  }

  __builtin_amdgcn_s_setprio(HPRIO);
  const int hw = w - 4, ht = tid - 256;
  const __amdgpu_buffer_rsrc_t rQ = __builtin_amdgcn_make_buffer_rsrc((void*)(wq + h * DH), 0, D_ * H_ * DH * 2 - h * DH * 2, 0x00020000);
  const __amdgpu_buffer_rsrc_t rO = __builtin_amdgcn_make_buffer_rsrc((void*)(wo + (size_t)h * DH * D_), 0, DH * D_ * 2, 0x00020000);
  const unsigned* x32 = (const unsigned*)(x + b * D_);
  unsigned xr[5];
#pragma unroll
  for (int i = 0; i < 5; ++i) xr[i] = x32[lane + 64 * i];
  const u16* xb = x + b * D_;
  const unsigned xo0 = xb[160 * hw + 4 * c + g];
  const unsigned xo1 = xb[160 * hw + 64 + 4 * c + g];
  const unsigned xo2 = xb[160 * hw + 128 + 4 * (c & 7) + g];
  const unsigned xg = xb[20 * h + (lane < 20 ? lane : 0)];
  const int gf = ht < 216 ? ht : 0;
  const v4u gw1 = ((const v4u*)w1)[gf], gb1 = ((const v4u*)b1)[gf], gw2 = ((const v4u*)w2)[gf];
  const unsigned b2v = b2[0];
  gu32* epw = (gu32*)(ws + WS_EPOCH) + b * 64;
  const unsigned tag = __hip_atomic_load(epw, __ATOMIC_RELAXED, __HIP_MEMORY_SCOPE_AGENT) + 0x9E3779B9u;
  const unsigned xres = x32[10 * h + (c < 10 ? c : 0)];
  __builtin_amdgcn_sched_barrier(0);
  v4u wr[40];
  const int qoff = (160 * hw + g) * (H_ * DH * 2) + c * 16;
#pragma unroll
  for (int t = 0; t < 40; ++t) { wr[t] = __builtin_amdgcn_raw_buffer_load_b128(rQ, qoff, t * 4 * (H_ * DH * 2), W_AUX); __builtin_amdgcn_sched_barrier(0); }
  ST(1);
  float xv[10];
#pragma unroll
  for (int i = 0; i < 5; ++i) { xv[2 * i] = bflo(xr[i]); xv[2 * i + 1] = bfhi(xr[i]); }
  float mb, sd;
  ln_stats(xv, mb, sd);
  unsigned xn0 = bfbits(ln_apply(bflo(xo0), mb, sd)), xn1 = bfbits(ln_apply(bflo(xo1), mb, sd)), xn2 = bfbits(ln_apply(bflo(xo2), mb, sd));
  xn0 |= xn0 << 16; xn1 |= xn1 << 16; xn2 |= xn2 << 16;
  {
    f32x4 acc = {0.f, 0.f, 0.f, 0.f};
    static_for<0, 40>([&](auto T) {
      constexpr int t = T;
      const v4u a = onehot<(t & 15)>(t < 16 ? xn0 : t < 32 ? xn1 : xn2, mask);
      acc = mfma16(a, wr[t], acc);
    });
    if (lane < 32) *(f32x4*)&lds[hw * 128 + 8 * c + 4 * g] = acc;
  }
  ST(2);
  lds_barrier();
  __builtin_amdgcn_s_setprio(0);
  {
    float w1v[8], b1v[8], w2v[8];
    {
      const bool ok = ht < 216;
#pragma unroll
      for (int i = 0; i < 4; ++i) {
        w1v[2 * i] = bflo(gw1[i]); w1v[2 * i + 1] = bfhi(gw1[i]);
        b1v[2 * i] = bflo(gb1[i]); b1v[2 * i + 1] = bfhi(gb1[i]);
        w2v[2 * i] = ok ? bflo(gw2[i]) : 0.f; w2v[2 * i + 1] = ok ? bfhi(gw2[i]) : 0.f;
      }
    }
#pragma unroll
    for (int i = 0; i < 10; ++i) xv[i] = ln_apply(xv[i], mb, sd);
    float mb2, sd2;
    ln_stats(xv, mb2, sd2);
    const float hcol = ln_apply(ln_apply(bflo(xg), mb, sd), mb2, sd2);
    float ycol = 0.f;
#pragma unroll 1
    for (int j = 0; j < 20; ++j) {
      const float hv = __builtin_bit_cast(float, __builtin_amdgcn_readlane(__builtin_bit_cast(int, hcol), j));
      float y = 0.f;
#pragma unroll
      for (int i = 0; i < 8; ++i) y = fmaf(gelu_bf(rbf(rbf(hv * w1v[i]) + b1v[i])), w2v[i], y);
      y = wave_sum(y);
      ycol = (lane == j) ? y : ycol;
    }
    if (lane < 20) lds[L_GATE + hw * 20 + lane] = ycol;
  }
  ST(3);
  __builtin_amdgcn_s_setprio(HPRIO);
  const int ooff = (32 * hw + g) * (D_ * 2) + c * 16;
  static_for<0, 40>([&](auto I) {
    constexpr int i = I;
    wr[i] = __builtin_amdgcn_raw_buffer_load_b128(rO, ooff, (i / 5) * 4 * (D_ * 2) + (i % 5) * 256, W_AUX);
    if constexpr (i >= WO_INFL - 1) asm volatile("s_waitcnt vmcnt(%0)" :: "i"(WO_INFL - 1) : "memory");
    __builtin_amdgcn_sched_barrier(0);
  });
  ST(4);
  lds_barrier();
  float gt0, gt1;
  {
    const int cc = c < 10 ? 2 * c : 0;
    float y0 = (lds[L_GATE + cc] + lds[L_GATE + 20 + cc]) + (lds[L_GATE + 40 + cc] + lds[L_GATE + 60 + cc]);
    float y1 = (lds[L_GATE + cc + 1] + lds[L_GATE + 21 + cc]) + (lds[L_GATE + 41 + cc] + lds[L_GATE + 61 + cc]);
    y0 = rbf(rbf(y0) + bflo(b2v)); y1 = rbf(rbf(y1) + bflo(b2v));
    gt0 = rbf(y0 / (1.0f + __expf(-y0))); gt1 = rbf(y1 / (1.0f + __expf(-y1)));
  }
  int poff[3];
#pragma unroll
  for (int r = 0; r < 3; ++r) { const int col = ht + 256 * r, cons = col / 20; poff[r] = (cons * 32 + (col - cons * 20)) * 8; }
  int goff = g * 8192 + h * 256 + (c < 10 ? c : 0) * 16;
  asm volatile("" : "+v"(gt0), "+v"(gt1), "+v"(poff[0]), "+v"(poff[1]), "+v"(poff[2]), "+v"(goff));
  lds_barrier();
  ST(5);
  unsigned od;
  {
    const int k = 32 * hw + 4 * (c & 7) + g;
    const float s = (lds[L_OP + k] + lds[L_OP + 128 + k]) + (lds[L_OP + 256 + k] + lds[L_OP + 384 + k]);
    od = bfbits(s); od |= od << 16;
  }
  f32x4 sacc[5];
#pragma unroll
  for (int u = 0; u < 5; ++u) sacc[u] = f32x4{0.f, 0.f, 0.f, 0.f};
  static_for<0, 8>([&](auto T) {
    constexpr int t = T;
    const v4u a = onehot<t>(od, mask);
#pragma unroll
    for (int u = 0; u < 5; ++u) sacc[u] = mfma16(a, wr[t * 5 + u], sacc[u]);
  });
  if (lane < 32) {
#pragma unroll
    for (int u = 0; u < 5; ++u) *(f32x4*)&lds[hw * 640 + u * 128 + 8 * c + 4 * g] = sacc[u];
  }
  lds_barrier();
  {
    const __amdgpu_buffer_rsrc_t rP = __builtin_amdgcn_make_buffer_rsrc((void*)(ws + WS_GRAN + (size_t)bh * 8192), 0, 8192, 0x00020000);
    float sv[3];
#pragma unroll
    for (int r = 0; r < 3; ++r) { const int col = ht + 256 * r; sv[r] = (lds[col] + lds[640 + col]) + (lds[1280 + col] + lds[1920 + col]); }
#pragma unroll
    for (int r = 0; r < 3; ++r) { v2u gv; gv.x = __float_as_uint(sv[r]); gv.y = tag; __builtin_amdgcn_raw_buffer_store_b64(gv, rP, poff[r], 0, 16); }
  }
  ST(6);
  if (hw != 0) return;
  {
    const __amdgpu_buffer_rsrc_t rG = __builtin_amdgcn_make_buffer_rsrc((void*)(ws + WS_GRAN + (size_t)b * 32 * 8192), 0, 32 * 8192, 0x00020000);
    v4u gr[8];
    const unsigned long long tlim = __builtin_amdgcn_s_memrealtime() + 2000000ull;
    for (;;) {
      asm volatile("" : "+v"(goff) :: "memory");
#pragma unroll
      for (int i = 0; i < 8; ++i) gr[i] = __builtin_amdgcn_raw_buffer_load_b128(rG, goff, i * 4 * 8192, 16);
      unsigned bad = 0;
#pragma unroll
      for (int i = 0; i < 8; ++i) bad |= (gr[i].y ^ tag) | (gr[i].w ^ tag);
      if (__all(bad == 0u) || __builtin_amdgcn_s_memrealtime() > tlim) break;
      __builtin_amdgcn_s_sleep(1);
      asm volatile("" ::: "memory");
    }
    float s0 = __uint_as_float(gr[0].x), s1 = __uint_as_float(gr[0].z);
#pragma unroll
    for (int i = 1; i < 8; ++i) { s0 += __uint_as_float(gr[i].x); s1 += __uint_as_float(gr[i].z); }
    s0 = rows_sum(s0); s1 = rows_sum(s1);
    if (lane < 10) {
      const unsigned o0 = bfbits(bflo(xres) + rbf(rbf(s0) * gt0)), o1 = bfbits(bfhi(xres) + rbf(rbf(s1) * gt1));
      ((unsigned*)out)[b * (D_ / 2) + 10 * h + lane] = o0 | (o1 << 16);
    }
    if (h == 0 && lane == 0) __hip_atomic_store(epw, tag, __ATOMIC_RELAXED, __HIP_MEMORY_SCOPE_AGENT);
  }
  while (__builtin_amdgcn_s_memrealtime() - tstart < 15000ull) __builtin_amdgcn_s_sleep(1);
  ST(7);
}

extern "C" void kernel_launch(void* const* d_in, const int* in_sizes, int n_in, void* d_out, int out_size, void* d_ws,
                              size_t ws_size, hipStream_t stream) {
  const u16* x = (const u16*)d_in[0];
  const u16* kc = (const u16*)d_in[1];
  const u16* vc = (const u16*)d_in[2];
  const u16* wq = (const u16*)d_in[3];
  const u16* wo = (const u16*)d_in[4];
  const u16* w1 = (const u16*)d_in[6];
  const u16* b1 = (const u16*)d_in[7];
  const u16* w2 = (const u16*)d_in[8];
  const u16* b2 = (const u16*)d_in[9];
  attn_kernel<<<256, 512, 0, stream>>>(x, kc, vc, wq, wo, w1, b1, w2, b2, (unsigned char*)d_ws, (u16*)d_out);
}
```

```cpp
#include <hip/hip_runtime.h>
#include <hip/hip_bf16.h>
#include <stdint.h>
#include <type_traits>

typedef unsigned v2u __attribute__((ext_vector_type(2)));
typedef unsigned v4u __attribute__((ext_vector_type(4)));
typedef unsigned v8u __attribute__((ext_vector_type(8)));
typedef float f32x2 __attribute__((ext_vector_type(2)));
typedef float f32x4 __attribute__((ext_vector_type(4)));
typedef __bf16 bf16x2 __attribute__((ext_vector_type(2)));
typedef __bf16 bf16x8 __attribute__((ext_vector_type(8)));
typedef __bf16 bf16x16 __attribute__((ext_vector_type(16)));
typedef unsigned short u16;
typedef __attribute__((address_space(1))) unsigned gu32;
typedef __attribute__((address_space(3))) unsigned lu32;
typedef __attribute__((address_space(3))) f32x2 lf32x2;
typedef __attribute__((address_space(3))) f32x4 lf32x4;

constexpr int D_ = 640, H_ = 32, SK = 4096, DH = 128;
constexpr size_t WS_SG = 0, WS_EPB = 256 * 5120, WS_AN = WS_EPB + 4096;
constexpr unsigned TAG_STEP = 0x9E3779B9u;

constexpr int TLO = 5;
constexpr int TAU256 = 3430;
constexpr int TOFF = 190;
constexpr int DELTA = 35;
constexpr int FAR = 70;
constexpr int NL = 2, NX = 2;
constexpr int WQ_INFL = 3, WO_INFL = 2;
constexpr int NT = 2, SC1 = 16, OOB = (int)0x80000000;

__device__ __forceinline__ float rbf(float x) { return (float)(__bf16)x; }
__device__ __forceinline__ unsigned bfbits(float x) { return (unsigned)__builtin_bit_cast(u16, (__bf16)x); }
__device__ __forceinline__ unsigned bfpack(f32x2 v) { return __builtin_bit_cast(unsigned, __builtin_convertvector(v, bf16x2)); }
__device__ __forceinline__ float bflo(unsigned u) { return __builtin_bit_cast(float, u << 16); }
__device__ __forceinline__ float bfhi(unsigned u) { return __builtin_bit_cast(float, u & 0xffff0000u); }

template <int CTRL> __device__ __forceinline__ float dppf(float x) {
  return __builtin_bit_cast(float, __builtin_amdgcn_mov_dpp(__builtin_bit_cast(int, x), CTRL, 0xf, 0xf, true));
}
template <int CTRL> __device__ __forceinline__ unsigned dppu(unsigned x) {
  return (unsigned)__builtin_amdgcn_mov_dpp((int)x, CTRL, 0xf, 0xf, true);
}
constexpr int XOR1 = 0xB1, XOR2 = 0x4E, XOR7 = 0x141, XOR8 = 0x128, NEWBCAST = 0x150;

__device__ __forceinline__ float swap16_sum(float x) {
  auto s = __builtin_amdgcn_permlane16_swap(__float_as_uint(x), __float_as_uint(x), false, false);
  return __uint_as_float(s[0]) + __uint_as_float(s[1]);
}
__device__ __forceinline__ float swap32_sum(float x) {
  auto t = __builtin_amdgcn_permlane32_swap(__float_as_uint(x), __float_as_uint(x), false, false);
  return __uint_as_float(t[0]) + __uint_as_float(t[1]);
}
__device__ __forceinline__ float wave_sum(float x) {
  x += dppf<XOR1>(x); x += dppf<XOR2>(x); x += dppf<XOR7>(x); x += dppf<XOR8>(x);
  return swap32_sum(swap16_sum(x));
}
__device__ __forceinline__ float wave_max(float x) {
  x = fmaxf(x, dppf<XOR1>(x)); x = fmaxf(x, dppf<XOR2>(x)); x = fmaxf(x, dppf<XOR7>(x)); x = fmaxf(x, dppf<XOR8>(x));
  auto s = __builtin_amdgcn_permlane16_swap(__float_as_uint(x), __float_as_uint(x), false, false);
  x = fmaxf(__uint_as_float(s[0]), __uint_as_float(s[1]));
  auto t = __builtin_amdgcn_permlane32_swap(__float_as_uint(x), __float_as_uint(x), false, false);
  return fmaxf(__uint_as_float(t[0]), __uint_as_float(t[1]));
}
__device__ __forceinline__ float rdlane(float x, int l) { return __builtin_bit_cast(float, __builtin_amdgcn_readlane(__builtin_bit_cast(int, x), l)); }
__device__ __forceinline__ float dot2(unsigned a, unsigned b, float acc) {
  return __builtin_amdgcn_fdot2_f32_bf16(__builtin_bit_cast(bf16x2, a), __builtin_bit_cast(bf16x2, b), acc, false);
}
__device__ __forceinline__ f32x4 mfma16(v4u a, v4u b, f32x4 c) {
  return __builtin_amdgcn_mfma_f32_16x16x32_bf16(__builtin_bit_cast(bf16x8, a), __builtin_bit_cast(bf16x8, b), c, 0, 0, 0);
}
__device__ __forceinline__ f32x4 smfma16(v4u a, v4u b0, v4u b1, f32x4 c, int idx) {
  const v8u b = __builtin_shufflevector(b0, b1, 0, 1, 2, 3, 4, 5, 6, 7);
  return __builtin_amdgcn_smfmac_f32_16x16x64_bf16(__builtin_bit_cast(bf16x8, a), __builtin_bit_cast(bf16x16, b), c, idx, 0, 0);
}
template <int J> __device__ __forceinline__ v4u onehot(unsigned val2, const v4u& mask) {
  const unsigned pb = dppu<NEWBCAST + J>(val2);
  v4u a;
  a.x = pb & mask.x; a.y = pb & mask.y; a.z = pb & mask.z; a.w = pb & mask.w;
  return a;
}
__device__ __forceinline__ v4u onehot_mask(int c) {
  const unsigned mm = (c & 1) ? 0xffff0000u : 0x0000ffffu;
  v4u mask;
  mask.x = (c >> 1) == 0 ? mm : 0u; mask.y = (c >> 1) == 1 ? mm : 0u; mask.z = (c >> 1) == 2 ? mm : 0u; mask.w = (c >> 1) == 3 ? mm : 0u;
  return mask;
}
__device__ __forceinline__ void lds_barrier() {
  asm volatile("s_waitcnt lgkmcnt(0)" ::: "memory");
  __builtin_amdgcn_s_barrier();
  asm volatile("" ::: "memory");
}
__device__ __forceinline__ unsigned lds_get(float* p) { return __hip_atomic_load((lu32*)p, __ATOMIC_RELAXED, __HIP_MEMORY_SCOPE_WORKGROUP); }
__device__ __forceinline__ void lds_put(float* p, unsigned v) { __hip_atomic_store((lu32*)p, v, __ATOMIC_RELAXED, __HIP_MEMORY_SCOPE_WORKGROUP); }
template <int N> __device__ __forceinline__ void wait_vm() { asm volatile("s_waitcnt vmcnt(%0)" ::"i"(N) : "memory"); }
__device__ __forceinline__ void wait_var(int state) {
  asm volatile("s_cmp_eq_u32 %0, 3\n\ts_cbranch_scc1 .Lwc%=\n\ts_waitcnt vmcnt(%3)\n\t"
               "s_cmp_eq_u32 %0, 2\n\ts_cbranch_scc1 .Lwc%=\n\ts_waitcnt vmcnt(%2)\n\t"
               "s_cmp_eq_u32 %0, 1\n\ts_cbranch_scc1 .Lwc%=\n\ts_waitcnt vmcnt(%1)\n.Lwc%=:"
               ::"s"(state), "i"(TLO - 1), "i"(TLO), "i"(TLO + 1) : "memory", "scc");
}
__device__ __forceinline__ void ln_stats(const float (&xv)[10], float& mb, float& sd) {
  float s = 0.f;
#pragma unroll
  for (int i = 0; i < 10; ++i) s += xv[i];
  const float mean = wave_sum(s) * (1.0f / D_);
  float q = 0.f;
#pragma unroll
  for (int i = 0; i < 10; ++i) { const float d = xv[i] - mean; q = fmaf(d, d, q); }
  const float var = rbf(wave_sum(q) * (1.0f / D_));
  mb = rbf(mean);
  sd = rbf(sqrtf(rbf(var + 1e-5f)));
}
__device__ __forceinline__ float ln_apply(float x, float mb, float sd) { return rbf(rbf(x - mb) / sd); }
__device__ __forceinline__ float gelu_bf(float u) {
  constexpr float K1 = -2.0f * 0.7978845608028654f * 1.4426950408889634f;
  constexpr float K2 = K1 * 0.044715f;
  const float e = __builtin_amdgcn_exp2f(u * fmaf(K2, u * u, K1));
  return rbf(u * __builtin_amdgcn_rcpf(1.0f + e));
}
template <int I, int N, typename Fn> __device__ __forceinline__ void static_for(Fn&& f) {
  if constexpr (I < N) { f(std::integral_constant<int, I>{}); static_for<I + 1, N>(f); }
}
constexpr unsigned long long PERM = 0x3502784b1a96ull;
constexpr int ppos(int i) { return (int)((PERM >> (4 * (11 - i))) & 15); }
constexpr int scat4(int v, int i) {
  return (((v & 1) << ppos(i)) | (((v >> 1) & 1) << ppos(i + 1)) | (((v >> 2) & 1) << ppos(i + 2)) | (((v >> 3) & 1) << ppos(i + 3))) * 256;
}
constexpr int soff(int n) { return scat4(n & 15, 4) + scat4(n >> 4, 8); }
constexpr int brev4(int j) { return ((j & 1) << 3) | ((j & 2) << 1) | ((j & 4) >> 1) | ((j & 8) >> 3); }

constexpr int L_S = 0;
constexpr int L_SM = 2560;
constexpr int L_TAG = 2568;
constexpr int L_CNT = 2569;
constexpr int L_AN = 2570;
constexpr int L_POS = 2571;
constexpr int L_TS = 2572;
constexpr int L_GATE = 2576;
constexpr int L_OP = 2704;
constexpr int L_SC = 3216;
constexpr int L_K = L_SC + 4096;
constexpr int L_END = L_K + 4 * 16 * NL * 256;

__global__ void __launch_bounds__(512, 2)
fused_kernel(const u16* __restrict__ x, const u16* __restrict__ kc, const u16* __restrict__ vc,
             const u16* __restrict__ wq, const u16* __restrict__ wo, const u16* __restrict__ w1, const u16* __restrict__ b1,
             const u16* __restrict__ w2, const u16* __restrict__ b2, unsigned char* ws, u16* out) {
  __shared__ __attribute__((aligned(16))) float lds[L_END];
  asm volatile("" ::"s"(x), "s"(kc), "s"(vc), "s"(wq));
  const int bh = blockIdx.x, b = bh >> 5, h = bh & 31;
  const int tid = threadIdx.x, lane = tid & 63, w = __builtin_amdgcn_readfirstlane(tid >> 6);
  const int g = lane >> 4, c = lane & 15;
  constexpr float L2E = 1.4426950408889634f;

  auto publish = [&](__amdgpu_buffer_rsrc_t rP, int t, unsigned tag) __attribute__((always_inline)) {
    const f32x2 sv = (*(const f32x2*)&lds[L_S + 2 * t] + *(const f32x2*)&lds[L_S + 640 + 2 * t]) +
                     (*(const f32x2*)&lds[L_S + 1280 + 2 * t] + *(const f32x2*)&lds[L_S + 1920 + 2 * t]);
    v4u gv; gv.x = __float_as_uint(sv.x); gv.y = tag; gv.z = __float_as_uint(sv.y); gv.w = tag;
    __builtin_amdgcn_raw_buffer_store_b128(gv, rP, t * 16, 0, SC1);
  };

  if (w >= 4) {
    __builtin_amdgcn_s_setprio(3);
    const int hw = w - 4, ht = tid - 256;
    gu32* epb = (gu32*)(ws + WS_EPB) + b * 64;
    const unsigned* x32 = (const unsigned*)(x + b * D_);
    const u16* xb = x + b * D_;
    unsigned xr[5];
#pragma unroll
    for (int i = 0; i < 5; ++i) xr[i] = x32[lane + 64 * i];
    const unsigned xo0 = xb[160 * hw + 4 * c + g], xo1 = xb[160 * hw + 64 + 4 * c + g], xo2 = xb[160 * hw + 128 + 4 * (c & 7) + g];
    __builtin_amdgcn_sched_barrier(0);
    v4u pre[5];
    {
      const __amdgpu_buffer_rsrc_t rW = __builtin_amdgcn_make_buffer_rsrc((void*)wq, 0, D_ * H_ * DH * 2, 0x00020000);
      const int pi = (bh >> 3) * 4 + hw;
      const int po = ((pi >> 1) * 2 + (g >> 1)) * 8192 + ((h & 7) + 16 * (pi & 1) + 8 * (g & 1)) * 256 + c * 16;
#pragma unroll
      for (int j = 0; j < 5; ++j) { pre[j] = __builtin_amdgcn_raw_buffer_load_b128(rW, po, j * 128 * 8192, 0); __builtin_amdgcn_sched_barrier(0); }
    }
    const __amdgpu_buffer_rsrc_t rQ = __builtin_amdgcn_make_buffer_rsrc((void*)(wq + h * DH), 0, D_ * H_ * DH * 2 - h * DH * 2, 0x00020000);
    const int qoff = (160 * hw + g) * (H_ * DH * 2) + c * 16;
    v4u wr[40];
    static_for<0, 2 * WQ_INFL>([&](auto T) {
      constexpr int t = T;
      if constexpr (t >= WQ_INFL) wait_vm<WQ_INFL - 1>();
      wr[t] = __builtin_amdgcn_raw_buffer_load_b128(rQ, qoff, t * 4 * (H_ * DH * 2), 0);
      __builtin_amdgcn_sched_barrier(0);
    });
    const v4u mask = onehot_mask(c);
    float xv[10];
#pragma unroll
    for (int i = 0; i < 5; ++i) { xv[2 * i] = bflo(xr[i]); xv[2 * i + 1] = bfhi(xr[i]); }
    float mb, sd;
    ln_stats(xv, mb, sd);
    asm volatile("" : "+v"(mb), "+v"(sd) : "v"(pre[0]), "v"(pre[1]), "v"(pre[2]), "v"(pre[3]), "v"(pre[4]));
    static_for<2 * WQ_INFL, 3 * WQ_INFL>([&](auto T) {
      constexpr int t = T;
      wait_vm<WQ_INFL - 1>();
      wr[t] = __builtin_amdgcn_raw_buffer_load_b128(rQ, qoff, t * 4 * (H_ * DH * 2), 0);
      __builtin_amdgcn_sched_barrier(0);
    });
    unsigned xn0 = bfbits(ln_apply(bflo(xo0), mb, sd)), xn1 = bfbits(ln_apply(bflo(xo1), mb, sd)), xn2 = bfbits(ln_apply(bflo(xo2), mb, sd));
    xn0 |= xn0 << 16; xn1 |= xn1 << 16; xn2 |= xn2 << 16;
    asm volatile("" : "+v"(xn0), "+v"(xn1), "+v"(xn2));
    {
      f32x4 acc = {0.f, 0.f, 0.f, 0.f};
      static_for<0, 40>([&](auto T) {
        constexpr int t = T, n = t + 3 * WQ_INFL;
        if constexpr (n < 40) {
          wait_vm<WQ_INFL - 1 + (n % 9 == 0 ? 1 : 0)>();
          wr[n] = __builtin_amdgcn_raw_buffer_load_b128(rQ, qoff, n * 4 * (H_ * DH * 2), 0);
        }
        const v4u a = onehot<(t & 15)>(t < 16 ? xn0 : t < 32 ? xn1 : xn2, mask);
        acc = mfma16(a, wr[t], acc);
        asm volatile("" : "+v"(acc));
        __builtin_amdgcn_sched_barrier(0);
      });
      if (lane < 32) *(f32x4*)&lds[L_S + hw * 128 + 8 * c + 4 * g] = acc;
    }
    lds_barrier();
    __builtin_amdgcn_sched_barrier(0);
    const int ncol = h < 8 ? 32 : 16;
    const unsigned xg = xb[16 * h + (lane & 15) + ((lane & 16) && h < 8 ? 512 : 0)];
    const int gf = ht % 192;
    const v4u gw1 = ((const v4u*)w1)[gf], gb1 = ((const v4u*)b1)[gf], gw2 = ((const v4u*)w2)[gf];
    const unsigned ew1 = w1[1536 + gf], eb1 = b1[1536 + gf], ew2 = w2[1536 + gf];
    const unsigned b2v = b2[0];
    const unsigned tag = __hip_atomic_load(epb, __ATOMIC_RELAXED, __HIP_MEMORY_SCOPE_AGENT) + TAG_STEP;
    const int cgrp = (hw == 1 && h < 8) ? 32 + h : h;
    const unsigned xres = x32[8 * cgrp + (lane & 7)];
    __builtin_amdgcn_sched_barrier(0);
    const __amdgpu_buffer_rsrc_t rO = __builtin_amdgcn_make_buffer_rsrc((void*)(wo + (size_t)h * DH * D_), 0, DH * D_ * 2, 0x00020000);
    const int ooff = (32 * hw + g) * (D_ * 2) + c * 16;
    {
      const int pi = b * 4 + hw, pg = pi & 15;
      const int po = (pi >> 4) * 16384 + ((pg & 7) + ((pg >> 3) << 4) + ((g & 1) << 3) + ((g >> 1) << 5)) * 256 + c * 16;
#pragma unroll
      for (int j = 0; j < 5; ++j) { pre[j] = __builtin_amdgcn_raw_buffer_load_b128(rO, po, j * 32768, 0); __builtin_amdgcn_sched_barrier(0); }
      asm volatile("" ::"v"(pre[0]), "v"(pre[1]), "v"(pre[2]), "v"(pre[3]), "v"(pre[4]));
      __builtin_amdgcn_s_sleep(40);
      __builtin_amdgcn_sched_barrier(0);
    }
    static_for<0, 40>([&](auto I) {
      constexpr int i = I;
      if constexpr (i >= WO_INFL) wait_vm<WO_INFL - 1>();
      wr[i] = __builtin_amdgcn_raw_buffer_load_b128(rO, ooff, (i / 5) * 4 * (D_ * 2) + (i % 5) * 256, 0);
      __builtin_amdgcn_sched_barrier(0);
    });
    if (hw == 1) {
      const unsigned char* tp = lane == 0 ? (const unsigned char*)(vc + (size_t)bh * SK * DH)
                              : lane == 1 ? (const unsigned char*)(vc + (size_t)(bh + 1) * SK * DH) - 128
                              : lane == 2 ? (const unsigned char*)out
                              : lane == 3 ? ws + WS_SG + (size_t)b * 32 * 5120
                              : ws + WS_SG + (size_t)(b + 1) * 32 * 5120 - 16;
      const unsigned tv = __hip_atomic_load((gu32*)tp, __ATOMIC_RELAXED, __HIP_MEMORY_SCOPE_AGENT);
      asm volatile("" ::"v"(tv));
    }
    if (ht == 0) { ((unsigned*)lds)[L_TAG] = tag; ((unsigned*)lds)[L_CNT] = 0u; }
    auto pacer = [&](int round, unsigned pos) __attribute__((always_inline)) {
      const __amdgpu_buffer_rsrc_t rA = __builtin_amdgcn_make_buffer_rsrc((void*)(ws + WS_AN + round * 4096), 0, 4096, 0x00020000);
      unsigned at = 0;
      for (int it = 0; it < 4096; ++it) {
        at = __builtin_amdgcn_readfirstlane(lds_get(&lds[L_POS]));
        if (at >= pos) break;
        __builtin_amdgcn_s_sleep(8);
      }
      if (at > pos + 16u) return;
      __builtin_amdgcn_s_sleep(16);
      const unsigned tv = lds_get(&lds[L_TS + (lane & 3)]);
      const unsigned ref = __builtin_amdgcn_readfirstlane(tv);
      int d = (int)(tv - ref);
      d += (int)dppu<XOR1>((unsigned)d); d += (int)dppu<XOR2>((unsigned)d);
      const unsigned mine = ref + (unsigned)(d >> 2);
      v4u gv; gv.x = mine; gv.y = mine; gv.z = mine; gv.w = mine;
      __builtin_amdgcn_raw_buffer_store_b128(gv, rA, ((h >> 2) == b && lane < 8) ? h * 128 + lane * 16 : OOB, 0, SC1);
      __builtin_amdgcn_s_sleep(110);
      asm volatile("" ::: "memory");
      const int r = (int)(__builtin_amdgcn_raw_buffer_load_b32(rA, lane < 32 ? lane * 128 : OOB, 0, SC1) - mine);
      const bool ok = lane < 32 && r > -1000 && r < 1000;
      const int cnt = __builtin_popcountll(__builtin_amdgcn_ballot_w64(ok));
      const int mean = (int)(wave_sum(ok ? (float)r : 0.f) / (float)(cnt > 0 ? cnt : 1));
      if (cnt >= 16) lds_put(&lds[L_AN], mine + (unsigned)(mean + DELTA));
    };
    if (hw == 3) pacer(0, 208);
    lds_barrier();
    if (hw == 3) {
#pragma unroll 1
      for (int r = 1; r < 3; ++r) pacer(r, r == 1 ? 304 : 400);
    }
    __builtin_amdgcn_s_setprio(0);
    if (hw < 3) {
      float mb2, sd2;
#pragma unroll
      for (int i = 0; i < 10; ++i) xv[i] = ln_apply(xv[i], mb, sd);
      ln_stats(xv, mb2, sd2);
      const float hcol = ln_apply(ln_apply(bflo(xg), mb, sd), mb2, sd2);
      float w1v[9], b1v[9], w2v[9];
#pragma unroll
      for (int i = 0; i < 4; ++i) {
        w1v[2 * i] = bflo(gw1[i]); w1v[2 * i + 1] = bfhi(gw1[i]);
        b1v[2 * i] = bflo(gb1[i]); b1v[2 * i + 1] = bfhi(gb1[i]);
        w2v[2 * i] = bflo(gw2[i]); w2v[2 * i + 1] = bfhi(gw2[i]);
      }
      w1v[8] = bflo(ew1); b1v[8] = bflo(eb1); w2v[8] = bflo(ew2);
      float ycol = 0.f;
#pragma unroll 1
      for (int j = 0; j < ncol; ++j) {
        const float hv = rdlane(hcol, j);
        float y = 0.f;
#pragma unroll
        for (int i = 0; i < 9; ++i) y = fmaf(gelu_bf(rbf(rbf(hv * w1v[i]) + b1v[i])), w2v[i], y);
        y = wave_sum(y);
        ycol = (lane == j) ? y : ycol;
      }
      if (lane < 32) lds[L_GATE + hw * 32 + lane] = ycol;
      asm volatile("s_waitcnt lgkmcnt(0)" ::: "memory");
      if (lane == 0) __hip_atomic_fetch_add((lu32*)&lds[L_CNT], 1u, __ATOMIC_RELAXED, __HIP_MEMORY_SCOPE_WORKGROUP);
      while (lds_get(&lds[L_CNT]) < 3u) __builtin_amdgcn_s_sleep(2);
      asm volatile("" ::: "memory");
    }
    __builtin_amdgcn_s_setprio(3);
    float gt0, gt1;
    {
      const int cc = (cgrp >= 32 ? 16 : 0) + 2 * (lane & 7);
      float y0 = (lds[L_GATE + cc] + lds[L_GATE + 32 + cc]) + lds[L_GATE + 64 + cc];
      float y1 = (lds[L_GATE + cc + 1] + lds[L_GATE + 33 + cc]) + lds[L_GATE + 65 + cc];
      y0 = rbf(rbf(y0) + bflo(b2v)); y1 = rbf(rbf(y1) + bflo(b2v));
      gt0 = rbf(y0 / (1.0f + __expf(-y0))); gt1 = rbf(y1 / (1.0f + __expf(-y1)));
    }
    int goff = (lane >> 3) * 5120 + cgrp * 128 + (lane & 7) * 16;
    float xr0 = bflo(xres), xr1 = bfhi(xres);
    v4u smask;
    smask.x = (c >> 2) == 0 ? 0x0000ffffu : 0u; smask.y = (c >> 2) == 1 ? 0x0000ffffu : 0u;
    smask.z = smask.x << 16; smask.w = smask.y << 16;
    int sidx = (c & 3) * 0x5555;
    lf32x2* op = (lf32x2*)&lds[L_OP + 32 * hw + 8 * (c & 3) + 2 * g];
    lf32x4* sp = (lf32x4*)&lds[L_S + hw * 640 + 8 * c + 4 * (g & 1)];
    f32x4 sacc[5];
#pragma unroll
    for (int u = 0; u < 5; ++u) { sacc[u] = f32x4{0.f, 0.f, 0.f, 0.f}; asm volatile("" : "+v"(sacc[u])); }
    asm volatile("" : "+v"(gt0), "+v"(gt1), "+v"(goff), "+v"(smask), "+v"(xr0), "+v"(xr1), "+v"(sidx), "+v"(op), "+v"(sp));
    const __amdgpu_buffer_rsrc_t rG = __builtin_amdgcn_make_buffer_rsrc((void*)(ws + WS_SG + (size_t)b * 32 * 5120), 0, 32 * 5120, 0x00020000);
    const __amdgpu_buffer_rsrc_t rOut = __builtin_amdgcn_make_buffer_rsrc((void*)(out + b * D_ + 16 * cgrp), 0, 32, 0x00020000);
    const __amdgpu_buffer_rsrc_t rP = __builtin_amdgcn_make_buffer_rsrc((void*)(ws + WS_SG + (size_t)bh * 5120), 0, 5120, 0x00020000);
    asm volatile("" ::"s"(rG), "s"(rOut), "s"(rP));
    __builtin_amdgcn_s_waitcnt(0x0F70);
    lds_barrier();
    const unsigned od = bfpack((op[0] + op[64]) + (op[128] + op[192]));
    static_for<0, 4>([&](auto T) {
      constexpr int t = T;
      const v4u a = onehot<t>(od, smask);
#pragma unroll
      for (int u = 0; u < 5; ++u) sacc[u] = smfma16(a, wr[10 * t + u], wr[10 * t + 5 + u], sacc[u], sidx);
    });
    if (lane < 32) {
#pragma unroll
      for (int u = 0; u < 5; ++u) sp[u * 32] = sacc[u];
    }
    lds_barrier();
    publish(rP, ht, tag);
    if (hw == 0 || cgrp >= 32) {
      v4u gr[4];
      __builtin_amdgcn_s_sleep(22);
#pragma unroll 1
      for (int spin = 0; spin < (1 << 20); ++spin) {
        asm volatile("" : "+v"(goff)::"memory");
#pragma unroll
        for (int i = 0; i < 4; ++i) gr[i] = __builtin_amdgcn_raw_buffer_load_b128(rG, goff, i * 8 * 5120, SC1);
        unsigned bad = 0;
#pragma unroll
        for (int i = 0; i < 4; ++i) bad |= (gr[i].y ^ tag) | (gr[i].w ^ tag);
        asm volatile("" : "+v"(bad));
        if (__all(bad == 0u)) break;
        __builtin_amdgcn_s_sleep(1);
      }
      float s0 = (__uint_as_float(gr[0].x) + __uint_as_float(gr[1].x)) + (__uint_as_float(gr[2].x) + __uint_as_float(gr[3].x));
      float s1 = (__uint_as_float(gr[0].z) + __uint_as_float(gr[1].z)) + (__uint_as_float(gr[2].z) + __uint_as_float(gr[3].z));
      s0 += dppf<XOR8>(s0); s1 += dppf<XOR8>(s1);
      s0 = swap32_sum(swap16_sum(s0)); s1 = swap32_sum(swap16_sum(s1));
      const unsigned o0 = bfbits(xr0 + rbf(rbf(s0) * gt0)), o1 = bfbits(xr1 + rbf(rbf(s1) * gt1));
      __builtin_amdgcn_raw_buffer_store_b32(o0 | (o1 << 16), rOut, lane * 4, 0, SC1);
      if (cgrp == 0 && lane == 0) __hip_atomic_store(epb, tag, __ATOMIC_RELAXED, __HIP_MEMORY_SCOPE_AGENT);
    }
    return;
  }

  {
    __builtin_amdgcn_s_setprio(1);
    const __amdgpu_buffer_rsrc_t rK = __builtin_amdgcn_make_buffer_rsrc((void*)(kc + (size_t)bh * SK * DH), 0, SK * DH * 2, 0x00020000);
    const __amdgpu_buffer_rsrc_t rV = __builtin_amdgcn_make_buffer_rsrc((void*)(vc + (size_t)bh * SK * DH), 0, SK * DH * 2, 0x00020000);
    const int koff = scat4(tid >> 4, 0) + c * 16;
    v4u* lk = (v4u*)&lds[L_K] + w * (16 * NL * 64) + lane;
    unsigned t0;
    v4u stg[8], land[16 * NX], ring[16];
    static_for<0, 16 * (NL + NX + 1)>([&](auto N) {
      constexpr int n = N;
      if constexpr (n >= TLO && n < 16 * (NL + NX + 1) - 2) wait_vm<TLO - 1>();
      const v4u kv = __builtin_amdgcn_raw_buffer_load_b128(rK, koff, soff(n), NT);
      if constexpr (n < 16 * NL) stg[n & 7] = kv;
      else if constexpr (n < 16 * (NL + NX)) land[n - 16 * NL] = kv;
      else ring[n & 15] = kv;
      if constexpr (n >= 6 && n - 6 < 16 * NL) lk[(n - 6) * 64] = stg[(n - 6) & 7];
      if constexpr (n == TLO - 1) t0 = (unsigned)__builtin_amdgcn_s_memrealtime();
      __builtin_amdgcn_sched_barrier(0);
    });
    if (lane == 0) { ((unsigned*)lds)[L_TS + w] = t0; ((unsigned*)lds)[L_AN] = t0 + TOFF; ((unsigned*)lds)[L_POS] = 0u; }
    lds_barrier();
    v4u qp;
    {
      f32x4 s0 = *(const f32x4*)&lds[L_S + 8 * c], s1 = *(const f32x4*)&lds[L_S + 8 * c + 4];
#pragma unroll
      for (int ww = 1; ww < 4; ++ww) { s0 += *(const f32x4*)&lds[L_S + ww * 128 + 8 * c]; s1 += *(const f32x4*)&lds[L_S + ww * 128 + 8 * c + 4]; }
      qp.x = bfbits(s0.x) | (bfbits(s0.y) << 16); qp.y = bfbits(s0.z) | (bfbits(s0.w) << 16);
      qp.z = bfbits(s1.x) | (bfbits(s1.y) << 16); qp.w = bfbits(s1.z) | (bfbits(s1.w) << 16);
    }
    auto pace = [&](int n) __attribute__((always_inline)) -> int {
      const unsigned anchor = lds_get(&lds[L_AN]);
      const unsigned v = (unsigned)__builtin_amdgcn_s_memrealtime() - (unsigned)((n * TAU256) >> 8);
      lds_put(&lds[L_TS + w], v);
      lds_put(&lds[L_POS], (unsigned)n);
      const int late = (int)(v - (unsigned)__builtin_amdgcn_readfirstlane(anchor));
      return late < 0 ? 0 : late < FAR ? 1 : 2;
    };
    float* scw = &lds[L_SC + w * 1024 + lane];
    float mrun = -__builtin_inff(), lrun = 0.f;
    float ra[4], rb[4];
    auto kstep = [&](auto J, const v4u& kv, float (&r)[4], int grp) __attribute__((always_inline)) {
      constexpr int j = J;
      float d = dot2(kv.x, qp.x, 0.f);
      d = dot2(kv.y, qp.y, d); d = dot2(kv.z, qp.z, d); d = dot2(kv.w, qp.w, d);
      d += dppf<XOR8>(d);
      if constexpr (!(j & 1)) { r[0] = d; asm volatile("" : "+v"(r[0])); return; }
      d = (c & 8) ? d : r[0];
      d += dppf<XOR7>(d);
      if constexpr (!(j & 2)) { r[1] = d; asm volatile("" : "+v"(r[1])); return; }
      d = (c & 4) ? d : r[1];
      d += dppf<XOR2>(d);
      if constexpr (!(j & 4)) { r[2] = d; asm volatile("" : "+v"(r[2])); return; }
      d = (c & 2) ? d : r[2];
      d += dppf<XOR1>(d);
      if constexpr (!(j & 8)) { r[3] = d; asm volatile("" : "+v"(r[3])); return; }
      d = (c & 1) ? d : r[3];
      const float s = rbf(rbf(d) * 0.08838834764831845f);
      scw[grp * 64] = s;
      const float mn = fmaxf(fmaxf(mrun, s), -3.4e38f);
      lrun = fmaf(lrun, __builtin_amdgcn_exp2f((mrun - mn) * L2E), __builtin_amdgcn_exp2f((s - mn) * L2E));
      mrun = mn;
      asm volatile("" : "+v"(mrun), "+v"(lrun));
    };
    static_for<0, 16 * NX>([&](auto M) {
      constexpr int m = M, i = m & 15, k = NL + NX + (m >> 4);
      kstep(std::integral_constant<int, i>{}, ring[i], ra, k);
      wait_vm<TLO - 1>();
      ring[i] = __builtin_amdgcn_raw_buffer_load_b128(rK, koff, soff(16 * (k + 1) + i), NT);
      kstep(std::integral_constant<int, i>{}, land[m], rb, NL + (m >> 4));
      __builtin_amdgcn_sched_barrier(0);
    });
#pragma unroll 1
    for (int kk = 0; kk < NL; ++kk) {
      const int k = NL + 2 * NX + kk;
      int state = pace(16 * k + 16);
      if (kk == 0) state = 1;
      static_for<0, 16>([&](auto I) {
        constexpr int i = I;
        const v4u kv = lk[(kk * 16 + i) * 64];
        kstep(I, ring[i], ra, k);
        wait_var(state);
        ring[i] = __builtin_amdgcn_raw_buffer_load_b128(rK, koff, scat4(k + 1, 8) + soff(i), NT);
        kstep(I, kv, rb, kk);
        __builtin_amdgcn_sched_barrier(0);
      });
    }
#pragma unroll 1
    for (int k = 2 * (NL + NX); k < 16; ++k) {
      const bool last = k == 15;
      const __amdgpu_buffer_rsrc_t rN = last ? rV : rK;
      const int sbase = scat4((k + 1) & 15, 8);
      int state = last ? 3 : pace(16 * k + 16);
      if (k == 2 * (NL + NX)) state = 1;
      static_for<0, 16>([&](auto I) {
        constexpr int i = I;
        kstep(I, ring[i], ra, k);
        wait_var(state);
        ring[i] = __builtin_amdgcn_raw_buffer_load_b128(rN, koff, sbase + soff(i), NT);
        __builtin_amdgcn_sched_barrier(0);
      });
    }
    {
      const float m = wave_max(mrun);
      const float l = wave_sum(lrun * __builtin_amdgcn_exp2f((mrun - m) * L2E));
      if (lane == 0) { lds[L_SM + 2 * w] = m; lds[L_SM + 2 * w + 1] = l; }
    }
    float snext = scw[0];
    const v4u mask = onehot_mask(c);
    lds_barrier();
    float mx, inv;
    {
      const f32x4 s0 = *(const f32x4*)&lds[L_SM], s1 = *(const f32x4*)&lds[L_SM + 4];
      mx = fmaxf(fmaxf(s0.x, s0.z), fmaxf(s1.x, s1.z));
      float l = s0.y * __builtin_amdgcn_exp2f((s0.x - mx) * L2E);
      l = fmaf(s0.w, __builtin_amdgcn_exp2f((s0.z - mx) * L2E), l);
      l = fmaf(s1.y, __builtin_amdgcn_exp2f((s1.x - mx) * L2E), l);
      l = fmaf(s1.w, __builtin_amdgcn_exp2f((s1.z - mx) * L2E), l);
      inv = 1.0f / l;
    }
    f32x4 oacc = {0.f, 0.f, 0.f, 0.f};
#pragma unroll 1
    for (int k = 0; k < 16; ++k) {
      const bool last = k == 15;
      const int voff = last ? OOB : koff;
      const int sbase = scat4((k + 1) & 15, 8);
      const int state = last ? 3 : k == 0 ? 0 : pace(256 + 16 * k + 16);
      unsigned pd = bfbits(__builtin_amdgcn_exp2f((snext - mx) * L2E) * inv);
      pd |= pd << 16;
      snext = scw[((k + 1) & 15) * 64];
      static_for<0, 16>([&](auto I) {
        constexpr int i = I;
        const v4u a = onehot<brev4(i)>(pd, mask);
        oacc = mfma16(a, ring[i], oacc);
        asm volatile("" : "+v"(oacc));
        wait_var(state);
        ring[i] = __builtin_amdgcn_raw_buffer_load_b128(rV, voff, sbase + soff(i), NT);
        __builtin_amdgcn_sched_barrier(0);
      });
    }
    if (lane < 32) *(f32x4*)&lds[L_OP + w * 128 + 8 * c + 4 * g] = oacc;
    lds_barrier();
    if (w == 2) {
      const unsigned tag = ((const unsigned*)lds)[L_TAG];
      unsigned char* wsp = ws;
      asm volatile("" : "+s"(wsp));
      const __amdgpu_buffer_rsrc_t rP = __builtin_amdgcn_make_buffer_rsrc((void*)(wsp + WS_SG + (size_t)bh * 5120), 0, 5120, 0x00020000);
      asm volatile("" ::"s"(rP));
      lds_barrier();
      publish(rP, 256 + lane, tag);
    }
  }
}

extern "C" void kernel_launch(void* const* d_in, const int* in_sizes, int n_in, void* d_out, int out_size, void* d_ws,
                              size_t ws_size, hipStream_t stream) {
  fused_kernel<<<256, 512, 0, stream>>>((const u16*)d_in[0], (const u16*)d_in[1], (const u16*)d_in[2], (const u16*)d_in[3],
                                        (const u16*)d_in[4], (const u16*)d_in[6], (const u16*)d_in[7], (const u16*)d_in[8],
                                        (const u16*)d_in[9], (unsigned char*)d_ws, (u16*)d_out);
}
```
